```python
import torch

def _softmax(x, axis=-1):
    return torch.softmax(x, dim=axis)
def _gelu(x):
    return torch.nn.functional.gelu(x, approximate="tanh")
def _silu(x):
    return torch.nn.functional.silu(x)
def _sigmoid(x):
    return torch.sigmoid(x)
def _layernorm(x, axis=-1, eps=1e-5):
    m = x.mean(dim=axis, keepdim=True)
    v = x.var(dim=axis, keepdim=True, unbiased=False)
    return (x - m) / torch.sqrt(v + eps)
def _scatter_add(tgt, idx, src, axis=0):
    return tgt.clone().index_add_(axis, idx, src)
def _segment_sum(src, seg, n):
    out = torch.zeros((n,) + src.shape[1:], dtype=src.dtype)
    return out.index_add_(0, seg, src)
def _pad(x, pw):
    flat = tuple(v for pair in reversed(pw) for v in pair)
    return torch.nn.functional.pad(x, flat)

kernel_name = 'gated_mix__mha_block__compressed_kv_cla__prompt'

def setup_inputs():

    _w = (lambda a: (lambda t: t.to(torch.bfloat16) if t.is_floating_point() else t)(torch.as_tensor(a)))
    def _A_setup():
        B, S, D, H = 1, 2048, 448, 16
        return {
            "x": _w((lambda *s: torch.randn(*s, dtype=torch.bfloat16) * 0.05)(B, S, D)),
            "wqkv": _w((lambda *s: torch.randn(*s, dtype=torch.bfloat16) * 1.6)(D, 3 * D)),
            "wo": _w((lambda *s: torch.randn(*s, dtype=torch.bfloat16) * 0.05)(D, D)),
        }
    _A_in = _A_setup()

    def _B_setup():
        B, H, Sq, Sk, D, M = 1, 16, 2048, 2048, 256, 448
        Dc = 4 * D
        return {
            "x": _w((lambda *s: torch.randn(*s, dtype=torch.bfloat16) * 0.05)(B, Sq, M)),
            "c_kv": _w((lambda *s: torch.randn(*s, dtype=torch.bfloat16) * 1.6)(B, Sk, Dc)),
            "w_q": _w((lambda *s: torch.randn(*s, dtype=torch.bfloat16) * 6.4)(M, H, D) * (M ** -0.5)),
            "w_uk": _w((lambda *s: torch.randn(*s, dtype=torch.bfloat16) * 6.4)(Dc, H, D) * (Dc ** -0.5)),
            "w_uv": _w((lambda *s: torch.randn(*s, dtype=torch.bfloat16) * 1.6)(Dc, H, D) * (Dc ** -0.5)),
            "w_o": _w((lambda *s: torch.randn(*s, dtype=torch.bfloat16) * 1.6)(H, D, M) * ((H * D) ** -0.5)),
        }
    _B_in = _B_setup()

    return {
        "A_x": _A_in["x"],
        "A_wqkv": _A_in["wqkv"],
        "A_wo": _A_in["wo"],
        "B_x": _B_in["x"],
        "B_c_kv": _B_in["c_kv"],
        "B_w_q": _B_in["w_q"],
        "B_w_uk": _B_in["w_uk"],
        "B_w_uv": _B_in["w_uv"],
        "B_w_o": _B_in["w_o"],
        "gate_w": _w((lambda *s: torch.randn(*s, dtype=torch.bfloat16) * 0.05)(448) * 16.0),
    }

def reference(A_x, A_wqkv, A_wo, B_x, B_c_kv, B_w_q, B_w_uk, B_w_uv, B_w_o, gate_w):
    def _A_ref(x, wqkv, wo):
        B, S, D = x.shape
        H = 16
        Dh = D // H
        qkv = (lambda x, *d: x.permute(*d))((x @ wqkv).reshape(B, S, 3, H, Dh), 2, 0, 3, 1, 4)
        q, k, v = qkv[0], qkv[1], qkv[2]
        scores = torch.einsum("bhqd,bhkd->bhqk", q, k) * (Dh ** -0.5)
        p = _softmax(scores, axis=-1)
        o = torch.einsum("bhqk,bhkd->bhqd", p, v)
        return (lambda x, *d: x.permute(*d))(o, 0, 2, 1, 3).reshape(B, S, D) @ wo

    def _B_ref(x, c_kv, w_q, w_uk, w_uv, w_o):
        B, Sq, M = x.shape[0], x.shape[1], x.shape[2]
        Sk = c_kv.shape[1]
        H, D = w_q.shape[1], w_q.shape[2]
        q = torch.einsum("bqm,mhd->bhqd", x, w_q)
        k = torch.einsum("bkc,chd->bhkd", c_kv, w_uk)
        v = torch.einsum("bkc,chd->bhkd", c_kv, w_uv)
        scores = torch.einsum("bhqd,bhkd->bhqk", q, k) * (D ** -0.5)
        off = Sk - Sq
        qi = torch.arange(Sq)[:, None]
        kj = torch.arange(Sk)[None, :]
        cmask = kj <= (qi + off)
        scores = torch.where(cmask[None, None], scores, -1e9)
        p = _softmax(scores, axis=-1)
        ctx = torch.einsum("bhqk,bhkd->bhqd", p, v)
        return torch.einsum("bhqd,hdm->bqm", ctx, w_o)

    x = A_x
    g = _sigmoid((x * gate_w).sum(-1, keepdims=True))
    a = _A_ref(x, A_wqkv, A_wo)
    b = _B_ref(x, B_c_kv, B_w_q, B_w_uk, B_w_uv, B_w_o)
    return g * a + (1 - g) * b
```

```cpp
#include <hip/hip_runtime.h>
#include <hip/hip_bf16.h>
#include <stdint.h>
typedef unsigned short u16;
__device__ __forceinline__ float bf2f(u16 b){ return __uint_as_float(((unsigned)b)<<16); }
__device__ __forceinline__ u16 f2bf(float f){ unsigned u=__float_as_uint(f); u += 0x7FFFu + ((u>>16)&1u); return (u16)(u>>16); }

__global__ void gemm_naive(const u16* A, const u16* B, u16* C, int M, int N, int K){
  __shared__ float As[16][17], Bs[16][17];
  int tx=threadIdx.x, ty=threadIdx.y; int n=blockIdx.x*16+tx, m=blockIdx.y*16+ty;
  float acc=0;
  for(int k0=0;k0<K;k0+=16){
    As[ty][tx]=bf2f(A[(size_t)m*K+k0+tx]);
    Bs[ty][tx]=bf2f(B[(size_t)(k0+ty)*N+n]);
    __syncthreads();
    for(int k=0;k<16;k++) acc+=As[ty][k]*Bs[k][tx];
    __syncthreads();
  }
  C[(size_t)m*N+n]=f2bf(acc);
}
__global__ void attn_naive(const u16* Q,int qs,const u16* K,int ks,const u16* V,int vs,u16* O,int os,int D,int S,int causal,float scale){
  __shared__ float p[2048]; __shared__ float q[256]; __shared__ float red[256];
  int row=blockIdx.x,h=blockIdx.y,t=threadIdx.x;
  if(t<D) q[t]=bf2f(Q[(size_t)row*qs+h*D+t]);
  __syncthreads();
  int nk=causal?row+1:S;
  float mx=-1e30f;
  for(int j=t;j<nk;j+=256){ float s=0; const u16* kr=K+(size_t)j*ks+h*D; for(int d=0;d<D;d++) s+=q[d]*bf2f(kr[d]); s*=scale; p[j]=s; mx=fmaxf(mx,s);}
  red[t]=mx; __syncthreads(); for(int o=128;o;o>>=1){ if(t<o) red[t]=fmaxf(red[t],red[t+o]); __syncthreads(); }
  mx=red[0]; __syncthreads();
  float sum=0; for(int j=t;j<nk;j+=256){ float e=__expf(p[j]-mx); p[j]=e; sum+=e; }
  red[t]=sum; __syncthreads(); for(int o=128;o;o>>=1){ if(t<o) red[t]+=red[t+o]; __syncthreads(); }
  sum=red[0];
  if(t<D){ float a=0; for(int j=0;j<nk;j++) a+=p[j]*bf2f(V[(size_t)j*vs+h*D+t]); O[(size_t)row*os+h*D+t]=f2bf(a/sum);}
}
__global__ void mix_naive(const u16* x,const u16* gw,const u16* a,const u16* b,u16* out){
  __shared__ float red[512]; int row=blockIdx.x,t=threadIdx.x;
  red[t]= t<448 ? bf2f(x[row*448+t])*bf2f(gw[t]) : 0.f; __syncthreads();
  for(int o=256;o;o>>=1){ if(t<o) red[t]+=red[t+o]; __syncthreads(); }
  float g=1.f/(1.f+__expf(-red[0]));
  if(t<448) out[row*448+t]=f2bf(g*bf2f(a[row*448+t])+(1.f-g)*bf2f(b[row*448+t]));
}
extern "C" void kernel_launch(void* const* d_in, const int* in_sizes, int n_in, void* d_out, int out_size, void* d_ws, size_t ws_size, hipStream_t stream) {
  const u16* x=(const u16*)d_in[0]; const u16* wqkv=(const u16*)d_in[1]; const u16* wo=(const u16*)d_in[2];
  const u16* ckv=(const u16*)d_in[4]; const u16* wq=(const u16*)d_in[5]; const u16* wuk=(const u16*)d_in[6]; const u16* wuv=(const u16*)d_in[7]; const u16* wob=(const u16*)d_in[8]; const u16* gw=(const u16*)d_in[9];
  u16* ws=(u16*)d_ws; size_t o=0;
  u16* qkv=ws+o; o+=2048*1344; u16* oa=ws+o; o+=2048*448; u16* a=ws+o; o+=2048*448; u16* b=ws+o; o+=2048*448;
  u16* q=ws+o; o+=2048*4096; u16* k=ws+o; o+=2048*4096; u16* v=ws+o; o+=2048*4096; u16* ctx=ws+o; o+=2048*4096;
  dim3 tb(16,16);
  gemm_naive<<<dim3(1344/16,128),tb,0,stream>>>(x,wqkv,qkv,2048,1344,448);
  gemm_naive<<<dim3(256,128),tb,0,stream>>>(x,wq,q,2048,4096,448);
  gemm_naive<<<dim3(256,128),tb,0,stream>>>(ckv,wuk,k,2048,4096,1024);
  gemm_naive<<<dim3(256,128),tb,0,stream>>>(ckv,wuv,v,2048,4096,1024);
  attn_naive<<<dim3(2048,16),256,0,stream>>>(qkv,1344,qkv+448,1344,qkv+896,1344,oa,448,28,2048,0,1.f/sqrtf(28.f));
  attn_naive<<<dim3(2048,16),256,0,stream>>>(q,4096,k,4096,v,4096,ctx,4096,256,2048,1,1.f/16.f);
  gemm_naive<<<dim3(28,128),tb,0,stream>>>(oa,wo,a,2048,448,448);
  gemm_naive<<<dim3(28,128),tb,0,stream>>>(ctx,wob,b,2048,448,4096);
  mix_naive<<<2048,512,0,stream>>>(x,gw,a,b,(u16*)d_out);
}
```

```cpp
#include <hip/hip_runtime.h>
#include <stdint.h>
#include <utility>

typedef short bf16x8 __attribute__((ext_vector_type(8)));
typedef short s16x4 __attribute__((ext_vector_type(4)));
typedef float f32x16 __attribute__((ext_vector_type(16)));
typedef float f32x2 __attribute__((ext_vector_type(2)));
typedef __bf16 bf16x2_t __attribute__((ext_vector_type(2)));
typedef unsigned u32x4 __attribute__((ext_vector_type(4)));
typedef unsigned u32x2 __attribute__((ext_vector_type(2)));
#define LAS __attribute__((address_space(3)))
#define GAS __attribute__((address_space(1)))
#define DI __device__ __forceinline__
#define SBAR() __builtin_amdgcn_sched_barrier(0)
#define MFMA(a, b, c) __builtin_amdgcn_mfma_f32_32x32x16_bf16((a), (b), (c), 0, 0, 0)
#define PIN(x) asm volatile("" : "+v"(x))

constexpr size_t MB = 1u << 20;
constexpr size_t WS_QB = 0, WS_KB = 16 * MB, WS_VB = 32 * MB, WS_QA = 48 * MB, WS_KVA = 50 * MB, WS_G = 54 * MB,
                 WS_FLAG = 54 * MB + 65536, WS_ML = 55 * MB, WS_PO = 56 * MB, WS_OP = 64 * MB, WS_END = 92 * MB;
constexpr int TA = 17;
constexpr float LOG2E = 1.4426950408889634f;
constexpr float QSCALE_B = 0.0625f * LOG2E;
constexpr float QSCALE_A = 0.18898223650461362f * LOG2E;

template <int I> struct IC { static constexpr int v = I; };
template <class F, int... Is> DI void sfor_(F&& f, std::integer_sequence<int, Is...>) { (f(IC<Is>{}), ...); }
template <int N, class F> DI void sfor(F&& f) { sfor_(f, std::make_integer_sequence<int, N>{}); }
DI void mfma_vc(f32x16& d, const bf16x8& a, const bf16x8& b, const f32x16& c) { asm volatile("v_mfma_f32_32x32x16_bf16 %0, %1, %2, %3" : "=&v"(d) : "v"(a), "v"(b), "v"(c)); }
DI void mfma_v(f32x16& d, const bf16x8& a, const bf16x8& b) { asm volatile("v_mfma_f32_32x32x16_bf16 %0, %1, %2, %0" : "+v"(d) : "v"(a), "v"(b)); }
DI void mfma_vn(f32x16& d, const bf16x8& a, const bf16x8& b) { asm volatile("s_nop 1\n\tv_mfma_f32_32x32x16_bf16 %0, %1, %2, %0" : "+v"(d) : "v"(a), "v"(b)); }
DI void mfma_a(f32x16& d, const bf16x8& a, const bf16x8& b) { asm volatile("v_mfma_f32_32x32x16_bf16 %0, %1, %2, %0" : "+a"(d) : "v"(a), "v"(b)); }
#define MFMA_DRAIN() asm volatile("s_nop 7\n\ts_nop 7" ::: "memory")
DI unsigned rfl(unsigned x) { return __builtin_amdgcn_readfirstlane(x); }
DI unsigned cvtpk(float lo, float hi) { f32x2 v = {lo, hi}; bf16x2_t b = __builtin_convertvector(v, bf16x2_t); return __builtin_bit_cast(unsigned, b); }
DI float bflo(unsigned w) { return __uint_as_float(w << 16); }
DI float bfhi(unsigned w) { return __uint_as_float(w & 0xffff0000u); }
DI u32x4 mksrd(const void* p, unsigned bytes) { u32x4 s = {(unsigned)(uintptr_t)p, (unsigned)((uintptr_t)p >> 32) & 0xffffu, bytes, 0x20000u}; return s; }
DI bf16x8 ldsr(unsigned a) { return *(const LAS bf16x8*)(uintptr_t)a; }
DI bf16x8 ldstr(unsigned a, unsigned a2) {
  s16x4 x = __builtin_amdgcn_ds_read_tr16_b64_v4i16((LAS s16x4*)(uintptr_t)a);
  s16x4 y = __builtin_amdgcn_ds_read_tr16_b64_v4i16((LAS s16x4*)(uintptr_t)a2);
  return __builtin_shufflevector(x, y, 0, 1, 2, 3, 4, 5, 6, 7);
}
template <int OFF> DI void dma16(unsigned m0v, unsigned voff, u32x4 srd, unsigned soff) {
  asm volatile("s_mov_b32 m0, %0\n\ts_nop 0\n\tbuffer_load_dwordx4 %1, %2, %3 offen offset:%4 lds" ::"s"(m0v), "v"(voff), "s"(srd), "s"(soff), "i"(OFF) : "memory");
}
template <int V, bool L0> DI void wait_bar() {
  asm volatile("" ::: "memory");
  __builtin_amdgcn_s_waitcnt((V & 15) | ((V >> 4) << 14) | (7 << 4) | (L0 ? 0 : (15 << 8)));
  __builtin_amdgcn_s_barrier();
  asm volatile("" ::: "memory");
  SBAR();
}
DI bf16x8 pack8(const f32x16& a, int s, float sc) {
  u32x4 w = {cvtpk(a[8 * s] * sc, a[8 * s + 1] * sc), cvtpk(a[8 * s + 2] * sc, a[8 * s + 3] * sc), cvtpk(a[8 * s + 4] * sc, a[8 * s + 5] * sc), cvtpk(a[8 * s + 6] * sc, a[8 * s + 7] * sc)};
  return __builtin_bit_cast(bf16x8, w);
}
DI bf16x8 pack8(const f32x16& a, int s) {
  u32x4 w = {cvtpk(a[8 * s], a[8 * s + 1]), cvtpk(a[8 * s + 2], a[8 * s + 3]), cvtpk(a[8 * s + 4], a[8 * s + 5]), cvtpk(a[8 * s + 6], a[8 * s + 7])};
  return __builtin_bit_cast(bf16x8, w);
}
DI float swap_other(float v) {
  auto r = __builtin_amdgcn_permlane32_swap(__float_as_uint(v), __float_as_uint(v), false, false);
  return (threadIdx.x & 32) ? __uint_as_float(r[0]) : __uint_as_float(r[1]);
}

constexpr unsigned XRING = 65536;
struct Prov { u32x4 srd; unsigned voff, soff, pstr, kstr; };
struct K1State { unsigned xs, ws, xd; };

template <int Q> DI void k1_dma_w(Prov& w, unsigned m0w) { dma16<0>(m0w + Q * 4096, w.voff, w.srd, w.soff + Q * w.pstr); }
template <int Q> DI void k1_dma_x(Prov& x, unsigned m0x) { dma16<0>(m0x + Q * 4096, x.voff, x.srd, x.soff + Q * x.pstr); }

template <bool SWAP, int NTL, bool FIRST>
DI void k1_ktile(f32x16 (&acc)[4][NTL], bf16x8 (&xa)[2][4], bf16x8 (&wf)[2][4], unsigned (&xr)[4], unsigned (&wr)[4], K1State& st, Prov& xp, Prov& wp,
                 const Prov& xn, const Prov& wn, const int t, const int NKT, const unsigned wid) {
  constexpr int NM = 4 * NTL;
#pragma unroll
  for (int ks = 0; ks < 4; ++ks) {
    const int cur = ks & 1, nx = cur ^ 1;
    unsigned m0w = 0, m0x = 0;
    if (ks == 1) {
      if (t == NKT - 2) wp = wn;
      wait_bar<16, true>();
      m0w = st.ws + wid * 1024; m0x = XRING + st.xd + wid * 1024 + 16384;
    }
    if (ks == 3) {
      wait_bar<16, true>();
      xp.soff += xp.kstr; if (t == NKT - 3) xp = xn;
      st.xd = st.xs;
      m0w = st.ws + 16384 + wid * 1024; m0x = XRING + st.xd + wid * 1024;
      const unsigned xs2 = st.xs == 65536 ? 0u : st.xs + 32768u, dx = xs2 - st.xs, dw = st.ws ? (unsigned)-32768 : 32768u;
      st.xs = xs2; st.ws ^= 32768u;
#pragma unroll
      for (int i = 0; i < 4; ++i) { xr[i] += dx; PIN(xr[i]); }
#pragma unroll
      for (int i = 0; i < NTL; ++i) { wr[i] += dw; PIN(wr[i]); }
    }
    const int kn = (ks + 1) & 3;
#pragma unroll
    for (int i = 0; i < NM; ++i) {
      const int m = i / NTL, n = i % NTL;
      if (FIRST && ks == 0) { const f32x16 z = {}; acc[m][n] = SWAP ? MFMA(wf[cur][n], xa[cur][m], z) : MFMA(xa[cur][m], wf[cur][n], z); }
      else acc[m][n] = SWAP ? MFMA(wf[cur][n], xa[cur][m], acc[m][n]) : MFMA(xa[cur][m], wf[cur][n], acc[m][n]);
      if (i == 0) xa[nx][0] = ldsr(xr[kn]);
      else if (i <= NTL) wf[nx][i - 1] = ldstr(wr[i - 1] + (kn & 1) * 8192 + (kn >> 1) * 16384, wr[i - 1] + (kn & 1) * 8192 + (kn >> 1) * 16384 + 2048);
      else if (i <= NTL + 3) xa[nx][i - NTL] = ldsr(xr[kn] + (i - NTL) * 4096);
      if (ks & 1) {
        constexpr int D0 = NM - 8;
        if (i == D0 + 0) { if (ks == 1) k1_dma_x<4>(xp, m0x - 16384); else k1_dma_x<0>(xp, m0x); }
        if (i == D0 + 1) { if (ks == 1) k1_dma_x<5>(xp, m0x - 16384); else k1_dma_x<1>(xp, m0x); }
        if (i == D0 + 2) { if (ks == 1) k1_dma_x<6>(xp, m0x - 16384); else k1_dma_x<2>(xp, m0x); }
        if (i == D0 + 3) { if (ks == 1) k1_dma_x<7>(xp, m0x - 16384); else k1_dma_x<3>(xp, m0x); }
        if (i == D0 + 4) k1_dma_w<0>(wp, m0w);
        if (i == D0 + 5) k1_dma_w<1>(wp, m0w);
        if (i == D0 + 6) k1_dma_w<2>(wp, m0w);
        if (i == D0 + 7) { k1_dma_w<3>(wp, m0w); wp.soff += wp.kstr; }
      }
      SBAR();
    }
  }
}

template <bool SWAP, int NTL>
DI void k1_gemm(f32x16 (&acc)[4][NTL], K1State& st, Prov& xp, Prov& wp, const Prov& xn, const Prov& wn, const int NKT, const unsigned wid, const unsigned lane) {
  const unsigned r = lane & 31, hh = lane >> 5, q = (lane >> 2) & 3, wrow = wid >> 1, wc = wid & 1;
  unsigned xr[4], wr[4];
#pragma unroll
  for (int ks = 0; ks < 4; ++ks) { xr[ks] = XRING + st.xs + (128 * wrow + r) * 128 + 16 * ((2 * ks + hh) ^ ((r >> 1) & 7)); PIN(xr[ks]); }
#pragma unroll
  for (int n = 0; n < NTL; ++n) { wr[n] = st.ws + (8 * hh + q) * 512 + 64 * ((NTL * wc + n) ^ q) + 32 * ((lane >> 4) & 1) + 8 * (lane & 3); PIN(wr[n]); }
  bf16x8 xa[2][4], wf[2][4];
#pragma unroll
  for (int m = 0; m < 4; ++m) xa[0][m] = ldsr(xr[0] + m * 4096);
#pragma unroll
  for (int n = 0; n < NTL; ++n) wf[0][n] = ldstr(wr[n], wr[n] + 2048);
  SBAR();
  k1_ktile<SWAP, NTL, true>(acc, xa, wf, xr, wr, st, xp, wp, xn, wn, 0, NKT, wid);
  for (int t = 1; t < NKT; ++t) k1_ktile<SWAP, NTL, false>(acc, xa, wf, xr, wr, st, xp, wp, xn, wn, t, NKT, wid);
}

DI Prov mk_xprov(const char* X, unsigned ld, unsigned bytes, unsigned rb, unsigned wid, unsigned lane) {
  Prov p; p.srd = mksrd(X, bytes);
  p.voff = (lane >> 3) * ld + 16 * ((lane & 7) ^ (4 * (wid & 1) + (lane >> 4)));
  p.soff = (256 * rb + 8 * wid) * ld; p.pstr = 32 * ld; p.kstr = 128; return p;
}
DI Prov mk_wprov(const char* W, unsigned ld, unsigned bytes, unsigned col0, unsigned nchunk, unsigned wid, unsigned lane) {
  Prov p; p.srd = mksrd(W, bytes);
  const unsigned ch = (lane & 31) ^ (((2 * (wid & 1) + (lane >> 5)) & 3) << 2);
  p.voff = ch < nchunk ? (lane >> 5) * ld + 16 * ch : 0x7ffffff0u;
  p.soff = 2 * wid * ld + 2 * col0; p.pstr = 8 * ld; p.kstr = 32 * ld; return p;
}

__global__ __launch_bounds__(256, 1) void k1_proj(const char* __restrict__ x, const char* __restrict__ wqkv, const char* __restrict__ ckv, const char* __restrict__ wq,
                                                  const char* __restrict__ wuk, const char* __restrict__ wuv, const char* __restrict__ gate_w, char* __restrict__ ws) {
  extern __shared__ __attribute__((aligned(16))) char lds[];
  const unsigned tid = threadIdx.x, lane = tid & 63, wid = rfl(tid >> 6), r = lane & 31, hh = lane >> 5, wrow = wid >> 1, wc = wid & 1;
  const unsigned bid = blockIdx.x, xcd = bid & 7, loc = bid >> 3, rb = loc & 7, sel = loc >> 3;
  const unsigned cb = xcd + 8 * sel;
  const bool qvalid = cb < 30;
  const unsigned head2 = 2 * xcd + (sel & 1), isV = sel >> 1;
  Prov xkv = mk_xprov(ckv, 2048, 2048 * 2048, rb, wid, lane);
  Prov wkv = mk_wprov(isV ? wuv : wuk, 8192, 1024 * 8192, head2 * 256, 32, wid, lane);
  Prov none = xkv; none.srd[2] = 0;
  Prov xp, wp;
  if (qvalid) {
    xp = mk_xprov(x, 896, 2048 * 896, rb, wid, lane);
    if (cb < 22) wp = mk_wprov(wq, 8192, 448 * 8192, cb * 192, 24, wid, lane);
    else wp = mk_wprov(wqkv, 2688, 448 * 2688, cb < 27 ? (cb - 22) * 192 : 896 + (cb - 27) * 192, 24, wid, lane);
  } else { xp = xkv; wp = wkv; }
  K1State st = {0u, 0u, 65536u};
  {
    const unsigned mx = XRING + wid * 1024, mw = wid * 1024;
    k1_dma_x<0>(xp, mx); k1_dma_x<1>(xp, mx); k1_dma_x<2>(xp, mx); k1_dma_x<3>(xp, mx);
    k1_dma_x<4>(xp, mx); k1_dma_x<5>(xp, mx); k1_dma_x<6>(xp, mx); k1_dma_x<7>(xp, mx); xp.soff += xp.kstr;
    k1_dma_w<0>(wp, mw); k1_dma_w<1>(wp, mw); k1_dma_w<2>(wp, mw); k1_dma_w<3>(wp, mw); wp.soff += wp.kstr;
    k1_dma_x<0>(xp, mx + 32768); k1_dma_x<1>(xp, mx + 32768); k1_dma_x<2>(xp, mx + 32768); k1_dma_x<3>(xp, mx + 32768);
    k1_dma_w<0>(wp, mw + 16384); k1_dma_w<1>(wp, mw + 16384); k1_dma_w<2>(wp, mw + 16384); k1_dma_w<3>(wp, mw + 16384); wp.soff += wp.kstr;
    k1_dma_x<4>(xp, mx + 32768); k1_dma_x<5>(xp, mx + 32768); k1_dma_x<6>(xp, mx + 32768); k1_dma_x<7>(xp, mx + 32768); xp.soff += xp.kstr;
    k1_dma_w<0>(wp, mw + 32768); k1_dma_w<1>(wp, mw + 32768); k1_dma_w<2>(wp, mw + 32768); k1_dma_w<3>(wp, mw + 32768); wp.soff += wp.kstr;
    k1_dma_x<0>(xp, mx + 65536); k1_dma_x<1>(xp, mx + 65536); k1_dma_x<2>(xp, mx + 65536); k1_dma_x<3>(xp, mx + 65536);
    k1_dma_w<0>(wp, mw + 49152); k1_dma_w<1>(wp, mw + 49152); k1_dma_w<2>(wp, mw + 49152); k1_dma_w<3>(wp, mw + 49152); wp.soff += wp.kstr;
  }
  if (!qvalid) {
    const unsigned sp = (xcd - 6) * 8 + rb, row = sp * 128 + (tid >> 1), half = tid & 1;
    const u32x4* xr_ = (const u32x4*)(x + row * 896 + half * 448); const u32x4* gw = (const u32x4*)(gate_w + half * 448);
    float s = 0.f;
#pragma unroll 4
    for (int i = 0; i < 28; ++i) { const u32x4 a = xr_[i], b = gw[i];
#pragma unroll
      for (int e = 0; e < 4; ++e) { s = fmaf(bflo(a[e]), bflo(b[e]), s); s = fmaf(bfhi(a[e]), bfhi(b[e]), s); } }
    s += __shfl_xor(s, 1);
    if (!half) ((float*)(ws + WS_G))[row] = 1.f / (1.f + __expf(-s));
    if (tid < 32) *(unsigned*)(ws + WS_FLAG + (sp * 32 + tid) * 256) = 0u;
  }
  wait_bar<24, true>();
  const unsigned tb0 = rb * 8 + wrow * 4;
  if (qvalid) {
    f32x16 acc[4][3];
    if (cb < 27) {
      k1_gemm<true, 3>(acc, st, xp, wp, xkv, wkv, 7, wid, lane);
      if (cb < 22) {
#pragma unroll
        for (int n = 0; n < 3; ++n) { const unsigned col0 = cb * 192 + wc * 96 + n * 32;
          if (col0 < 4096) {
            char* dst = ws + WS_QB + (size_t)(((col0 >> 8) * 64 + tb0) * 16 + ((col0 & 255) >> 4)) * 1024 + lane * 16;
#pragma unroll
            for (int m = 0; m < 4; ++m) { *(bf16x8*)(dst + m * 16384) = pack8(acc[m][n], 0, QSCALE_B); *(bf16x8*)(dst + m * 16384 + 1024) = pack8(acc[m][n], 1, QSCALE_B); } } }
      } else {
#pragma unroll
        for (int n = 0; n < 3; ++n)
#pragma unroll
          for (int gq = 0; gq < 4; ++gq) {
            const unsigned col = (cb - 22) * 192 + wc * 96 + n * 32 + 8 * gq + 4 * hh;
            if (col < 896) {
              const unsigned G = col >> 2, isK = G >= 112, g2 = G - 112 * isK, hd = g2 / 7, d4 = g2 - 7 * hd;
              char* dst = (isK ? ws + WS_KVA + (size_t)(hd * 64 + tb0) * 4096 : ws + WS_QA + (size_t)(hd * 64 + tb0) * 2048) + (d4 >> 2) * 1024 + (((d4 >> 1) & 1) * 32 + r) * 16 + (d4 & 1) * 8;
              const float sc = isK ? 1.f : QSCALE_A; const unsigned mst = isK ? 4096 : 2048;
#pragma unroll
              for (int m = 0; m < 4; ++m) { const u32x2 w = {cvtpk(acc[m][n][4 * gq] * sc, acc[m][n][4 * gq + 1] * sc), cvtpk(acc[m][n][4 * gq + 2] * sc, acc[m][n][4 * gq + 3] * sc)};
                *(u32x2*)(dst + m * mst) = w; if (d4 == 6) *(u32x2*)(dst + m * mst + 8) = (u32x2){0u, 0u}; }
            } }
      }
    } else {
      k1_gemm<false, 3>(acc, st, xp, wp, xkv, wkv, 7, wid, lane);
#pragma unroll
      for (int n = 0; n < 3; ++n) { const unsigned c = (cb - 27) * 192 + wc * 96 + n * 32 + r;
        if (c < 448) { const unsigned hd = c / 28, d = c - 28 * hd;
          char* dst = ws + WS_KVA + (size_t)(hd * 64 + tb0) * 4096 + 2048 + (hh * 32 + d) * 16;
          const unsigned fill = d == 24 ? 0x3f803f80u : 0u;
#pragma unroll
          for (int m = 0; m < 4; ++m)
#pragma unroll
            for (int s = 0; s < 2; ++s) { *(bf16x8*)(dst + m * 4096 + s * 1024) = pack8(acc[m][n], s);
              if (d >= 24) *(u32x4*)(dst + m * 4096 + s * 1024 + 64) = (u32x4){fill, fill, fill, fill}; } } }
    }
  }
  {
    f32x16 acc[4][4];
    if (!isV) {
      k1_gemm<true, 4>(acc, st, xp, wp, none, none, 16, wid, lane);
      char* dst = ws + WS_KB + (size_t)((head2 * 64 + tb0) * 16 + wc * 8) * 1024 + lane * 16;
#pragma unroll
      for (int m = 0; m < 4; ++m)
#pragma unroll
        for (int n = 0; n < 4; ++n) { *(bf16x8*)(dst + m * 16384 + n * 2048) = pack8(acc[m][n], 0); *(bf16x8*)(dst + m * 16384 + n * 2048 + 1024) = pack8(acc[m][n], 1); }
    } else {
      k1_gemm<false, 4>(acc, st, xp, wp, none, none, 16, wid, lane);
      char* dst = ws + WS_VB + (size_t)((head2 * 64 + tb0) * 16 + wc * 4) * 1024 + lane * 16;
#pragma unroll
      for (int m = 0; m < 4; ++m)
#pragma unroll
        for (int n = 0; n < 4; ++n) { *(bf16x8*)(dst + m * 16384 + n * 1024) = pack8(acc[m][n], 0); *(bf16x8*)(dst + m * 16384 + n * 1024 + 8192) = pack8(acc[m][n], 1); }
    }
  }
  asm volatile("s_waitcnt vmcnt(0)" ::: "memory");
}

struct Stream {
  unsigned tlo, thi, tn;
  unsigned voffL, voffW;
  unsigned dseq, dslot, wo0;
  unsigned rslot;
  u32x4 srd; unsigned voff, s1, s2, s3;
};
DI unsigned nslot(unsigned s) { return s == 131072u ? 0u : s + 32768u; }
DI void st_prep(Stream& S) {
  S.srd[0] = __builtin_amdgcn_readlane(S.tlo, S.dseq); S.srd[1] = __builtin_amdgcn_readlane(S.thi, S.dseq); S.srd[2] = __builtin_amdgcn_readlane(S.tn, S.dseq); S.srd[3] = 0x20000u;
  const bool w = S.dseq >= S.wo0;
  S.voff = w ? S.voffW : S.voffL; S.s1 = w ? 6144u : 0u; S.s3 = w ? 18432u : 0u; S.s2 = w ? (S.dseq == S.wo0 ? 128u : 28672u) : 4096u;
}
template <int Q> DI void st_piece(Stream& S, unsigned wid) {
  if (Q == 0) asm volatile("s_nop 4" ::: "memory");
  dma16<(Q & 3) * 1024>(S.dslot + wid * 8192 + (Q >> 2) * 4096, S.voff, S.srd, ((Q & 3) == 3 ? S.s3 : (Q & 3) * S.s1) + (Q >> 2) * S.s2);
  if (Q == 7) { S.dseq += 1; S.dslot = nslot(S.dslot); }
}
DI void st_item(Stream& S, unsigned wid) {
  st_prep(S); st_piece<0>(S, wid); st_piece<1>(S, wid); st_piece<2>(S, wid); st_piece<3>(S, wid); st_piece<4>(S, wid); st_piece<5>(S, wid); st_piece<6>(S, wid); st_piece<7>(S, wid);
}
DI float maskv(float v, int dq, int rr) { return dq >= ((rr & 3) + 8 * (rr >> 2)) ? v : -__builtin_inff(); }
DI float max16(const f32x16& s) {
  float a = fmaxf(fmaxf(s[0], s[1]), s[2]), b = fmaxf(fmaxf(s[3], s[4]), s[5]);
  a = fmaxf(fmaxf(a, s[6]), s[7]); b = fmaxf(fmaxf(b, s[8]), s[9]); a = fmaxf(fmaxf(a, s[10]), s[11]); b = fmaxf(fmaxf(b, s[12]), s[13]);
  a = fmaxf(fmaxf(a, s[14]), s[15]); a = fmaxf(a, b);
  return fmaxf(a, swap_other(a));
}
DI f32x16 splat16(float v) { f32x16 r; for (int i = 0; i < 16; ++i) r[i] = v; return r; }

template <bool MASK>
DI void b_tile(f32x16 (&o)[8], const bf16x8 (&q)[16], bf16x8 (&fr)[8], const f32x16& negm, float& rs0, float& rs1, Stream& S, unsigned& rb_, const unsigned lane16, const unsigned wid, const int dq) {
  f32x16 s0, s1; u32x4 p[4]; float pe[32];
  sfor<64>([&](auto J) __attribute__((always_inline)) { constexpr int j = decltype(J)::v;
    if constexpr (j == 24 || j == 56) {
      if constexpr (j == 24) wait_bar<16, false>(); else wait_bar<11, false>();
      rb_ = S.rslot + lane16; PIN(rb_); S.rslot = nslot(S.rslot);
    }
    if constexpr (j == 0) mfma_vc(s0, fr[0], q[0], negm);
    else if constexpr (j < 16) mfma_v(s0, fr[j & 7], q[j]);
    else if constexpr (j == 16) mfma_vc(s1, fr[0], q[0], negm);
    else if constexpr (j < 32) mfma_v(s1, fr[j & 7], q[j - 16]);
    else mfma_a(o[j & 7], fr[j & 7], __builtin_bit_cast(bf16x8, p[(j - 32) >> 3]));
    fr[j & 7] = ldsr(rb_ + ((j + 8) & 31) * 1024);
    if constexpr (j >= 18 && j < 50) { constexpr int e = j - 18;
      float v = e < 16 ? s0[e & 15] : s1[e & 15]; if (MASK) v = maskv(v, e < 16 ? dq : dq - 32, e & 15); v = __builtin_amdgcn_exp2f(v); PIN(v); pe[e] = v; }
    if constexpr (j >= 19 && j < 51) { constexpr int e = j - 19;
      if constexpr (e & 1) { rs1 += pe[e]; PIN(rs1); unsigned w = cvtpk(pe[e - 1], pe[e]); PIN(w); p[e >> 3][(e >> 1) & 3] = w; }
      else { rs0 += pe[e]; PIN(rs0); } }
    if constexpr (j == 0 || j == 49) st_prep(S);
    if constexpr (j >= 1 && j <= 15 && (j & 1)) st_piece<(j >> 1)>(S, wid);
    if constexpr (j == 50 || j == 52 || j == 54) st_piece<((j - 50) >> 1)>(S, wid);
    if constexpr (j == 57 || j == 58 || j == 60 || j == 62 || j == 63) st_piece<(j == 57 ? 3 : j == 58 ? 4 : j == 60 ? 5 : j == 62 ? 6 : 7)>(S, wid);
    SBAR();
  });
}

DI void b_segment(f32x16 (&o)[8], const bf16x8 (&q)[16], float& mref, float& lsum, Stream& S, const char* kb_g, const char* vb_g, const int qb, const int t0, const int t1,
                  const unsigned lane, const unsigned wid) {
  const unsigned lane16 = lane * 16; const int r = lane & 31, hh = lane >> 5;
  const int qrow = qb * 128 + (int)wid * 32 + r - 4 * hh;
  unsigned rb_ = S.rslot + lane16; PIN(rb_); S.rslot = nslot(S.rslot);
  bf16x8 fr[8];
  {
    f32x16 s = {};
#pragma unroll
    for (int j = 0; j < 16; ++j) s = MFMA(ldsr(rb_ + j * 1024), q[j], s);
#pragma unroll
    for (int e = 0; e < 16; ++e) s[e] = maskv(s[e], qrow - t0 * 64, e);
    const float m = max16(s); mref = m == -__builtin_inff() ? 0.f : m;
  }
  f32x16 negm = splat16(-mref); PIN(negm);
#pragma unroll
  for (int j = 0; j < 8; ++j) { fr[j] = ldsr(rb_ + j * 1024); o[j] = f32x16{}; }
  float rs0 = 0.f, rs1 = 0.f;
  SBAR();
  const int tm = t1 < 2 * qb ? t1 : 2 * qb;
  int t = t0;
  for (; t < tm; ++t) b_tile<false>(o, q, fr, negm, rs0, rs1, S, rb_, lane16, wid, 0);
  for (; t < t1; ++t) b_tile<true>(o, q, fr, negm, rs0, rs1, S, rb_, lane16, wid, qrow - t * 64);
  MFMA_DRAIN();
  S.rslot = S.rslot == 0u ? 131072u : S.rslot - 32768u;
  float l = rs0 + rs1; l += swap_other(l);
  f32x2 chk = {l * 0.f, 0.f};
#pragma unroll
  for (int d = 0; d < 8; ++d)
#pragma unroll
    for (int e = 0; e < 16; e += 2) chk = __builtin_elementwise_fma((f32x2){o[d][e], o[d][e + 1]}, (f32x2){0.f, 0.f}, chk);
  const bool bad = !(chk[0] + chk[1] == 0.f);
  if (__builtin_expect(__any(bad), 0)) {
    float m = -1e30f, ls = 0.f;
#pragma unroll
    for (int d = 0; d < 8; ++d) o[d] = f32x16{};
    for (int kb = 2 * t0; kb < 2 * t1; ++kb) {
      const char* kp = kb_g + (size_t)kb * 16384 + lane16; const char* vp = vb_g + (size_t)kb * 16384 + lane16;
      f32x16 s = {};
#pragma unroll
      for (int j = 0; j < 16; ++j) s = MFMA(*(const bf16x8*)(kp + j * 1024), q[j], s);
#pragma unroll
      for (int e = 0; e < 16; ++e) s[e] = maskv(s[e], qrow - kb * 32, e);
      const float mn = fmaxf(m, max16(s)), al = __builtin_amdgcn_exp2f(m - mn); m = mn; ls *= al;
#pragma unroll
      for (int e = 0; e < 16; ++e) { s[e] = __builtin_amdgcn_exp2f(s[e] - mn); ls += s[e]; }
      const bf16x8 p0 = pack8(s, 0), p1 = pack8(s, 1);
#pragma unroll
      for (int d = 0; d < 8; ++d) { o[d] *= al; o[d] = MFMA(*(const bf16x8*)(vp + d * 1024), p0, o[d]); o[d] = MFMA(*(const bf16x8*)(vp + 8192 + d * 1024), p1, o[d]); }
    }
    l = ls + swap_other(ls); mref = m;
  }
  lsum = l;
}

DI void a_phase(bf16x8 (&ca)[2], Stream& S, const char* qa_g, const char* kva_g, const float g, const unsigned lane, const unsigned wid) {
  const unsigned lane16 = lane * 16;
  const bf16x8 q0 = *(const bf16x8*)(qa_g + lane16), q1 = *(const bf16x8*)(qa_g + 1024 + lane16);
  unsigned rb_ = S.rslot + lane16; PIN(rb_); S.rslot = nslot(S.rslot);
  bf16x8 kf[2][2], vf[2][2];
  kf[0][0] = ldsr(rb_); kf[0][1] = ldsr(rb_ + 1024); kf[1][0] = ldsr(rb_ + 4096); kf[1][1] = ldsr(rb_ + 5120); vf[0][0] = ldsr(rb_ + 2048); vf[0][1] = ldsr(rb_ + 3072);
  f32x16 s[2], o = {}, negm;
  { f32x16 z = {}; z = MFMA(kf[0][0], q0, z); z = MFMA(kf[0][1], q1, z); negm = splat16(-max16(z)); PIN(negm); }
  asm volatile("s_nop 1" ::: "memory");
  mfma_vc(s[0], kf[0][0], q0, negm); mfma_v(s[0], kf[0][1], q1);
  MFMA_DRAIN();
  SBAR();
  for (int it = 0; it < 8; ++it) {
    unsigned rn_ = rb_;
    sfor<8>([&](auto KB) __attribute__((always_inline)) { constexpr int kb = decltype(KB)::v, c = kb & 1, n = c ^ 1;
      if constexpr (kb == 6) { wait_bar<16, false>(); rn_ = S.rslot + lane16; PIN(rn_); S.rslot = nslot(S.rslot); }
      u32x4 p0, p1; float pe[16];
      mfma_vc(s[n], kf[n][0], q0, negm);
      vf[n][0] = ldsr(kb < 7 ? rb_ + (kb + 1) * 4096 + 2048 : rn_ + 2048);
      if constexpr (kb == 0) st_prep(S);
#pragma unroll
      for (int e = 0; e < 4; ++e) { pe[e] = __builtin_amdgcn_exp2f(s[c][e]); PIN(pe[e]); }
      SBAR();
      mfma_v(s[n], kf[n][1], q1);
      vf[n][1] = ldsr(kb < 7 ? rb_ + (kb + 1) * 4096 + 3072 : rn_ + 3072);
#pragma unroll
      for (int e = 4; e < 8; ++e) { pe[e] = __builtin_amdgcn_exp2f(s[c][e]); PIN(pe[e]); }
      kf[c][0] = ldsr(kb < 6 ? rb_ + (kb + 2) * 4096 : rn_ + (kb - 6) * 4096);
#pragma unroll
      for (int e = 0; e < 4; ++e) p0[e] = cvtpk(pe[2 * e], pe[2 * e + 1]);
      PIN(p0);
      SBAR();
      kf[c][1] = ldsr(kb < 6 ? rb_ + (kb + 2) * 4096 + 1024 : rn_ + (kb - 6) * 4096 + 1024);
#pragma unroll
      for (int e = 8; e < 10; ++e) { pe[e] = __builtin_amdgcn_exp2f(s[c][e]); PIN(pe[e]); }
      SBAR();
      mfma_v(o, vf[c][0], __builtin_bit_cast(bf16x8, p0));
#pragma unroll
      for (int e = 10; e < 16; ++e) { pe[e] = __builtin_amdgcn_exp2f(s[c][e]); PIN(pe[e]); }
      if constexpr (kb < 4) st_piece<2 * kb>(S, wid);
#pragma unroll
      for (int e = 0; e < 4; ++e) p1[e] = cvtpk(pe[8 + 2 * e], pe[8 + 2 * e + 1]);
      PIN(p1);
      if constexpr (kb < 4) st_piece<2 * kb + 1>(S, wid);
      SBAR();
      mfma_vn(o, vf[c][1], __builtin_bit_cast(bf16x8, p1));
      SBAR();
    });
    rb_ = rn_;
  }
  MFMA_DRAIN();
  S.rslot = S.rslot == 0u ? 131072u : S.rslot - 32768u;
  const bool up = lane >= 32;
  float chk = 0.f;
#pragma unroll
  for (int e = 0; e < 16; ++e) chk = fmaf(o[e], 0.f, chk);
  const bool bad = !(chk == 0.f);
  if (__builtin_expect(__any(bad), 0)) {
    float m = -1e30f; o = f32x16{};
    for (int kb = 0; kb < 64; ++kb) {
      const char* p = kva_g + (size_t)kb * 4096 + lane16;
      f32x16 t = {}; t = MFMA(*(const bf16x8*)p, q0, t); t = MFMA(*(const bf16x8*)(p + 1024), q1, t);
      const float mn = fmaxf(m, max16(t)), al = __builtin_amdgcn_exp2f(m - mn); m = mn;
#pragma unroll
      for (int e = 0; e < 16; ++e) t[e] = __builtin_amdgcn_exp2f(t[e] - mn);
      o *= al; o = MFMA(*(const bf16x8*)(p + 2048), pack8(t, 0), o); o = MFMA(*(const bf16x8*)(p + 3072), pack8(t, 1), o);
    }
  }
  const float lo = swap_other(o[12]), l = up ? o[12] : lo;
  const float sc = g / l;
#pragma unroll
  for (int e = 12; e < 16; ++e) o[e] = up ? 0.f : o[e];
  ca[0] = pack8(o, 0, sc); ca[1] = pack8(o, 1, sc);
}

__global__ __launch_bounds__(256, 1) void k2_attn(const char* __restrict__ wo_a, const char* __restrict__ wo_b, char* __restrict__ ws) {
  extern __shared__ __attribute__((aligned(16))) char lds[];
  const unsigned tid = threadIdx.x, lane = tid & 63, wid = rfl(tid >> 6), lane16 = lane * 16;
  const unsigned bid = blockIdx.x, head = 2 * (bid & 7) + ((bid >> 3) & 1), pi = bid >> 5, roleb = (bid >> 4) & 1;
  const int qlong = 15 - (int)pi, nt1 = 32 - 2 * (int)pi - TA;
  const int qb = roleb ? (int)pi : qlong, nt2 = roleb ? 2 * (int)pi + 2 : TA;
  const char* kb_g = ws + WS_KB + (size_t)head * MB; const char* vb_g = ws + WS_VB + (size_t)head * MB;
  const char* kva_g = ws + WS_KVA + (size_t)head * 262144;
  Stream S;
  {
    const unsigned n0 = roleb ? 2 * nt1 : 0, n1 = n0 + 8, n2 = n1 + 2 * nt2, j = lane;
    const char* a; unsigned n = 8192;
    if (j < n0) a = ((j & 1) ? vb_g : kb_g) + (size_t)(TA + (j >> 1)) * 32768 + wid * 8192;
    else if (j < n1) a = kva_g + (size_t)(j - n0) * 32768 + wid * 8192;
    else if (j < n2) a = (((j - n1) & 1) ? vb_g : kb_g) + (size_t)((j - n1) >> 1) * 32768 + wid * 8192;
    else if (j == n2) { const unsigned off = head * 28 * 896 + wid * 256; a = wo_a + off; n = 448 * 896 - off; }
    else if (j < n2 + 8) { const unsigned off = (head * 256 + wid * 64) * 896 + (j - n2 - 1) * 128; a = wo_b + off; n = 4096 * 896 - off; }
    else { a = kb_g; n = 0; }
    S.tlo = (unsigned)(uintptr_t)a; S.thi = (unsigned)((uintptr_t)a >> 32) & 0xffffu; S.tn = n;
    S.voffL = lane16; S.voffW = (lane >> 3) * 896 + 16 * ((lane & 7) ^ (((lane >> 4) & 1) << 2));
    S.dseq = 0; S.dslot = 0; S.wo0 = n2; S.rslot = 0;
  }
  st_item(S, wid); st_item(S, wid); st_item(S, wid);
  const unsigned pw = (head * 8 + pi) * 4 + wid;
  char* po_g = ws + WS_PO + (size_t)pw * 16384; GAS unsigned long long* ml_g = (GAS unsigned long long*)(ws + WS_ML + (size_t)pw * 512 + lane * 8);
  GAS unsigned* flag = (GAS unsigned*)(ws + WS_FLAG + (size_t)pw * 256);
  bf16x8 q[16]; f32x16 o[8]; float mref, lsum;
  if (roleb && nt1 > 0) {
    const char* qg = ws + WS_QB + (size_t)((head * 64 + qlong * 4 + wid) * 16) * 1024 + lane16;
#pragma unroll
    for (int j = 0; j < 16; ++j) q[j] = *(const bf16x8*)(qg + j * 1024);
    wait_bar<16, false>();
    b_segment(o, q, mref, lsum, S, kb_g, vb_g, qlong, TA, TA + nt1, lane, wid);
    const float inv = lsum > 0.f ? 1.f / lsum : 0.f;
    const __amdgpu_buffer_rsrc_t rs = __builtin_amdgcn_make_buffer_rsrc(po_g, 0, 16384, 0x20000);
#pragma unroll
    for (int c = 0; c < 16; ++c) __builtin_amdgcn_raw_buffer_store_b128(__builtin_bit_cast(u32x4, pack8(o[c >> 1], c & 1, inv)), rs, lane16 + c * 1024, 0, 16);
    __hip_atomic_store(ml_g, ((unsigned long long)__float_as_uint(lsum) << 32) | __float_as_uint(mref), __ATOMIC_RELAXED, __HIP_MEMORY_SCOPE_AGENT);
    asm volatile("s_waitcnt vmcnt(0)" ::: "memory");
    if (lane == 0) __hip_atomic_store(flag, 1u, __ATOMIC_RELAXED, __HIP_MEMORY_SCOPE_AGENT);
  } else wait_bar<16, false>();
  const unsigned tb = qb * 4 + wid;
  const float g = ((const float*)(ws + WS_G))[tb * 32 + (lane & 31)];
  {
    const char* qg = ws + WS_QB + (size_t)((head * 64 + tb) * 16) * 1024 + lane16;
#pragma unroll
    for (int j = 0; j < 16; ++j) q[j] = *(const bf16x8*)(qg + j * 1024);
  }
  bf16x8 ca[2];
  a_phase(ca, S, ws + WS_QA + (size_t)(head * 64 + tb) * 2048, kva_g, g, lane, wid);
  b_segment(o, q, mref, lsum, S, kb_g, vb_g, qb, 0, nt2, lane, wid);
  bf16x8 cb[16];
  if (!roleb && nt1 > 0) {
    for (unsigned spins = 0; spins < (1u << 22); ++spins) {
      if (rfl(__hip_atomic_load(flag, __ATOMIC_RELAXED, __HIP_MEMORY_SCOPE_AGENT)) == 1u) break;
      __builtin_amdgcn_s_sleep(1);
    }
    __builtin_amdgcn_fence(__ATOMIC_ACQUIRE, "agent");
    const unsigned long long ml = __hip_atomic_load(ml_g, __ATOMIC_RELAXED, __HIP_MEMORY_SCOPE_AGENT);
    u32x4 pb[16];
#pragma unroll
    for (int c = 0; c < 16; ++c) pb[c] = *(const GAS u32x4*)(po_g + lane16 + c * 1024);
    const float mb = __uint_as_float((unsigned)ml), lb = __uint_as_float((unsigned)(ml >> 32));
    const float m = lb > 0.f ? fmaxf(mref, mb) : mref, ea = __builtin_amdgcn_exp2f(mref - m), wb = lb > 0.f ? lb * __builtin_amdgcn_exp2f(mb - m) : 0.f;
    const float inv = (1.f - g) / (lsum * ea + wb), fa = ea * inv, fb = wb * inv;
#pragma unroll
    for (int c = 0; c < 16; ++c) { u32x4 w;
#pragma unroll
      for (int e = 0; e < 4; ++e) w[e] = cvtpk(fmaf(o[c >> 1][8 * (c & 1) + 2 * e], fa, bflo(pb[c][e]) * fb), fmaf(o[c >> 1][8 * (c & 1) + 2 * e + 1], fa, bfhi(pb[c][e]) * fb));
      cb[c] = __builtin_bit_cast(bf16x8, w); }
  } else {
    const float inv = (1.f - g) / lsum;
#pragma unroll
    for (int c = 0; c < 16; ++c) cb[c] = pack8(o[c >> 1], c & 1, inv);
  }
  {
    const unsigned hh = lane >> 5, qq = (lane >> 2) & 3;
    unsigned tbase[2];
#pragma unroll
    for (int nl = 0; nl < 2; ++nl) tbase[nl] = (4 * hh + qq) * 128 + 64 * (nl ^ (qq >> 1)) + 32 * ((lane >> 4) & 1) + 8 * (lane & 3);
    char* og = ws + WS_OP + (size_t)((head * 64 + tb) * 28) * 1024 + lane16;
    f32x16 acc[14];
    unsigned ra[2] = {S.rslot + tbase[0], S.rslot + tbase[1]}; PIN(ra[0]); PIN(ra[1]); S.rslot = nslot(S.rslot);
    st_item(S, wid);
#pragma unroll
    for (int nb = 0; nb < 14; ++nb) { const unsigned a = ra[nb & 1] + (nb >> 1) * 4096; const f32x16 z = {};
      acc[nb] = MFMA(ldstr(a, a + 1024), ca[0], z); acc[nb] = MFMA(ldstr(a + 2048, a + 3072), ca[1], acc[nb]); }
    wait_bar<16, false>();
    ra[0] = S.rslot + tbase[0]; ra[1] = S.rslot + tbase[1]; PIN(ra[0]); PIN(ra[1]); S.rslot = nslot(S.rslot);
    bf16x8 fr[8]; u32x4 ow;
#pragma unroll
    for (int j = 0; j < 8; ++j) fr[j] = ldstr(ra[0] + j * 2048, ra[0] + j * 2048 + 1024);
    SBAR();
    sfor<224>([&](auto F) __attribute__((always_inline)) { constexpr int f = decltype(F)::v, nb = f >> 4, ks = f & 15, f8 = f + 8;
      if constexpr ((f & 31) == 24 && f8 < 224) { wait_bar<16, false>(); ra[0] = S.rslot + tbase[0]; PIN(ra[0]); ra[1] = S.rslot + tbase[1]; PIN(ra[1]); S.rslot = nslot(S.rslot); }
      acc[nb] = MFMA(fr[f & 7], cb[ks], acc[nb]);
      if constexpr (f8 < 224) { const unsigned a = ra[(f8 >> 4) & 1] + (f8 & 15) * 2048; fr[f & 7] = ldstr(a, a + 1024); }
      if constexpr ((f & 31) == 0) st_prep(S);
      if constexpr ((f & 31) >= 1 && (f & 31) <= 15 && (f & 1)) st_piece<((f & 31) >> 1)>(S, wid);
      if constexpr (nb > 0 && ks >= 3 && ks < 13) { constexpr int u = ks - 3, s = u / 5, e = u % 5;
        if constexpr (e < 4) { unsigned w = cvtpk(acc[nb - 1][8 * s + 2 * e], acc[nb - 1][8 * s + 2 * e + 1]); PIN(w); ow[e] = w; }
        else *(u32x4*)(og + ((nb - 1) * 2 + s) * 1024) = ow; }
      SBAR();
    });
    *(bf16x8*)(og + 26 * 1024) = pack8(acc[13], 0); *(bf16x8*)(og + 27 * 1024) = pack8(acc[13], 1);
  }
  asm volatile("s_waitcnt vmcnt(0)" ::: "memory");
}

__global__ __launch_bounds__(256) void k3_sum(const char* __restrict__ ws, char* __restrict__ out) {
  const unsigned c = blockIdx.x * 256 + threadIdx.x, lane = c & 63, f = c >> 6, tb = f / 28, rem = f - tb * 28;
  const char* p = ws + WS_OP + (size_t)c * 16;
  u32x4 v[16];
#pragma unroll
  for (int h = 0; h < 16; ++h) v[h] = *(const u32x4*)(p + (size_t)h * (64 * 28 * 1024));
  float a[8] = {};
#pragma unroll
  for (int h = 0; h < 16; ++h)
#pragma unroll
    for (int e = 0; e < 4; ++e) { a[2 * e] += bflo(v[h][e]); a[2 * e + 1] += bfhi(v[h][e]); }
  char* o = out + (size_t)(tb * 32 + (lane & 31)) * 896 + (rem * 16 + 4 * (lane >> 5)) * 2;
  *(u32x2*)o = (u32x2){cvtpk(a[0], a[1]), cvtpk(a[2], a[3])};
  *(u32x2*)(o + 16) = (u32x2){cvtpk(a[4], a[5]), cvtpk(a[6], a[7])};
}

extern "C" void kernel_launch(void* const* d_in, const int* in_sizes, int n_in, void* d_out, int out_size, void* d_ws, size_t ws_size, hipStream_t stream) {
  (void)in_sizes; (void)n_in; (void)out_size; (void)ws_size;
  (void)hipFuncSetAttribute((const void*)k1_proj, hipFuncAttributeMaxDynamicSharedMemorySize, 163840);
  (void)hipFuncSetAttribute((const void*)k2_attn, hipFuncAttributeMaxDynamicSharedMemorySize, 163840);
  char* ws = (char*)d_ws;
  k1_proj<<<256, 256, 163840, stream>>>((const char*)d_in[0], (const char*)d_in[1], (const char*)d_in[4], (const char*)d_in[5], (const char*)d_in[6], (const char*)d_in[7], (const char*)d_in[9], ws);
  k2_attn<<<256, 256, 163840, stream>>>((const char*)d_in[2], (const char*)d_in[8], ws);
  k3_sum<<<448, 256, 0, stream>>>(ws, (char*)d_out);
}
```

```cpp
#include <hip/hip_runtime.h>
#include <stdint.h>
#include <utility>

typedef short bf16x8 __attribute__((ext_vector_type(8)));
typedef short s16x4 __attribute__((ext_vector_type(4)));
typedef float f32x16 __attribute__((ext_vector_type(16)));
typedef float f32x2 __attribute__((ext_vector_type(2)));
typedef __bf16 bf16x2_t __attribute__((ext_vector_type(2)));
typedef unsigned u32x4 __attribute__((ext_vector_type(4)));
typedef unsigned u32x2 __attribute__((ext_vector_type(2)));
#define LAS __attribute__((address_space(3)))
#define GAS __attribute__((address_space(1)))
#define DI __device__ __forceinline__
#define SBAR() __builtin_amdgcn_sched_barrier(0)
#define MFMA(a, b, c) __builtin_amdgcn_mfma_f32_32x32x16_bf16((a), (b), (c), 0, 0, 0)
#define PIN(x) asm volatile("" : "+v"(x))

constexpr size_t MB = 1u << 20;
constexpr size_t WS_QB = 0, WS_KB = 16 * MB, WS_VB = 32 * MB, WS_QA = 48 * MB, WS_KVA = 50 * MB, WS_G = 54 * MB,
                 WS_ML = 55 * MB, WS_PO = 56 * MB, WS_OP = 64 * MB, WS_WOF = 92 * MB, WS_END = 96 * MB;
static_assert(true, "");
constexpr int TA = 17;
constexpr float LOG2E = 1.4426950408889634f;
constexpr float QSCALE_B = 0.0625f * LOG2E;
constexpr float QSCALE_A = 0.18898223650461362f * LOG2E;

template <int I> struct IC { static constexpr int v = I; };
template <class F, int... Is> DI void sfor_(F&& f, std::integer_sequence<int, Is...>) { (f(IC<Is>{}), ...); }
template <int N, class F> DI void sfor(F&& f) { sfor_(f, std::make_integer_sequence<int, N>{}); }
DI void mfma_vc(f32x16& d, const bf16x8& a, const bf16x8& b, const f32x16& c) { asm volatile("v_mfma_f32_32x32x16_bf16 %0, %1, %2, %3" : "=&v"(d) : "v"(a), "v"(b), "v"(c)); }
DI void mfma_v(f32x16& d, const bf16x8& a, const bf16x8& b) { asm volatile("v_mfma_f32_32x32x16_bf16 %0, %1, %2, %0" : "+v"(d) : "v"(a), "v"(b)); }
DI void mfma_vn(f32x16& d, const bf16x8& a, const bf16x8& b) { asm volatile("s_nop 1\n\tv_mfma_f32_32x32x16_bf16 %0, %1, %2, %0" : "+v"(d) : "v"(a), "v"(b)); }
DI void mfma_a(f32x16& d, const bf16x8& a, const bf16x8& b) { asm volatile("v_mfma_f32_32x32x16_bf16 %0, %1, %2, %0" : "+a"(d) : "v"(a), "v"(b)); }
#define MFMA_DRAIN_V(x) asm volatile("s_nop 7\n\ts_nop 7" : "+v"(x))
#define MFMA_DRAIN_O(o) asm volatile("s_nop 7\n\ts_nop 7" : "+a"(o[0]), "+a"(o[1]), "+a"(o[2]), "+a"(o[3]), "+a"(o[4]), "+a"(o[5]), "+a"(o[6]), "+a"(o[7]))
DI float ard(float a) { float r; asm volatile("v_accvgpr_read_b32 %0, %1" : "=v"(r) : "a"(a)); return r; }
DI float opaque0() { float z; asm volatile("v_mov_b32 %0, 0" : "=v"(z)); return z; }
DI unsigned rfl(unsigned x) { return __builtin_amdgcn_readfirstlane(x); }
DI unsigned cvtpk(float lo, float hi) { f32x2 v = {lo, hi}; bf16x2_t b = __builtin_convertvector(v, bf16x2_t); return __builtin_bit_cast(unsigned, b); }
DI float bflo(unsigned w) { return __uint_as_float(w << 16); }
DI float bfhi(unsigned w) { return __uint_as_float(w & 0xffff0000u); }
DI u32x4 mksrd(const void* p, unsigned bytes) { u32x4 s = {(unsigned)(uintptr_t)p, (unsigned)((uintptr_t)p >> 32) & 0xffffu, bytes, 0x20000u}; return s; }
DI bf16x8 ldsr(unsigned a) { return *(const LAS bf16x8*)(uintptr_t)a; }
DI bf16x8 ldstr(unsigned a, unsigned a2) {
  s16x4 x = __builtin_amdgcn_ds_read_tr16_b64_v4i16((LAS s16x4*)(uintptr_t)a);
  s16x4 y = __builtin_amdgcn_ds_read_tr16_b64_v4i16((LAS s16x4*)(uintptr_t)a2);
  return __builtin_shufflevector(x, y, 0, 1, 2, 3, 4, 5, 6, 7);
}
DI void set_m0(unsigned v) { asm volatile("s_mov_b32 m0, %0" ::"s"(v) : "memory"); }
template <int OFF, bool SC1 = false> DI void dma16(unsigned voff, u32x4 srd, unsigned soff) {
  if constexpr (SC1) asm volatile("buffer_load_dwordx4 %0, %1, %2 offen offset:%3 sc1 lds" ::"v"(voff), "s"(srd), "s"(soff), "i"(OFF) : "memory");
  else asm volatile("buffer_load_dwordx4 %0, %1, %2 offen offset:%3 lds" ::"v"(voff), "s"(srd), "s"(soff), "i"(OFF) : "memory");
}
#define PINS(x) asm volatile("" : "+s"(x))
#define PINA(x) asm volatile("" : "+a"(x))
#define NOP0() asm volatile("s_nop 0" ::: "memory")
template <int V, bool L0> DI void wait_bar() {
  asm volatile("" ::: "memory");
  __builtin_amdgcn_s_waitcnt((V & 15) | ((V >> 4) << 14) | (7 << 4) | (L0 ? 0 : (15 << 8)));
  __builtin_amdgcn_s_barrier();
  asm volatile("" ::: "memory");
  SBAR();
}
DI bf16x8 pack8(const f32x16& a, int s, float sc) {
  u32x4 w = {cvtpk(a[8 * s] * sc, a[8 * s + 1] * sc), cvtpk(a[8 * s + 2] * sc, a[8 * s + 3] * sc), cvtpk(a[8 * s + 4] * sc, a[8 * s + 5] * sc), cvtpk(a[8 * s + 6] * sc, a[8 * s + 7] * sc)};
  return __builtin_bit_cast(bf16x8, w);
}
DI bf16x8 pack8(const f32x16& a, int s) {
  u32x4 w = {cvtpk(a[8 * s], a[8 * s + 1]), cvtpk(a[8 * s + 2], a[8 * s + 3]), cvtpk(a[8 * s + 4], a[8 * s + 5]), cvtpk(a[8 * s + 6], a[8 * s + 7])};
  return __builtin_bit_cast(bf16x8, w);
}
DI float swap_other(float v) {
  auto r = __builtin_amdgcn_permlane32_swap(__float_as_uint(v), __float_as_uint(v), false, false);
  return (threadIdx.x & 32) ? __uint_as_float(r[0]) : __uint_as_float(r[1]);
}

#define STAMP_DECL
#define STAMPIT(i)
#define STAMPRT(i)
#define STAMP_OUT(k, n)
#define TR_DECL
#define TR_ARG
#define TR_PASS
#define TRC()
#define TR_OUT()
__host__ __device__ constexpr bool wf_isa(int f) { return f < 14 || (f >= 126 && (f - 126) % 18 < 2); }
__host__ __device__ constexpr int wf_nb(int f) { return f < 14 ? f >> 1 : f < 126 ? (f - 14) % 7 : 7 + (f - 126) / 18; }
__host__ __device__ constexpr int wf_ks(int f) { return f < 14 ? f & 1 : f < 126 ? (f - 14) / 7 : (f - 126) % 18 < 2 ? (f - 126) % 18 : (f - 126) % 18 - 2; }
constexpr unsigned XRING = 65536;
struct Prov { u32x4 srd; unsigned voff, soff, pstr, kstr; };
DI void prov_next(Prov& p) { p.soff += p.kstr; }
struct K1State { unsigned xs, ws, xd; };

template <int Q> DI void k1_dma(Prov& p, unsigned m0b) { set_m0(m0b + Q * 4096); NOP0(); dma16<0>(p.voff, p.srd, p.soff + Q * p.pstr); }
#define k1_dma_w k1_dma
#define k1_dma_x k1_dma

template <bool SWAP, int NTL, bool FIRST>
DI void k1_ktile(f32x16 (&acc)[4][NTL], bf16x8 (&xa)[2][4], bf16x8 (&wf)[2][4], unsigned (&xr)[4], unsigned (&wr)[4], K1State& st, Prov& xp, Prov& wp,
                 const Prov& xn, const Prov& wn, const int t, const int NKT, const unsigned wid) {
  constexpr int NM = 4 * NTL;
  sfor<4>([&](auto KS) __attribute__((always_inline)) { constexpr int ks = decltype(KS)::v, cur = ks & 1, nx = cur ^ 1, kn = (ks + 1) & 3;
    unsigned m0b;
    if constexpr (ks == 0) m0b = (st.ws ^ 32768u) + 16384 + wid * 1024;
    if constexpr (ks == 1) { wait_bar<16, true>(); m0b = XRING + st.xd + wid * 1024 + 16384; }
    if constexpr (ks == 2) { if (t == NKT - 2) wp = wn; m0b = st.ws + wid * 1024; }
    if constexpr (ks == 3) {
      wait_bar<16, true>();
      prov_next(xp); if (t == NKT - 3) xp = xn;
      st.xd = st.xs;
      m0b = XRING + st.xd + wid * 1024;
      const unsigned xs2 = st.xs == 65536 ? 0u : st.xs + 32768u, dx = xs2 - st.xs, dw = st.ws ? (unsigned)-32768 : 32768u;
      st.xs = xs2; st.ws ^= 32768u;
#pragma unroll
      for (int i = 0; i < 4; ++i) { xr[i] += dx; PIN(xr[i]); }
#pragma unroll
      for (int i = 0; i < NTL; ++i) { wr[i] += dw; PIN(wr[i]); }
    }
    Prov& pp = (ks & 1) ? xp : wp;
    unsigned so = pp.soff + (ks == 1 ? 4 * pp.pstr : 0);
    sfor<NM>([&](auto I) __attribute__((always_inline)) { constexpr int i = decltype(I)::v, m = i / NTL, n = i % NTL;
      if constexpr (FIRST && ks == 0) { const f32x16 z = {}; acc[m][n] = SWAP ? MFMA(wf[cur][n], xa[cur][m], z) : MFMA(xa[cur][m], wf[cur][n], z); }
      else acc[m][n] = SWAP ? MFMA(wf[cur][n], xa[cur][m], acc[m][n]) : MFMA(xa[cur][m], wf[cur][n], acc[m][n]);
      if constexpr (i == 0) xa[nx][0] = ldsr(xr[kn]);
      else if constexpr (i <= NTL) wf[nx][i - 1] = ldstr(wr[i - 1] + (kn & 1) * 8192 + (kn >> 1) * 16384, wr[i - 1] + (kn & 1) * 8192 + (kn >> 1) * 16384 + 2048);
      else if constexpr (i <= NTL + 3) xa[nx][i - NTL] = ldsr(xr[kn] + (i - NTL) * 4096);
      constexpr int D0 = NM - 8;
      if constexpr (i >= D0) {
        if constexpr (((i - D0) & 1) == 0) { set_m0(m0b + ((i - D0) >> 1) * 4096); if constexpr (i > D0) so += pp.pstr; PINS(so); }
        else dma16<0>(pp.voff, pp.srd, so);
      }
      SBAR();
    });
    if constexpr (!(ks & 1)) prov_next(wp);
  });
}

template <bool SWAP, int NTL>
DI void k1_gemm(f32x16 (&acc)[4][NTL], K1State& st, Prov& xp, Prov& wp, const Prov& xn, const Prov& wn, const int NKT, const unsigned wid, const unsigned lane) {
  const unsigned r = lane & 31, hh = lane >> 5, q = (lane >> 2) & 3, wrow = wid >> 1, wc = wid & 1;
  unsigned xr[4], wr[4];
#pragma unroll
  for (int ks = 0; ks < 4; ++ks) { xr[ks] = XRING + st.xs + (128 * wrow + r) * 128 + 16 * ((2 * ks + hh) ^ ((r >> 1) & 7)); PIN(xr[ks]); }
#pragma unroll
  for (int n = 0; n < NTL; ++n) { wr[n] = st.ws + (8 * hh + q) * 512 + 64 * ((NTL * wc + n) ^ q) + 32 * ((lane >> 4) & 1) + 8 * (lane & 3); PIN(wr[n]); }
  bf16x8 xa[2][4], wf[2][4];
#pragma unroll
  for (int m = 0; m < 4; ++m) xa[0][m] = ldsr(xr[0] + m * 4096);
#pragma unroll
  for (int n = 0; n < NTL; ++n) wf[0][n] = ldstr(wr[n], wr[n] + 2048);
  SBAR();
  k1_ktile<SWAP, NTL, true>(acc, xa, wf, xr, wr, st, xp, wp, xn, wn, 0, NKT, wid);
  for (int t = 1; t < NKT; ++t) k1_ktile<SWAP, NTL, false>(acc, xa, wf, xr, wr, st, xp, wp, xn, wn, t, NKT, wid);
}

DI Prov mk_xprov(const char* X, unsigned ld, unsigned bytes, unsigned rb, unsigned wid, unsigned lane) {
  Prov p; p.srd = mksrd(X, bytes);
  p.voff = (lane >> 3) * ld + 16 * ((lane & 7) ^ (4 * (wid & 1) + (lane >> 4)));
  p.soff = (256 * rb + 8 * wid) * ld; p.pstr = 32 * ld; p.kstr = 128; return p;
}
template <class F> DI Prov mk_wprov(const char* W, unsigned ld, unsigned bytes, unsigned wid, unsigned lane, F colf) {
  Prov p; p.srd = mksrd(W, bytes);
  const unsigned ch = (lane & 31) ^ (((2 * (wid & 1) + (lane >> 5)) & 3) << 2), col = colf(ch);
  p.voff = col < 0x10000u ? (lane >> 5) * ld + 2 * col : 0x7ffffff0u;
  p.soff = 2 * wid * ld; p.pstr = 8 * ld; p.kstr = 32 * ld; return p;
}
#define ST_AUX 0
DI void st16(__amdgpu_buffer_rsrc_t rs, unsigned voff, unsigned soff, bf16x8 v) { __builtin_amdgcn_raw_buffer_store_b128(__builtin_bit_cast(u32x4, v), rs, voff, soff, ST_AUX); }

__global__ __launch_bounds__(256, 1) void k1_proj(const char* __restrict__ x, const char* __restrict__ wqkv, const char* __restrict__ ckv, const char* __restrict__ wq,
                                                  const char* __restrict__ wuk, const char* __restrict__ wuv, const char* __restrict__ gate_w,
                                                  const char* __restrict__ wo_a, const char* __restrict__ wo_b, char* __restrict__ ws) {
  extern __shared__ __attribute__((aligned(16))) char lds[];
  const unsigned tid = threadIdx.x, lane = tid & 63, wid = rfl(tid >> 6), r = lane & 31, hh = lane >> 5, wrow = wid >> 1, wc = wid & 1;
  const unsigned bid = blockIdx.x, xcd = bid & 7, loc = bid >> 3, rb = loc & 7, sel = loc >> 3;
  STAMP_DECL STAMPRT(0); STAMPIT(1);
  const unsigned cb = xcd + 8 * sel;
  const unsigned head2 = 2 * xcd + (sel & 1), isV = sel >> 1;
  Prov xp = mk_xprov(ckv, 2048, 2048 * 2048, rb, wid, lane);
  Prov wp = mk_wprov(isV ? wuv : wuk, 8192, 1024 * 8192, wid, lane, [&](unsigned ch) { return head2 * 256 + 8 * ch; });
  K1State st = {0u, 0u, 65536u};
  {
    const unsigned mx = XRING + wid * 1024, mw = wid * 1024;
    k1_dma_x<0>(xp, mx); k1_dma_x<1>(xp, mx); k1_dma_x<2>(xp, mx); k1_dma_x<3>(xp, mx);
    k1_dma_x<4>(xp, mx); k1_dma_x<5>(xp, mx); k1_dma_x<6>(xp, mx); k1_dma_x<7>(xp, mx); prov_next(xp);
    k1_dma_w<0>(wp, mw); k1_dma_w<1>(wp, mw); k1_dma_w<2>(wp, mw); k1_dma_w<3>(wp, mw); prov_next(wp);
    k1_dma_x<0>(xp, mx + 32768); k1_dma_x<1>(xp, mx + 32768); k1_dma_x<2>(xp, mx + 32768); k1_dma_x<3>(xp, mx + 32768);
    k1_dma_w<0>(wp, mw + 16384); k1_dma_w<1>(wp, mw + 16384); k1_dma_w<2>(wp, mw + 16384); k1_dma_w<3>(wp, mw + 16384); prov_next(wp);
    k1_dma_x<4>(xp, mx + 32768); k1_dma_x<5>(xp, mx + 32768); k1_dma_x<6>(xp, mx + 32768); k1_dma_x<7>(xp, mx + 32768); prov_next(xp);
    k1_dma_w<0>(wp, mw + 32768); k1_dma_w<1>(wp, mw + 32768); k1_dma_w<2>(wp, mw + 32768); k1_dma_w<3>(wp, mw + 32768); prov_next(wp);
    k1_dma_x<0>(xp, mx + 65536); k1_dma_x<1>(xp, mx + 65536); k1_dma_x<2>(xp, mx + 65536); k1_dma_x<3>(xp, mx + 65536);
  }
  Prov xq = mk_xprov(x, 896, cb < 31 ? 2048 * 896 : 0, rb, wid, lane);
  Prov wq_ = cb < 22 ? mk_wprov(wq, 8192, 448 * 8192, wid, lane, [&](unsigned ch) { return ch < 24 ? cb * 192 + 8 * ch : 0x10000u; })
                     : mk_wprov(wqkv, 2688, cb < 31 ? 448 * 2688 : 0, wid, lane, [&](unsigned ch) { const unsigned vc = (cb < 28 ? cb - 22 : cb - 28) * 24 + ch, slot = vc >> 2;
                         return (ch < 24 && slot < (cb < 28 ? 32u : 16u)) ? (cb < 28 ? 0u : 896u) + 28 * slot + 8 * (vc & 3) : 0x10000u; });
  Prov none = xq; none.srd[2] = 0;
  wait_bar<20, true>();
  STAMPIT(2);
  const unsigned tb0 = rb * 8 + wrow * 4;
  const __amdgpu_buffer_rsrc_t rs = __builtin_amdgcn_make_buffer_rsrc(ws, 0, (int)WS_END, 0x20000);
  const unsigned lane16 = lane * 16;
  {
    f32x16 acc[4][4];
    if (!isV) {
      k1_gemm<true, 4>(acc, st, xp, wp, xq, wq_, 16, wid, lane);
      STAMPIT(3);
      const unsigned dst = (unsigned)WS_KB + ((head2 * 64 + tb0) * 16 + wc * 8) * 1024;
#pragma unroll
      for (int m = 0; m < 4; ++m)
#pragma unroll
        for (int n = 0; n < 4; ++n) { PINA(acc[m][n]); st16(rs, lane16, dst + m * 16384 + n * 2048, pack8(acc[m][n], 0)); st16(rs, lane16 + 1024, dst + m * 16384 + n * 2048, pack8(acc[m][n], 1)); SBAR(); }
    } else {
      k1_gemm<false, 4>(acc, st, xp, wp, xq, wq_, 16, wid, lane);
      STAMPIT(3);
      const unsigned dst = (unsigned)WS_VB + ((head2 * 64 + tb0) * 16 + wc * 4) * 1024;
#pragma unroll
      for (int m = 0; m < 4; ++m)
#pragma unroll
        for (int n = 0; n < 4; ++n) { PINA(acc[m][n]); st16(rs, lane16 + n * 1024, dst + m * 16384, pack8(acc[m][n], 0)); st16(rs, lane16 + n * 1024, dst + m * 16384 + 8192, pack8(acc[m][n], 1)); SBAR(); }
    }
  }
  STAMPIT(4);
  {
    const unsigned gw = bid * 4 + wid, hd = gw >> 6;
    unsigned short v[4][8];
#pragma unroll
    for (int i = 0; i < 4; ++i) { int f = (gw & 63) * 4 + i; f = f < 252 ? f : 251;
      const unsigned nb = wf_nb(f), ks = wf_ks(f); const bool isa = wf_isa(f);
      const char* src = (isa ? wo_a + hd * 28 * 896 : wo_b + hd * 256 * 896) + (32 * nb + r) * 2;
#pragma unroll
      for (int j = 0; j < 8; ++j) { unsigned d = 16 * ks + 8 * (j >> 2) + 4 * hh + (j & 3); if (isa && d >= 28) d = 0; v[i][j] = *(const unsigned short*)(src + d * 896); } }
    if (cb < 31) {
      f32x16 acc[4][3];
      if (cb < 28) {
        k1_gemm<true, 3>(acc, st, xp, wp, none, none, 7, wid, lane);
        STAMPIT(5);
#pragma unroll
        for (int n = 0; n < 3; ++n) { const unsigned c32 = (cb < 22 ? cb : cb - 22) * 6 + wc * 3 + n;
          if (c32 < (cb < 22 ? 128u : 32u)) {
            unsigned dst, mst; float sc;
            if (cb < 22) { dst = (unsigned)WS_QB + (((c32 >> 3) * 64 + tb0) * 16 + (c32 & 7) * 2) * 1024; mst = 16384; sc = QSCALE_B; }
            else if (c32 < 16) { dst = (unsigned)WS_QA + (c32 * 64 + tb0) * 2048; mst = 2048; sc = QSCALE_A; }
            else { dst = (unsigned)WS_KVA + ((c32 - 16) * 64 + tb0) * 4096; mst = 4096; sc = 1.f; }
            const bool pad = cb >= 22 && hh;
#pragma unroll
            for (int m = 0; m < 4; ++m) { PINA(acc[m][n]); u32x4 w1 = __builtin_bit_cast(u32x4, pack8(acc[m][n], 1, sc)); if (pad) { w1[2] = 0u; w1[3] = 0u; }
              st16(rs, lane16, dst + m * mst, pack8(acc[m][n], 0, sc)); st16(rs, lane16 + 1024, dst + m * mst, __builtin_bit_cast(bf16x8, w1)); SBAR(); } } }
      } else {
        k1_gemm<false, 3>(acc, st, xp, wp, none, none, 7, wid, lane);
        STAMPIT(5);
#pragma unroll
        for (int n = 0; n < 3; ++n) { const unsigned c32 = (cb - 28) * 6 + wc * 3 + n;
          if (c32 < 16) { const unsigned dst = (unsigned)WS_KVA + (c32 * 64 + tb0) * 4096 + 2048;
            const unsigned fill = r == 28 ? 0x3f803f80u : 0u;
#pragma unroll
            for (int m = 0; m < 4; ++m) { PINA(acc[m][n]);
#pragma unroll
              for (int s = 0; s < 2; ++s) { u32x4 w = __builtin_bit_cast(u32x4, pack8(acc[m][n], s)); if (r >= 28) w = (u32x4){fill, fill, fill, fill};
                st16(rs, lane16 + s * 1024, dst + m * 4096, __builtin_bit_cast(bf16x8, w)); }
              SBAR(); } } }
      }
    } else {
      const unsigned row = rb * 256 + tid;
      const u32x4* xr_ = (const u32x4*)(x + row * 896); const u32x4* gw_ = (const u32x4*)gate_w;
      float s = 0.f;
#pragma unroll 14
      for (int i = 0; i < 56; ++i) { const u32x4 a = xr_[i], b = gw_[i];
#pragma unroll
        for (int e = 0; e < 4; ++e) { s = fmaf(bflo(a[e]), bflo(b[e]), s); s = fmaf(bfhi(a[e]), bfhi(b[e]), s); } }
      ((float*)(ws + WS_G))[row] = 1.f / (1.f + __expf(-s));
#pragma unroll
      for (int i = 0; i < 8; ++i) *(u32x4*)(ws + WS_ML + rb * 32768 + i * 4096 + tid * 16) = (u32x4){0u, 0u, 0u, 0u};
    }
#pragma unroll
    for (int i = 0; i < 4; ++i) { u32x4 w;
#pragma unroll
      for (int e = 0; e < 4; ++e) w[e] = (unsigned)v[i][2 * e] | ((unsigned)v[i][2 * e + 1] << 16);
      *(u32x4*)(ws + WS_WOF + (size_t)(gw * 4 + i) * 1024 + lane * 16) = w; }
  }
  STAMPIT(6);
  asm volatile("s_waitcnt vmcnt(0)" ::: "memory");
  STAMPIT(7); STAMPRT(8); STAMP_OUT(0, 9);
}

struct Desc { unsigned so, m0; };
struct Stream {
  u32x4 srd; unsigned tab, lane16;
  unsigned dseq, dslot, rslot, so;
  Desc y;
};
DI unsigned nslot(unsigned s) { return s >= 131072u ? s - 131072u : s + 32768u; }
DI void st_prep(Stream& S, Desc& D) { D.so = __builtin_amdgcn_readlane(S.tab, S.dseq); PINS(D.so); D.m0 = S.dslot; S.dslot = nslot(S.dslot); S.dseq += 1; PINS(S.dslot); }
template <int Q> DI void st_pre(Stream& S, const Desc& D) {
  if constexpr (Q == 0) { set_m0(D.m0); S.so = D.so; } else if constexpr (Q == 4) { set_m0(D.m0 + 4096u); S.so = D.so + 4096u; PINS(S.so); }
}
template <int Q> DI void st_go(Stream& S) { dma16<(Q & 3) * 1024, true>(S.lane16, S.srd, S.so); }
DI void st_item(Stream& S) {
  sfor<8>([&](auto Q) __attribute__((always_inline)) { st_pre<decltype(Q)::v>(S, S.y); if constexpr (!(decltype(Q)::v & 3)) NOP0(); st_go<decltype(Q)::v>(S); });
  st_prep(S, S.y); asm volatile("s_nop 4" ::: "memory");
}
DI float maskv(float v, int dq, int rr) { return dq >= ((rr & 3) + 8 * (rr >> 2)) ? v : -__builtin_inff(); }
DI float max16(const f32x16& s) {
  float a = fmaxf(fmaxf(s[0], s[1]), s[2]), b = fmaxf(fmaxf(s[3], s[4]), s[5]);
  a = fmaxf(fmaxf(a, s[6]), s[7]); b = fmaxf(fmaxf(b, s[8]), s[9]); a = fmaxf(fmaxf(a, s[10]), s[11]); b = fmaxf(fmaxf(b, s[12]), s[13]);
  a = fmaxf(fmaxf(a, s[14]), s[15]); a = fmaxf(a, b);
  return fmaxf(a, swap_other(a));
}
DI float max3f(float a, float b, float c) { float r; asm("v_max3_f32 %0, %1, %2, %3" : "=v"(r) : "v"(a), "v"(b), "v"(c)); return r; }
DI f32x16 splat16(float v) { f32x16 r; for (int i = 0; i < 16; ++i) r[i] = v; return r; }
DI void open_item(Stream& S, unsigned& rb_, unsigned off) { rb_ = S.rslot + off; PIN(rb_); S.rslot = nslot(S.rslot); PINS(S.rslot); }
DI void unopen_item(Stream& S) { S.rslot = S.rslot == 0u ? 131072u : S.rslot - 32768u; }
template <int N> DI void wait_lgkm() { __builtin_amdgcn_s_waitcnt(15 | (3 << 14) | (7 << 4) | (N << 8)); }
DI void set_c(f32x16& c0, f32x16& c1, float v, int dq) {
#pragma unroll
  for (int e = 0; e < 16; ++e) { c0[e] = maskv(v, dq, e); c1[e] = maskv(v, dq - 32, e); }
  PIN(c0); PIN(c1); asm volatile("s_nop 1" ::: "memory");
}

template <bool FIRST>
DI void b_tile(f32x16 (&o)[8], const bf16x8 (&q)[16], bf16x8 (&fr)[8], f32x16& c0, f32x16& c1, float& mref, float& rs0, float& rs1, Stream& S, unsigned& rb_) {
  f32x16 s0, s1; u32x4 p[4]; float pe[32], ma, mb_; Desc X;
  constexpr int E0 = FIRST ? 22 : 18;
  sfor<64>([&](auto J) __attribute__((always_inline)) { constexpr int j = decltype(J)::v;
    if constexpr (j == 24) { wait_bar<16, false>(); open_item(S, rb_, S.lane16); }
    if constexpr (j == 56) { wait_bar<11, false>(); open_item(S, rb_, S.lane16); }
    if constexpr ((j & 3) == 0) wait_lgkm<4>();
    if constexpr (j == 0) mfma_vc(s0, fr[0], q[0], c0);
    else if constexpr (j < 16) mfma_v(s0, fr[j & 7], q[j]);
    else if constexpr (j == 16) mfma_vc(s1, fr[0], q[0], c1);
    else if constexpr (j < 32) mfma_v(s1, fr[j & 7], q[j - 16]);
    else if constexpr (FIRST && j < 40) asm volatile("v_mfma_f32_32x32x16_bf16 %0, %1, %2, 0" : "=&a"(o[j & 7]) : "v"(fr[j & 7]), "v"(p[0]));
    else mfma_a(o[j & 7], fr[j & 7], __builtin_bit_cast(bf16x8, p[(j - 32) >> 3]));
    fr[j & 7] = ldsr(rb_ + ((j + 8) & 31) * 1024);
    if constexpr (j < 16 && (j & 1)) st_go<(j >> 1)>(S);
    if constexpr (j == 51 || j == 53 || j == 55 || j == 57 || j == 59 || j == 61) st_go<((j - 51) >> 1)>(S);
    if constexpr (j == 62) st_go<6>(S);
    if constexpr (j == 63) st_go<7>(S);
    if constexpr (FIRST) {
      if constexpr (j == 18) { ma = max3f(s0[0], s0[1], s0[2]); mb_ = max3f(s0[3], s0[4], s0[5]); ma = max3f(ma, s0[6], s0[7]); mb_ = max3f(mb_, s0[8], s0[9]); PIN(ma); PIN(mb_); }
      if constexpr (j == 19) { ma = max3f(ma, s0[10], s0[11]); mb_ = max3f(mb_, s0[12], s0[13]); ma = max3f(ma, s0[14], s0[15]); ma = fmaxf(ma, mb_); PIN(ma); }
      if constexpr (j == 20) { ma = fmaxf(ma, swap_other(ma)); mref = ma == -__builtin_inff() ? 0.f : ma; PIN(mref); }
      if constexpr (j >= 56) { constexpr int e = 2 * (j - 56); float v = -mref; c0[e] = v; c0[e + 1] = v; c1[e] = v; c1[e + 1] = v; if constexpr (j == 63) { PIN(c0); PIN(c1); } }
    }
    if constexpr (j >= E0 && j < E0 + 32) { constexpr int e = j - E0;
      float v = e < 16 ? s0[e & 15] : s1[e & 15]; if constexpr (FIRST) v -= mref; v = __builtin_amdgcn_exp2f(v); PIN(v); pe[e] = v; }
    if constexpr (j >= E0 + 1 && j < E0 + 33) { constexpr int e = j - E0 - 1;
      if constexpr (e & 1) { rs1 += pe[e]; PIN(rs1); unsigned w = cvtpk(pe[e - 1], pe[e]); PIN(w); p[e >> 3][(e >> 1) & 3] = w; }
      else { rs0 += pe[e]; PIN(rs0); } }
    if constexpr (j == 0) st_pre<0>(S, S.y);
    if constexpr (j == 8) st_pre<4>(S, S.y);
    if constexpr (j == 16) st_prep(S, X);
    if constexpr (j == 17) st_prep(S, S.y);
    if constexpr (j == 50) st_pre<0>(S, X);
    if constexpr (j == 58) st_pre<4>(S, X);
    SBAR();
  });
}

DI void hand_step(GAS unsigned long long* ml, const int left) {
  if (left == 2) {
    int lw; asm volatile("v_accvgpr_read_b32 %0, a255" : "=v"(lw));
    if (!__all(lw < 0))
      for (unsigned spins = 0; spins < (1u << 22); ++spins) {
        __builtin_amdgcn_s_sleep(1);
        asm volatile("global_load_dwordx2 a[254:255], %1, off sc1\n\ts_waitcnt vmcnt(0)\n\tv_accvgpr_read_b32 %0, a255" : "=v"(lw) : "v"(ml) : "memory", "a254", "a255");
        if (__all(lw < 0)) break;
      }
  }
  if (left == 3) asm volatile("global_load_dwordx2 a[254:255], %0, off sc1" ::"v"(ml) : "memory", "a254", "a255");
}

template <bool HAND>
DI void b_segment(f32x16 (&o)[8], const bf16x8 (&q)[16], float& mref, float& lsum, Stream& S, const int qb, const int t0, const int t1,
                  const unsigned lane, const unsigned wid, GAS unsigned long long* ml, const bool consume, float& mb, float& lb TR_ARG) {
  TRC();
  const int qrow = qb * 128 + (int)wid * 32 + (int)(lane & 31) - 4 * (int)(lane >> 5);
  unsigned rb_; open_item(S, rb_, S.lane16);
  bf16x8 fr[8];
#pragma unroll
  for (int j = 0; j < 8; ++j) fr[j] = ldsr(rb_ + j * 1024);
  f32x16 c0, c1; float rs0 = 0.f, rs1 = 0.f;
  set_c(c0, c1, 0.f, qrow - t0 * 64);
  SBAR();
  b_tile<true>(o, q, fr, c0, c1, mref, rs0, rs1, S, rb_);
  const int tc = consume ? t1 : -8;
  for (int t = t0 + 1; t < t1; ++t) {
    TRC();
    if (HAND) hand_step(ml, tc - t);
    if (t >= 2 * qb) set_c(c0, c1, -mref, qrow - t * 64);
    b_tile<false>(o, q, fr, c0, c1, mref, rs0, rs1, S, rb_);
  }
  SBAR();
  if (HAND) asm volatile("v_accvgpr_read_b32 %0, a254\n\tv_accvgpr_read_b32 %1, a255" : "=v"(mb), "=v"(lb));
  MFMA_DRAIN_O(o); SBAR(); TRC();
  unopen_item(S);
  const float l = rs0 + rs1; lsum = l + swap_other(l);
}

DI void a_finish(bf16x8 (&ca)[2], f32x16 o, const float g, const unsigned lane) {
  const bool up = lane >= 32;
  const float lo = swap_other(o[12]), l = up ? o[12] : lo;
  const float sc = g / l + l * 0.f;
#pragma unroll
  for (int e = 12; e < 16; ++e) o[e] = up ? 0.f : o[e];
  ca[0] = pack8(o, 0, sc); ca[1] = pack8(o, 1, sc);
}
DI void a_phase(bf16x8 (&ca)[2], Stream& S, const bf16x8 q0, const bf16x8 q1, const float g, const unsigned lane, GAS unsigned long long* ml, const bool produce, const unsigned long long mlv TR_ARG) {
  TRC();
  unsigned rb_; open_item(S, rb_, S.lane16);
  bf16x8 kf[2][2], va[2], vb[2];
  kf[0][0] = ldsr(rb_); kf[0][1] = ldsr(rb_ + 1024); kf[1][0] = ldsr(rb_ + 4096); kf[1][1] = ldsr(rb_ + 5120);
  va[0] = ldsr(rb_ + 2048); vb[0] = ldsr(rb_ + 3072); va[1] = ldsr(rb_ + 4096 + 2048); vb[1] = vb[0];
  f32x16 s[2], o = {}, negm;
  { f32x16 z = {}; z = MFMA(kf[0][0], q0, z); z = MFMA(kf[0][1], q1, z); negm = splat16(-max16(z)); PIN(negm); }
  u32x4 p0, p1 = {0u, 0u, 0u, 0u}; float pt[4] = {0.f, 0.f, 0.f, 0.f};
  PIN(o); asm volatile("s_nop 1" ::: "memory");
  mfma_vc(s[0], kf[0][0], q0, negm); mfma_v(s[0], kf[0][1], q1);
  kf[0][0] = ldsr(rb_ + 8192); kf[0][1] = ldsr(rb_ + 9216);
  MFMA_DRAIN_V(s[0]);
  SBAR();
  for (int it = 0; it < 8; ++it) {
    TRC();
    unsigned rn_ = rb_;
    sfor<8>([&](auto KB) __attribute__((always_inline)) { constexpr int kb = decltype(KB)::v, c = kb & 1, n = c ^ 1;
#define AADDR(blk, off) ((blk) < 8 ? rb_ + (blk) * 4096 + (off) : rn_ + ((blk) - 8) * 4096 + (off))
#define AEXP(e) do { pe[e] = __builtin_amdgcn_exp2f(s[c][e]); PIN(pe[e]); } while (0)
#define ACVT(P, i, a, b) do { unsigned w_ = cvtpk(a, b); PIN(w_); P[i] = w_; } while (0)
      if constexpr (kb == 5) { wait_bar<16, false>(); open_item(S, rn_, S.lane16); }
      float pe[16];
      wait_lgkm<4>();
      mfma_vc(s[n], kf[n][0], q0, negm);
      kf[n][0] = ldsr(AADDR(kb + 3, 0)); SBAR();
      AEXP(0); AEXP(1); ACVT(p1, 2, pt[0], pt[1]); AEXP(2); AEXP(3); ACVT(p1, 3, pt[2], pt[3]);
      if constexpr (kb == 0) st_pre<0>(S, S.y);
      if constexpr (kb == 2) st_pre<4>(S, S.y);
      SBAR();
      mfma_v(s[n], kf[n][1], q1);
      kf[n][1] = ldsr(AADDR(kb + 3, 1024));
      if constexpr (kb < 4) st_go<2 * kb>(S);
      SBAR();
      AEXP(4); AEXP(5); ACVT(p0, 0, pe[0], pe[1]); AEXP(6); AEXP(7); ACVT(p0, 1, pe[2], pe[3]);
      SBAR();
      mfma_v(o, vb[n], __builtin_bit_cast(bf16x8, p1));
      vb[n] = ldsr(AADDR(kb + 1, 3072)); SBAR();
      AEXP(8); ACVT(p0, 2, pe[4], pe[5]); AEXP(9); ACVT(p0, 3, pe[6], pe[7]); SBAR(); AEXP(10); AEXP(11);
      SBAR();
      mfma_v(o, va[c], __builtin_bit_cast(bf16x8, p0));
      va[c] = ldsr(AADDR(kb + 2, 2048));
      if constexpr (kb < 4) st_go<2 * kb + 1>(S);
      SBAR();
      AEXP(12); AEXP(13); ACVT(p1, 0, pe[8], pe[9]); AEXP(14); AEXP(15); ACVT(p1, 1, pe[10], pe[11]);
      pt[0] = pe[12]; pt[1] = pe[13]; pt[2] = pe[14]; pt[3] = pe[15];
      if constexpr (kb == 4) st_prep(S, S.y);
      if constexpr (kb == 7) { if (it == 0 && produce) {
          asm volatile("s_waitcnt vmcnt(0)" ::: "memory");
          __hip_atomic_store(ml, mlv, __ATOMIC_RELAXED, __HIP_MEMORY_SCOPE_AGENT); } }
      SBAR();
#undef AADDR
#undef AEXP
#undef ACVT
    });
    rb_ = rn_;
  }
  p1[2] = cvtpk(pt[0], pt[1]); p1[3] = cvtpk(pt[2], pt[3]);
  mfma_vn(o, vb[1], __builtin_bit_cast(bf16x8, p1));
  MFMA_DRAIN_V(o);
  unopen_item(S);
  a_finish(ca, o, g, lane);
}

template <bool MERGE>
DI float out_proj(f32x16 (&o)[8], const bf16x8 (&ca)[2], const float fa, const float fb, Stream& S, char* og, const unsigned wid TR_ARG) {
  u32x4 pb[16], cb[16], ow; bf16x8 fr[8]; f32x16 acc[14]; unsigned rb_;
  if constexpr (MERGE) {
    open_item(S, rb_, wid * 8192 + S.lane16);
#pragma unroll
    for (int c = 0; c < 8; ++c) pb[c] = __builtin_bit_cast(u32x4, ldsr(rb_ + c * 1024));
    wait_bar<8, false>(); open_item(S, rb_, wid * 8192 + S.lane16);
#pragma unroll
    for (int c = 0; c < 8; ++c) pb[8 + c] = __builtin_bit_cast(u32x4, ldsr(rb_ + c * 1024));
    wait_bar<0, false>();
  }
  open_item(S, rb_, S.lane16);
#pragma unroll
  for (int j = 0; j < 8; ++j) fr[j] = ldsr(rb_ + j * 1024);
  SBAR();
  Desc X;
  sfor<252>([&](auto F) __attribute__((always_inline)) { constexpr int f = decltype(F)::v, f8 = f + 8, w = f >> 5, ph = f & 31;
    constexpr int nb = wf_nb(f), ks = wf_ks(f);
    if constexpr (ph == 24 && w < 7) { TRC();
      wait_bar<(MERGE && w == 0) ? 4 : (MERGE && w == 1) ? 12 : w < 5 ? 16 : 8, false>(); open_item(S, rb_, S.lane16); }
    if constexpr ((f & 3) == 0) wait_lgkm<4>();
    if constexpr (wf_isa(f)) { if constexpr (ks == 0) { const f32x16 z = {}; acc[nb] = MFMA(fr[f & 7], ca[0], z); } else acc[nb] = MFMA(fr[f & 7], ca[1], acc[nb]); }
    else acc[nb] = MFMA(fr[f & 7], __builtin_bit_cast(bf16x8, cb[ks]), acc[nb]);
    if constexpr (f8 < 252) fr[f & 7] = ldsr(rb_ + (f8 & 31) * 1024);
    constexpr bool r1 = MERGE ? w < 5 : w < 5, r2 = MERGE && w < 2;
    if constexpr (r1 && ph < 16 && (ph & 1)) st_go<(ph >> 1)>(S);
    if constexpr (r2 && ph >= 16 && (ph & 1)) st_go<((ph - 16) >> 1)>(S);
    if constexpr (r1 && (ph == 0 || ph == 8)) st_pre<ph / 2>(S, S.y);
    if constexpr (r2 && ph == 2) st_prep(S, X);
    if constexpr (r2 && (ph == 16 || ph == 24)) st_pre<(ph - 16) / 2>(S, X);
    if constexpr (r1 && ph == 18) st_prep(S, S.y);
    if constexpr (f < 96 && f % 6 < 4) { constexpr int c = f / 6, e = f % 6; const float x0 = ard(o[c >> 1][8 * (c & 1) + 2 * e]), x1 = ard(o[c >> 1][8 * (c & 1) + 2 * e + 1]);
      unsigned v = MERGE ? cvtpk(fmaf(x0, fa, bflo(pb[c][e]) * fb), fmaf(x1, fa, bfhi(pb[c][e]) * fb)) : cvtpk(x0 * fa, x1 * fa); PIN(v); cb[c][e] = v; }
    if constexpr (f >= 128 && f < 245 && (f - 128) % 9 < 8) { constexpr int t = (f - 128) / 9, u = (f - 128) % 9;
      unsigned v = cvtpk(ard(acc[t][2 * u]), ard(acc[t][2 * u + 1])); PIN(v); ow[u & 3] = v;
      if constexpr ((u & 3) == 3) *(u32x4*)(og + (t * 2 + (u >> 2)) * 1024) = ow; }
    SBAR();
  });
  *(bf16x8*)(og + 26 * 1024) = pack8(acc[13], 0); *(bf16x8*)(og + 27 * 1024) = pack8(acc[13], 1);
  return acc[13][0];
}

DI void safe_redo(const char* ws, char* og, const unsigned head, const int qb, const unsigned tb, const float g, const unsigned lane) {
  const unsigned lane16 = lane * 16;
  bf16x8 ca[2], cb[16];
  {
    const char* qa = ws + WS_QA + (size_t)(head * 64 + tb) * 2048 + lane16; const bf16x8 q0 = *(const bf16x8*)qa, q1 = *(const bf16x8*)(qa + 1024);
    float m = -1e30f; f32x16 o = splat16(opaque0());
    for (int kb = 0; kb < 64; ++kb) {
      const char* p = ws + WS_KVA + (size_t)(head * 64 + kb) * 4096 + lane16;
      f32x16 t = {}; t = MFMA(*(const bf16x8*)p, q0, t); t = MFMA(*(const bf16x8*)(p + 1024), q1, t);
      const float mn = fmaxf(m, max16(t)), al = __builtin_amdgcn_exp2f(m - mn); m = mn;
#pragma unroll
      for (int e = 0; e < 16; ++e) t[e] = __builtin_amdgcn_exp2f(t[e] - mn);
      o *= al; o = MFMA(*(const bf16x8*)(p + 2048), pack8(t, 0), o); o = MFMA(*(const bf16x8*)(p + 3072), pack8(t, 1), o);
    }
    a_finish(ca, o, g, lane);
  }
  {
    const char* qg = ws + WS_QB + (size_t)((head * 64 + tb) * 16) * 1024 + lane16;
    const int qrow = (int)tb * 32 + (int)(lane & 31) - 4 * (int)(lane >> 5);
    float m = -1e30f, ls = 0.f; f32x16 o[8];
#pragma unroll
    for (int d = 0; d < 8; ++d) o[d] = splat16(opaque0());
    for (int kb = 0; kb <= (int)tb; ++kb) {
      const char* kp = ws + WS_KB + (size_t)(head * 64 + kb) * 16384 + lane16; const char* vp = ws + WS_VB + (size_t)(head * 64 + kb) * 16384 + lane16;
      f32x16 s = {};
#pragma unroll
      for (int j = 0; j < 16; ++j) s = MFMA(*(const bf16x8*)(kp + j * 1024), *(const bf16x8*)(qg + j * 1024), s);
#pragma unroll
      for (int e = 0; e < 16; ++e) s[e] = maskv(s[e], qrow - kb * 32, e);
      const float mn = fmaxf(m, max16(s)), al = __builtin_amdgcn_exp2f(m - mn); m = mn; ls *= al;
#pragma unroll
      for (int e = 0; e < 16; ++e) { s[e] = __builtin_amdgcn_exp2f(s[e] - mn); ls += s[e]; }
      const bf16x8 p0 = pack8(s, 0), p1 = pack8(s, 1);
#pragma unroll
      for (int d = 0; d < 8; ++d) { o[d] *= al; o[d] = MFMA(*(const bf16x8*)(vp + d * 1024), p0, o[d]); o[d] = MFMA(*(const bf16x8*)(vp + 8192 + d * 1024), p1, o[d]); }
    }
    const float inv = (1.f - g) / (ls + swap_other(ls));
#pragma unroll
    for (int c = 0; c < 16; ++c) cb[c] = pack8(o[c >> 1], c & 1, inv);
  }
  const char* wf = ws + WS_WOF + (size_t)head * 262144 + lane16;
  asm volatile("s_waitcnt vmcnt(0)" ::: "memory");
  for (int nb = 0; nb < 14; ++nb) {
    const int fa = nb < 7 ? 2 * nb : 126 + 18 * (nb - 7);
    f32x16 a = {}; a = MFMA(*(const bf16x8*)(wf + fa * 1024), ca[0], a); a = MFMA(*(const bf16x8*)(wf + (fa + 1) * 1024), ca[1], a);
#pragma unroll
    for (int ks = 0; ks < 16; ++ks) a = MFMA(*(const bf16x8*)(wf + (nb < 7 ? 14 + 7 * ks + nb : fa + 2 + ks) * 1024), cb[ks], a);
    *(bf16x8*)(og + (2 * nb) * 1024) = pack8(a, 0); *(bf16x8*)(og + (2 * nb + 1) * 1024) = pack8(a, 1);
  }
}

__global__ __launch_bounds__(256, 1) void k2_attn(char* __restrict__ ws) {
  extern __shared__ __attribute__((aligned(16))) char lds[];
  const unsigned tid = threadIdx.x, lane = tid & 63, wid = rfl(tid >> 6), lane16 = lane * 16;
  const unsigned bid = blockIdx.x, head = 2 * (bid & 7) + ((bid >> 3) & 1), pi = bid >> 5, roleb = (bid >> 4) & 1;
  const int qlong = 15 - (int)pi, nt1 = 32 - 2 * (int)pi - TA;
  const int qb = roleb ? (int)pi : qlong, nt2 = roleb ? 2 * (int)pi + 2 : TA;
  const unsigned pw = (head * 8 + pi) * 4 + wid;
  STAMP_DECL STAMPRT(0); STAMPIT(1); TR_DECL TRC();
  Stream S;
  {
    const unsigned n0 = roleb ? 2 * nt1 : 0, n1 = n0 + 8, n2 = n1 + 2 * nt2, n3 = n2 + (roleb ? 0 : 2), j = lane;
    const unsigned jb = j < n0 ? j + 2 * TA : j - n1, kv = (unsigned)WS_KB + head * (unsigned)MB + (jb & 1) * (unsigned)(WS_VB - WS_KB) + (jb >> 1) * 32768;
    unsigned a = (j < n0 || j >= n1) ? kv : (unsigned)WS_KVA + head * 262144 + (j - n0) * 32768;
    a = j >= n3 ? (unsigned)WS_WOF + head * 262144 + (j - n3) * 32768 : a;
    a += wid * 8192;
    a = (j >= n2 && j < n3) ? (unsigned)WS_PO + pw * 16384 + (j - n2) * 8192 : a;
    S.tab = j < n3 + 8 ? a : 0x7ffffff0u; S.srd = mksrd(ws, (unsigned)WS_END); S.lane16 = lane16;
    S.dseq = 0; S.dslot = wid * 8192; S.rslot = 0;
  }
  st_prep(S, S.y); asm volatile("s_nop 4" ::: "memory");
  st_item(S);
  const unsigned tb = qb * 4 + wid;
  bf16x8 q[16];
  if (roleb) {
    const char* qg = ws + WS_QB + (size_t)((head * 64 + qlong * 4 + wid) * 16) * 1024 + lane16;
#pragma unroll
    for (int j = 0; j < 16; ++j) q[j] = *(const bf16x8*)(qg + j * 1024);
  }
  const bf16x8 qa0 = *(const bf16x8*)(ws + WS_QA + (size_t)(head * 64 + tb) * 2048 + lane16), qa1 = *(const bf16x8*)(ws + WS_QA + (size_t)(head * 64 + tb) * 2048 + 1024 + lane16);
  const float g = ((const float*)(ws + WS_G))[tb * 32 + (lane & 31)];
  SBAR();
  st_item(S); st_item(S);
  GAS unsigned long long* ml = (GAS unsigned long long*)(ws + WS_ML + (size_t)pw * 512 + lane * 8); unsigned long long mlv = 0ull;
  f32x16 o[8]; float mref, lsum, mb, lb;
  wait_bar<16, false>();
  STAMPIT(2);
  if (roleb) {
    b_segment<false>(o, q, mref, lsum, S, qlong, TA, TA + nt1, lane, wid, ml, false, mb, lb TR_PASS);
    STAMPIT(3);
    const float inv = lsum > 0.f ? 1.f / lsum : 0.f;
    const __amdgpu_buffer_rsrc_t rs = __builtin_amdgcn_make_buffer_rsrc(ws + WS_PO + (size_t)pw * 16384, 0, 16384, 0x20000);
#pragma unroll
    for (int c = 0; c < 16; ++c) { if (!(c & 1)) PINA(o[c >> 1]); __builtin_amdgcn_raw_buffer_store_b128(__builtin_bit_cast(u32x4, pack8(o[c >> 1], c & 1, inv)), rs, lane16 + c * 1024, 0, 16); SBAR(); }
    mlv = ((unsigned long long)(__float_as_uint(lsum) | 0x80000000u) << 32) | __float_as_uint(mref);
  }
  STAMPIT(4);
  {
    const char* qg = ws + WS_QB + (size_t)((head * 64 + tb) * 16) * 1024 + lane16;
#pragma unroll
    for (int j = 0; j < 16; ++j) q[j] = *(const bf16x8*)(qg + j * 1024);
  }
  bf16x8 ca[2];
  a_phase(ca, S, qa0, qa1, g, lane, ml, roleb, mlv TR_PASS);
  STAMPIT(5);
  b_segment<true>(o, q, mref, lsum, S, qb, 0, nt2, lane, wid, ml, !roleb, mb, lb TR_PASS);
  STAMPIT(6); STAMPIT(7);
  char* og = ws + WS_OP + (size_t)((head * 64 + tb) * 28) * 1024 + lane16;
  float chk;
  if (!roleb) {
    lb = fabsf(lb);
    const float m = lb > 0.f ? fmaxf(mref, mb) : mref, ea = __builtin_amdgcn_exp2f(mref - m), wb = lb > 0.f ? lb * __builtin_amdgcn_exp2f(mb - m) : 0.f;
    const float den = lsum * ea + wb, inv = (1.f - g) / den;
    chk = den + out_proj<true>(o, ca, ea * inv, wb * inv, S, og, wid TR_PASS);
  } else chk = lsum + out_proj<false>(o, ca, (1.f - g) / lsum, 0.f, S, og, wid TR_PASS);
  STAMPIT(8);
  if (__builtin_expect(__any(!(chk * 0.f == 0.f)), 0)) safe_redo(ws, og, head, qb, tb, g, lane);
  asm volatile("s_waitcnt vmcnt(0)" ::: "memory");
  STAMPIT(9); STAMPRT(10); STAMP_OUT(1, 11); TRC(); TR_OUT();
}

__global__ __launch_bounds__(256) void k3_sum(const char* __restrict__ ws, char* __restrict__ out) {
  const unsigned c = blockIdx.x * 256 + threadIdx.x, lane = c & 63, f = c >> 6, tb = f / 28, rem = f - tb * 28;
  const char* p = ws + WS_OP + (size_t)c * 16;
  u32x4 v[16];
#pragma unroll
  for (int h = 0; h < 16; ++h) v[h] = *(const u32x4*)(p + (size_t)h * (64 * 28 * 1024));
  float a[8] = {};
#pragma unroll
  for (int h = 0; h < 16; ++h)
#pragma unroll
    for (int e = 0; e < 4; ++e) { a[2 * e] += bflo(v[h][e]); a[2 * e + 1] += bfhi(v[h][e]); }
  char* o = out + (size_t)(tb * 32 + (lane & 31)) * 896 + (rem * 16 + 4 * (lane >> 5)) * 2;
  *(u32x2*)o = (u32x2){cvtpk(a[0], a[1]), cvtpk(a[2], a[3])};
  *(u32x2*)(o + 16) = (u32x2){cvtpk(a[4], a[5]), cvtpk(a[6], a[7])};
}

extern "C" void kernel_launch(void* const* d_in, const int* in_sizes, int n_in, void* d_out, int out_size, void* d_ws, size_t ws_size, hipStream_t stream) {
  (void)in_sizes; (void)n_in; (void)out_size; (void)ws_size;
  (void)hipFuncSetAttribute((const void*)k1_proj, hipFuncAttributeMaxDynamicSharedMemorySize, 163840);
  (void)hipFuncSetAttribute((const void*)k2_attn, hipFuncAttributeMaxDynamicSharedMemorySize, 163840);
  char* ws = (char*)d_ws;
  k1_proj<<<256, 256, 163840, stream>>>((const char*)d_in[0], (const char*)d_in[1], (const char*)d_in[4], (const char*)d_in[5], (const char*)d_in[6], (const char*)d_in[7], (const char*)d_in[9], (const char*)d_in[2], (const char*)d_in[8], ws);
  k2_attn<<<256, 256, 163840, stream>>>(ws);
  k3_sum<<<448, 256, 0, stream>>>(ws, (char*)d_out);
}
```

```cpp
#include <hip/hip_runtime.h>
#include <stdint.h>
#include <utility>

typedef short bf16x8 __attribute__((ext_vector_type(8)));
typedef short s16x4 __attribute__((ext_vector_type(4)));
typedef float f32x16 __attribute__((ext_vector_type(16)));
typedef float f32x2 __attribute__((ext_vector_type(2)));
typedef __bf16 bf16x2_t __attribute__((ext_vector_type(2)));
typedef unsigned u32x4 __attribute__((ext_vector_type(4)));
typedef unsigned u32x2 __attribute__((ext_vector_type(2)));
#define LAS __attribute__((address_space(3)))
#define GAS __attribute__((address_space(1)))
#define DI __device__ __forceinline__
#define SBAR() __builtin_amdgcn_sched_barrier(0)
#define MFMA(a, b, c) __builtin_amdgcn_mfma_f32_32x32x16_bf16((a), (b), (c), 0, 0, 0)
#define PIN(x) asm volatile("" : "+v"(x))

constexpr size_t MB = 1u << 20;
constexpr size_t WS_QB = 0, WS_KB = 16 * MB, WS_VB = 32 * MB, WS_QA = 48 * MB, WS_KVA = 50 * MB, WS_G = 54 * MB, WS_W0 = 54 * MB + 65536, WS_F1 = WS_W0 + 4096, WS_F2 = WS_W0 + 8192, WS_F3 = WS_W0 + 16384, WS_FH = WS_W0 + 32768,
                 WS_ML = 55 * MB, WS_PO = 56 * MB, WS_OP = 64 * MB, WS_WOF = 92 * MB, WS_END = 96 * MB;
constexpr int TA = 17;
constexpr float LOG2E = 1.4426950408889634f;
constexpr float QSCALE_B = 0.0625f * LOG2E;
constexpr float QSCALE_A = 0.18898223650461362f * LOG2E;

template <int I> struct IC { static constexpr int v = I; };
template <class F, int... Is> DI void sfor_(F&& f, std::integer_sequence<int, Is...>) { (f(IC<Is>{}), ...); }
template <int N, class F> DI void sfor(F&& f) { sfor_(f, std::make_integer_sequence<int, N>{}); }
DI void mfma_vc(f32x16& d, const bf16x8& a, const bf16x8& b, const f32x16& c) { asm volatile("v_mfma_f32_32x32x16_bf16 %0, %1, %2, %3" : "=&v"(d) : "v"(a), "v"(b), "v"(c)); }
DI void mfma_v(f32x16& d, const bf16x8& a, const bf16x8& b) { asm volatile("v_mfma_f32_32x32x16_bf16 %0, %1, %2, %0" : "+v"(d) : "v"(a), "v"(b)); }
DI void mfma_vn(f32x16& d, const bf16x8& a, const bf16x8& b) { asm volatile("s_nop 1\n\tv_mfma_f32_32x32x16_bf16 %0, %1, %2, %0" : "+v"(d) : "v"(a), "v"(b)); }
DI void mfma_a(f32x16& d, const bf16x8& a, const bf16x8& b) { asm volatile("v_mfma_f32_32x32x16_bf16 %0, %1, %2, %0" : "+a"(d) : "v"(a), "v"(b)); }
#define MFMA_DRAIN_V(x) asm volatile("s_nop 7\n\ts_nop 7" : "+v"(x))
#define MFMA_DRAIN_O(o) asm volatile("s_nop 7\n\ts_nop 7" : "+a"(o[0]), "+a"(o[1]), "+a"(o[2]), "+a"(o[3]), "+a"(o[4]), "+a"(o[5]), "+a"(o[6]), "+a"(o[7]))
DI float ard(float a) { float r; asm volatile("v_accvgpr_read_b32 %0, %1" : "=v"(r) : "a"(a)); return r; }
DI float opaque0() { float z; asm volatile("v_mov_b32 %0, 0" : "=v"(z)); return z; }
DI unsigned rfl(unsigned x) { return __builtin_amdgcn_readfirstlane(x); }
DI unsigned cvtpk(float lo, float hi) { f32x2 v = {lo, hi}; bf16x2_t b = __builtin_convertvector(v, bf16x2_t); return __builtin_bit_cast(unsigned, b); }
DI float bflo(unsigned w) { return __uint_as_float(w << 16); }
DI float bfhi(unsigned w) { return __uint_as_float(w & 0xffff0000u); }
DI u32x4 mksrd(const void* p, unsigned bytes) { u32x4 s = {(unsigned)(uintptr_t)p, (unsigned)((uintptr_t)p >> 32) & 0xffffu, bytes, 0x20000u}; return s; }
DI bf16x8 ldsr(unsigned a) { return *(const LAS bf16x8*)(uintptr_t)a; }
DI bf16x8 ldstr(unsigned a, unsigned a2) {
  s16x4 x = __builtin_amdgcn_ds_read_tr16_b64_v4i16((LAS s16x4*)(uintptr_t)a);
  s16x4 y = __builtin_amdgcn_ds_read_tr16_b64_v4i16((LAS s16x4*)(uintptr_t)a2);
  return __builtin_shufflevector(x, y, 0, 1, 2, 3, 4, 5, 6, 7);
}
DI void set_m0(unsigned v) { asm volatile("s_mov_b32 m0, %0" ::"s"(v) : "memory"); }
template <int OFF, bool SC1 = true> DI void dma16(unsigned voff, u32x4 srd, unsigned soff) {
  if constexpr (SC1) asm volatile("buffer_load_dwordx4 %0, %1, %2 offen offset:%3 sc1 lds" ::"v"(voff), "s"(srd), "s"(soff), "i"(OFF) : "memory");
  else asm volatile("buffer_load_dwordx4 %0, %1, %2 offen offset:%3 lds" ::"v"(voff), "s"(srd), "s"(soff), "i"(OFF) : "memory");
}
#define PINS(x) asm volatile("" : "+s"(x))
#define PINA(x) asm volatile("" : "+a"(x))
#define NOP0() asm volatile("s_nop 0" ::: "memory")
template <int V, bool L0> DI void wait_bar() {
  asm volatile("" ::: "memory");
  __builtin_amdgcn_s_waitcnt((V & 15) | ((V >> 4) << 14) | (7 << 4) | (L0 ? 0 : (15 << 8)));
  __builtin_amdgcn_s_barrier();
  asm volatile("" ::: "memory");
  SBAR();
}
DI bf16x8 pack8(const f32x16& a, int s, float sc) {
  u32x4 w = {cvtpk(a[8 * s] * sc, a[8 * s + 1] * sc), cvtpk(a[8 * s + 2] * sc, a[8 * s + 3] * sc), cvtpk(a[8 * s + 4] * sc, a[8 * s + 5] * sc), cvtpk(a[8 * s + 6] * sc, a[8 * s + 7] * sc)};
  return __builtin_bit_cast(bf16x8, w);
}
DI bf16x8 pack8(const f32x16& a, int s) {
  u32x4 w = {cvtpk(a[8 * s], a[8 * s + 1]), cvtpk(a[8 * s + 2], a[8 * s + 3]), cvtpk(a[8 * s + 4], a[8 * s + 5]), cvtpk(a[8 * s + 6], a[8 * s + 7])};
  return __builtin_bit_cast(bf16x8, w);
}
DI float swap_other(float v) {
  auto r = __builtin_amdgcn_permlane32_swap(__float_as_uint(v), __float_as_uint(v), false, false);
  return (threadIdx.x & 32) ? __uint_as_float(r[0]) : __uint_as_float(r[1]);
}

#define STAMP_DECL
#define STAMPIT(i)
#define STAMPRT(i)
#define STAMP_OUT(k, n)
#define TR_DECL
#define TR_ARG
#define TR_PASS
#define TRC()
#define TR_OUT()
__host__ __device__ constexpr bool wf_isa(int f) { return f < 14 || (f >= 126 && (f - 126) % 18 < 2); }
__host__ __device__ constexpr int wf_nb(int f) { return f < 14 ? f >> 1 : f < 126 ? (f - 14) % 7 : 7 + (f - 126) / 18; }
__host__ __device__ constexpr int wf_ks(int f) { return f < 14 ? f & 1 : f < 126 ? (f - 14) / 7 : (f - 126) % 18 < 2 ? (f - 126) % 18 : (f - 126) % 18 - 2; }
constexpr unsigned XRING = 65536;
#define K1SC1 true
struct Prov { u32x4 srd; unsigned voff, soff, pstr, kstr; };
DI void prov_next(Prov& p) { p.soff += p.kstr; }
struct K1State { unsigned xs, ws, xd; };

template <int Q> DI void k1_dma(Prov& p, unsigned m0b) { set_m0(m0b + Q * 4096); NOP0(); dma16<0, K1SC1>(p.voff, p.srd, p.soff + Q * p.pstr); }

struct Sync { char* ws; unsigned token, lane; u32x4 fv; };
DI void flag_set(char* p, unsigned token) { __hip_atomic_store((GAS unsigned*)p, token, __ATOMIC_RELAXED, __HIP_MEMORY_SCOPE_AGENT); }
DI unsigned flag_get(const char* p) { return __hip_atomic_load((GAS unsigned*)p, __ATOMIC_RELAXED, __HIP_MEMORY_SCOPE_AGENT); }
constexpr unsigned SPIN_CAP = 1u << 20;
DI void flag_wait(const char* p, unsigned token) {
  for (unsigned i = 0; i < SPIN_CAP; ++i) { if (__all(flag_get(p) == token)) break; __builtin_amdgcn_s_sleep(1); }
}
#define DRAIN() asm volatile("s_waitcnt vmcnt(0)" ::: "memory")

template <bool SWAP, int NTL, bool FIRST>
DI void k1_ktile(f32x16 (&acc)[4][NTL], bf16x8 (&xa)[2][4], bf16x8 (&wf)[2][4], unsigned (&xr)[4], unsigned (&wr)[4], K1State& st, Prov& xp, Prov& wp,
                 const Prov& xn, const Prov& wn, const int t, const int NKT, const unsigned wid, Sync& Y) {
  constexpr int NM = 4 * NTL;
  sfor<4>([&](auto KS) __attribute__((always_inline)) { constexpr int ks = decltype(KS)::v, cur = ks & 1, nx = cur ^ 1, kn = (ks + 1) & 3;
    unsigned m0b;
    constexpr int VW = (FIRST && NTL == 3) ? 48 : 16;
    if constexpr (ks == 0) { if constexpr (NTL == 4) { if (t == 3) DRAIN(); } m0b = (st.ws ^ 32768u) + 16384 + wid * 1024; }
    if constexpr (ks == 1) { wait_bar<VW, true>(); m0b = XRING + st.xd + wid * 1024 + 16384;
      if constexpr (NTL == 4) { if (t == 3 && wid == 0 && Y.lane == 0) flag_set(Y.ws + WS_F1 + blockIdx.x * 4, Y.token); } }
    if constexpr (ks == 2) { if (t == NKT - 2) wp = wn; m0b = st.ws + wid * 1024; }
    if constexpr (ks == 3) {
      wait_bar<VW, true>();
      if constexpr (NTL == 4) {
        if (t == 13) { asm volatile("" : "+v"(Y.fv));
          if (!__all(Y.fv[0] == Y.token && Y.fv[1] == Y.token && Y.fv[2] == Y.token && Y.fv[3] == Y.token))
            for (int i = 0; i < 4; ++i) flag_wait(Y.ws + WS_F1 + Y.lane * 16 + i * 4, Y.token); }
        asm volatile("global_load_dwordx4 %0, %1, off sc1" : "+v"(Y.fv) : "v"(Y.ws + WS_F1 + Y.lane * 16) : "memory");
      }
      prov_next(xp); if (t == NKT - 3) xp = xn;
      st.xd = st.xs;
      m0b = XRING + st.xd + wid * 1024;
      const unsigned xs2 = st.xs == 65536 ? 0u : st.xs + 32768u, dx = xs2 - st.xs, dw = st.ws ? (unsigned)-32768 : 32768u;
      st.xs = xs2; st.ws ^= 32768u;
#pragma unroll
      for (int i = 0; i < 4; ++i) { xr[i] += dx; PIN(xr[i]); }
#pragma unroll
      for (int i = 0; i < NTL; ++i) { wr[i] += dw; PIN(wr[i]); }
    }
    Prov& pp = (ks & 1) ? xp : wp;
    unsigned so = pp.soff + (ks == 1 ? 4 * pp.pstr : 0);
    sfor<NM>([&](auto I) __attribute__((always_inline)) { constexpr int i = decltype(I)::v, m = i / NTL, n = i % NTL;
      if constexpr (FIRST && ks == 0) { const f32x16 z = {}; acc[m][n] = SWAP ? MFMA(wf[cur][n], xa[cur][m], z) : MFMA(xa[cur][m], wf[cur][n], z); }
      else acc[m][n] = SWAP ? MFMA(wf[cur][n], xa[cur][m], acc[m][n]) : MFMA(xa[cur][m], wf[cur][n], acc[m][n]);
      if constexpr (i == 0) xa[nx][0] = ldsr(xr[kn]);
      else if constexpr (i <= NTL) wf[nx][i - 1] = ldstr(wr[i - 1] + (kn & 1) * 8192 + (kn >> 1) * 16384, wr[i - 1] + (kn & 1) * 8192 + (kn >> 1) * 16384 + 2048);
      else if constexpr (i <= NTL + 3) xa[nx][i - NTL] = ldsr(xr[kn] + (i - NTL) * 4096);
      constexpr int D0 = NM - 8;
      if constexpr (i >= D0) {
        if constexpr (((i - D0) & 1) == 0) { set_m0(m0b + ((i - D0) >> 1) * 4096); if constexpr (i > D0) so += pp.pstr; PINS(so); }
        else dma16<0, K1SC1>(pp.voff, pp.srd, so);
      }
      SBAR();
    });
    if constexpr (!(ks & 1)) prov_next(wp);
  });
}

template <bool SWAP, int NTL>
DI void k1_gemm(f32x16 (&acc)[4][NTL], K1State& st, Prov& xp, Prov& wp, const Prov& xn, const Prov& wn, const int NKT, const unsigned wid, const unsigned lane, Sync& Y) {
  const unsigned r = lane & 31, hh = lane >> 5, q = (lane >> 2) & 3, wrow = wid >> 1, wc = wid & 1;
  unsigned xr[4], wr[4];
#pragma unroll
  for (int ks = 0; ks < 4; ++ks) { xr[ks] = XRING + st.xs + (128 * wrow + r) * 128 + 16 * ((2 * ks + hh) ^ ((r >> 1) & 7)); PIN(xr[ks]); }
#pragma unroll
  for (int n = 0; n < NTL; ++n) { wr[n] = st.ws + (8 * hh + q) * 512 + 64 * ((NTL * wc + n) ^ q) + 32 * ((lane >> 4) & 1) + 8 * (lane & 3); PIN(wr[n]); }
  bf16x8 xa[2][4], wf[2][4];
#pragma unroll
  for (int m = 0; m < 4; ++m) xa[0][m] = ldsr(xr[0] + m * 4096);
#pragma unroll
  for (int n = 0; n < NTL; ++n) wf[0][n] = ldstr(wr[n], wr[n] + 2048);
  SBAR();
  k1_ktile<SWAP, NTL, true>(acc, xa, wf, xr, wr, st, xp, wp, xn, wn, 0, NKT, wid, Y);
  for (int t = 1; t < NKT; ++t) k1_ktile<SWAP, NTL, false>(acc, xa, wf, xr, wr, st, xp, wp, xn, wn, t, NKT, wid, Y);
}

DI Prov mk_xprov(const char* X, unsigned ld, unsigned bytes, unsigned rb, unsigned wid, unsigned lane) {
  Prov p; p.srd = mksrd(X, bytes);
  p.voff = (lane >> 3) * ld + 16 * ((lane & 7) ^ (4 * (wid & 1) + (lane >> 4)));
  p.soff = (256 * rb + 8 * wid) * ld; p.pstr = 32 * ld; p.kstr = 128; return p;
}
template <class F> DI Prov mk_wprov(const char* W, unsigned ld, unsigned bytes, unsigned wid, unsigned lane, F colf) {
  Prov p; p.srd = mksrd(W, bytes);
  const unsigned ch = (lane & 31) ^ (((2 * (wid & 1) + (lane >> 5)) & 3) << 2), col = colf(ch);
  p.voff = col < 0x10000u ? (lane >> 5) * ld + 2 * col : 0x7ffffff0u;
  p.soff = 2 * wid * ld; p.pstr = 8 * ld; p.kstr = 32 * ld; return p;
}
#define ST_AUX 16
DI void st16(__amdgpu_buffer_rsrc_t rs, unsigned voff, unsigned soff, bf16x8 v) {
  __builtin_amdgcn_raw_buffer_store_b128(__builtin_bit_cast(u32x4, v), rs, voff, soff, ST_AUX);
  asm volatile("s_nop 1" ::"v"(v) : "memory");
}

DI void proj_phase(const char* x, const char* wqkv, const char* ckv, const char* wq, const char* wuk, const char* wuv, const char* gate_w,
                   const char* wo_a, const char* wo_b, char* ws, const unsigned kva_off, Sync& Y, const unsigned lane, const unsigned wid TR_ARG) {
  const unsigned tid = threadIdx.x, r = lane & 31, hh = lane >> 5, wrow = wid >> 1, wc = wid & 1, lane16 = lane * 16;
  const unsigned bid = blockIdx.x, xcd = bid & 7, loc = bid >> 3, rb = loc & 7, sel = loc >> 3;
  const unsigned cb = xcd + 8 * sel;
  const unsigned head2 = 2 * xcd + (sel & 1), isV = sel >> 1;
  const Prov xkv = mk_xprov(ckv, 2048, 2048 * 2048, rb, wid, lane);
  const Prov wkv = mk_wprov(isV ? wuv : wuk, 8192, 1024 * 8192, wid, lane, [&](unsigned ch) { return head2 * 256 + 8 * ch; });
  Prov xp = cb < 31 ? mk_xprov(x, 896, 2048 * 896, rb, wid, lane) : xkv;
  Prov wp = cb < 22 ? mk_wprov(wq, 8192, 448 * 8192, wid, lane, [&](unsigned ch) { return ch < 24 ? cb * 192 + 8 * ch : 0x10000u; })
          : cb < 31 ? mk_wprov(wqkv, 2688, 448 * 2688, wid, lane, [&](unsigned ch) { const unsigned vc = (cb < 28 ? cb - 22 : cb - 28) * 24 + ch, slot = vc >> 2;
                         return (ch < 24 && slot < (cb < 28 ? 32u : 16u)) ? (cb < 28 ? 0u : 896u) + 28 * slot + 8 * (vc & 3) : 0x10000u; })
          : wkv;
  K1State st = cb < 31 ? K1State{32768u, 32768u, 0u} : K1State{65536u, 0u, 32768u};
  {
    const unsigned x0 = XRING + st.xs + wid * 1024, x1 = XRING + (st.xs == 65536u ? 0u : st.xs + 32768u) + wid * 1024, x2 = XRING + st.xd + wid * 1024, w0 = st.ws + wid * 1024, w2 = (st.ws ^ 32768u) + wid * 1024;
    k1_dma<0>(xp, x0); k1_dma<1>(xp, x0); k1_dma<2>(xp, x0); k1_dma<3>(xp, x0); k1_dma<4>(xp, x0); k1_dma<5>(xp, x0); k1_dma<6>(xp, x0); k1_dma<7>(xp, x0); prov_next(xp);
    k1_dma<0>(wp, w0); k1_dma<1>(wp, w0); k1_dma<2>(wp, w0); k1_dma<3>(wp, w0); prov_next(wp);
    k1_dma<0>(xp, x1); k1_dma<1>(xp, x1); k1_dma<2>(xp, x1); k1_dma<3>(xp, x1);
    k1_dma<0>(wp, w0 + 16384); k1_dma<1>(wp, w0 + 16384); k1_dma<2>(wp, w0 + 16384); k1_dma<3>(wp, w0 + 16384); prov_next(wp);
    k1_dma<4>(xp, x1); k1_dma<5>(xp, x1); k1_dma<6>(xp, x1); k1_dma<7>(xp, x1); prov_next(xp);
    k1_dma<0>(wp, w2); k1_dma<1>(wp, w2); k1_dma<2>(wp, w2); k1_dma<3>(wp, w2); prov_next(wp);
    k1_dma<0>(xp, x2); k1_dma<1>(xp, x2); k1_dma<2>(xp, x2); k1_dma<3>(xp, x2);
  }
  const unsigned gw = bid * 4 + wid, hd = gw >> 6;
  unsigned short v[4][8];
#pragma unroll
  for (int i = 0; i < 4; ++i) { int f = (gw & 63) * 4 + i; f = f < 252 ? f : 251;
    const unsigned nb = wf_nb(f), ks = wf_ks(f); const bool isa = wf_isa(f);
    const char* src = (isa ? wo_a + hd * 28 * 896 : wo_b + hd * 256 * 896) + (32 * nb + r) * 2;
#pragma unroll
    for (int j = 0; j < 8; ++j) { unsigned d = 16 * ks + 8 * (j >> 2) + 4 * hh + (j & 3); if (isa && d >= 28) d = 0; v[i][j] = *(const unsigned short*)(src + d * 896); } }
  wait_bar<52, true>();
  TRC();
  const unsigned tb0 = rb * 8 + wrow * 4;
  const __amdgpu_buffer_rsrc_t rs = __builtin_amdgcn_make_buffer_rsrc(ws, 0, (int)WS_END, 0x20000);
  if (cb < 31) {
    f32x16 acc[4][3];
    if (cb < 28) {
      k1_gemm<true, 3>(acc, st, xp, wp, xkv, wkv, 7, wid, lane, Y);
      TRC();
#pragma unroll
      for (int n = 0; n < 3; ++n) { const unsigned c32 = (cb < 22 ? cb : cb - 22) * 6 + wc * 3 + n;
        if (c32 < (cb < 22 ? 128u : 32u)) {
          unsigned dst, mst; float sc;
          if (cb < 22) { dst = (unsigned)WS_QB + (((c32 >> 3) * 64 + tb0) * 16 + (c32 & 7) * 2) * 1024; mst = 16384; sc = QSCALE_B; }
          else if (c32 < 16) { dst = (unsigned)WS_QA + (c32 * 64 + tb0) * 2048; mst = 2048; sc = QSCALE_A; }
          else { dst = (unsigned)WS_KVA + ((c32 - 16) * 64 + tb0) * 4096; mst = 4096; sc = 1.f; }
          const bool pad = cb >= 22 && hh;
#pragma unroll
          for (int m = 0; m < 4; ++m) { PINA(acc[m][n]); u32x4 w1 = __builtin_bit_cast(u32x4, pack8(acc[m][n], 1, sc)); if (pad) { w1[2] = 0u; w1[3] = 0u; }
            st16(rs, lane16, dst + m * mst, pack8(acc[m][n], 0, sc)); st16(rs, lane16 + 1024, dst + m * mst, __builtin_bit_cast(bf16x8, w1)); SBAR(); } } }
    } else {
      k1_gemm<false, 3>(acc, st, xp, wp, xkv, wkv, 7, wid, lane, Y);
      TRC();
#pragma unroll
      for (int n = 0; n < 3; ++n) { const unsigned c32 = (cb - 28) * 6 + wc * 3 + n;
        if (c32 < 16) { const unsigned dst = (unsigned)WS_KVA + (c32 * 64 + tb0) * 4096 + 2048;
          const unsigned fill = r == 28 ? 0x3f803f80u : 0u;
#pragma unroll
          for (int m = 0; m < 4; ++m) { PINA(acc[m][n]);
#pragma unroll
            for (int s = 0; s < 2; ++s) { u32x4 w = __builtin_bit_cast(u32x4, pack8(acc[m][n], s)); if (r >= 28) w = (u32x4){fill, fill, fill, fill};
              st16(rs, lane16 + s * 1024, dst + m * 4096, __builtin_bit_cast(bf16x8, w)); }
            SBAR(); } } }
    }
  } else {
    const unsigned row = rb * 256 + tid;
    const u32x4* xr_ = (const u32x4*)(x + row * 896); const u32x4* gw_ = (const u32x4*)gate_w;
    float s = 0.f;
#pragma unroll 14
    for (int i = 0; i < 56; ++i) { const u32x4 a = xr_[i], b = gw_[i];
#pragma unroll
      for (int e = 0; e < 4; ++e) { s = fmaf(bflo(a[e]), bflo(b[e]), s); s = fmaf(bfhi(a[e]), bfhi(b[e]), s); } }
    flag_set(ws + WS_G + row * 4, __float_as_uint(1.f / (1.f + __expf(-s))));
    TRC();
  }
#pragma unroll
  for (int i = 0; i < 4; ++i) { u32x4 w;
#pragma unroll
    for (int e = 0; e < 4; ++e) w[e] = (unsigned)v[i][2 * e] | ((unsigned)v[i][2 * e + 1] << 16);
    st16(rs, lane16, (unsigned)WS_WOF + (gw * 4 + i) * 1024, __builtin_bit_cast(bf16x8, w)); }
  Y.token = rfl(Y.token);
  TRC();
  Prov xa_, wa_; xa_.srd = mksrd(ws, (unsigned)WS_END); xa_.voff = lane16; xa_.soff = kva_off + wid * 1024; xa_.pstr = 4096; xa_.kstr = 32768;
  wa_ = xa_; wa_.soff += 3 * 32768; wa_.kstr = 16384;
  f32x16 acc[4][4];
#define KV_TAIL() do { const unsigned mw = (st.ws ^ 32768u) + 16384 + wid * 1024, mx = XRING + st.xd + wid * 1024; TRC(); \
    k1_dma<0>(wp, mw); k1_dma<1>(wp, mw); k1_dma<2>(wp, mw); k1_dma<3>(wp, mw); k1_dma<4>(xp, mx); k1_dma<5>(xp, mx); k1_dma<6>(xp, mx); k1_dma<7>(xp, mx); } while (0)
  if (!isV) {
    k1_gemm<true, 4>(acc, st, xp, wp, xa_, wa_, 16, wid, lane, Y);
    KV_TAIL();
    const unsigned dst = (unsigned)WS_KB + ((head2 * 64 + tb0) * 16 + wc * 8) * 1024;
#pragma unroll
    for (int m = 0; m < 4; ++m)
#pragma unroll
      for (int n = 0; n < 4; ++n) { PINA(acc[m][n]); st16(rs, lane16, dst + m * 16384 + n * 2048, pack8(acc[m][n], 0)); st16(rs, lane16 + 1024, dst + m * 16384 + n * 2048, pack8(acc[m][n], 1)); SBAR(); }
  } else {
    k1_gemm<false, 4>(acc, st, xp, wp, xa_, wa_, 16, wid, lane, Y);
    KV_TAIL();
    const unsigned dst = (unsigned)WS_VB + ((head2 * 64 + tb0) * 16 + wc * 4) * 1024;
#pragma unroll
    for (int m = 0; m < 4; ++m)
#pragma unroll
      for (int n = 0; n < 4; ++n) { PINA(acc[m][n]); st16(rs, lane16 + n * 1024, dst + m * 16384, pack8(acc[m][n], 0)); st16(rs, lane16 + n * 1024, dst + m * 16384 + 8192, pack8(acc[m][n], 1)); SBAR(); }
  }
#undef KV_TAIL
}

struct Desc { unsigned so, m0; };
struct Stream {
  u32x4 srd; unsigned tab, lane16;
  unsigned dseq, dslot, rslot, so;
  Desc y;
};
DI unsigned nslot(unsigned s) { return s >= 131072u ? s - 131072u : s + 32768u; }
DI void st_prep(Stream& S, Desc& D) { D.so = __builtin_amdgcn_readlane(S.tab, S.dseq); PINS(D.so); D.m0 = S.dslot; S.dslot = nslot(S.dslot); S.dseq += 1; PINS(S.dslot); }
template <int Q> DI void st_pre(Stream& S, const Desc& D) {
  if constexpr (Q == 0) { set_m0(D.m0); S.so = D.so; } else if constexpr (Q == 4) { set_m0(D.m0 + 4096u); S.so = D.so + 4096u; PINS(S.so); }
}
template <int Q> DI void st_go(Stream& S) { dma16<(Q & 3) * 1024, true>(S.lane16, S.srd, S.so); }
DI void st_item(Stream& S) {
  sfor<8>([&](auto Q) __attribute__((always_inline)) { st_pre<decltype(Q)::v>(S, S.y); if constexpr (!(decltype(Q)::v & 3)) NOP0(); st_go<decltype(Q)::v>(S); });
  st_prep(S, S.y); asm volatile("s_nop 4" ::: "memory");
}
DI float maskv(float v, int dq, int rr) { return dq >= ((rr & 3) + 8 * (rr >> 2)) ? v : -__builtin_inff(); }
DI float max16(const f32x16& s) {
  float a = fmaxf(fmaxf(s[0], s[1]), s[2]), b = fmaxf(fmaxf(s[3], s[4]), s[5]);
  a = fmaxf(fmaxf(a, s[6]), s[7]); b = fmaxf(fmaxf(b, s[8]), s[9]); a = fmaxf(fmaxf(a, s[10]), s[11]); b = fmaxf(fmaxf(b, s[12]), s[13]);
  a = fmaxf(fmaxf(a, s[14]), s[15]); a = fmaxf(a, b);
  return fmaxf(a, swap_other(a));
}
DI float max3f(float a, float b, float c) { float r; asm("v_max3_f32 %0, %1, %2, %3" : "=v"(r) : "v"(a), "v"(b), "v"(c)); return r; }
DI f32x16 splat16(float v) { f32x16 r; for (int i = 0; i < 16; ++i) r[i] = v; return r; }
DI void open_item(Stream& S, unsigned& rb_, unsigned off) { rb_ = S.rslot + off; PIN(rb_); S.rslot = nslot(S.rslot); PINS(S.rslot); }
DI void unopen_item(Stream& S) { S.rslot = S.rslot == 0u ? 131072u : S.rslot - 32768u; }
template <int N> DI void wait_lgkm() { __builtin_amdgcn_s_waitcnt(15 | (3 << 14) | (7 << 4) | (N << 8)); }
DI void set_c(f32x16& c0, f32x16& c1, float v, int dq) {
#pragma unroll
  for (int e = 0; e < 16; ++e) { c0[e] = maskv(v, dq, e); c1[e] = maskv(v, dq - 32, e); }
  PIN(c0); PIN(c1); asm volatile("s_nop 1" ::: "memory");
}

template <bool FIRST>
DI void b_tile(f32x16 (&o)[8], const bf16x8 (&q)[16], bf16x8 (&fr)[8], f32x16& c0, f32x16& c1, float& mref, float& rs0, float& rs1, Stream& S, unsigned& rb_) {
  f32x16 s0, s1; u32x4 p[4]; float pe[32], ma, mb_; Desc X;
  constexpr int E0 = FIRST ? 22 : 18;
  sfor<64>([&](auto J) __attribute__((always_inline)) { constexpr int j = decltype(J)::v;
    if constexpr (j == 24) { wait_bar<16, false>(); open_item(S, rb_, S.lane16); }
    if constexpr (j == 56) { wait_bar<11, false>(); open_item(S, rb_, S.lane16); }
    if constexpr ((j & 3) == 0) wait_lgkm<4>();
    if constexpr (j == 0) mfma_vc(s0, fr[0], q[0], c0);
    else if constexpr (j < 16) mfma_v(s0, fr[j & 7], q[j]);
    else if constexpr (j == 16) mfma_vc(s1, fr[0], q[0], c1);
    else if constexpr (j < 32) mfma_v(s1, fr[j & 7], q[j - 16]);
    else if constexpr (FIRST && j < 40) asm volatile("v_mfma_f32_32x32x16_bf16 %0, %1, %2, 0" : "=&a"(o[j & 7]) : "v"(fr[j & 7]), "v"(p[0]));
    else mfma_a(o[j & 7], fr[j & 7], __builtin_bit_cast(bf16x8, p[(j - 32) >> 3]));
    fr[j & 7] = ldsr(rb_ + ((j + 8) & 31) * 1024);
    if constexpr (j < 16 && (j & 1)) st_go<(j >> 1)>(S);
    if constexpr (j == 51 || j == 53 || j == 55 || j == 57 || j == 59 || j == 61) st_go<((j - 51) >> 1)>(S);
    if constexpr (j == 62) st_go<6>(S);
    if constexpr (j == 63) st_go<7>(S);
    if constexpr (FIRST) {
      if constexpr (j == 18) { ma = max3f(s0[0], s0[1], s0[2]); mb_ = max3f(s0[3], s0[4], s0[5]); ma = max3f(ma, s0[6], s0[7]); mb_ = max3f(mb_, s0[8], s0[9]); PIN(ma); PIN(mb_); }
      if constexpr (j == 19) { ma = max3f(ma, s0[10], s0[11]); mb_ = max3f(mb_, s0[12], s0[13]); ma = max3f(ma, s0[14], s0[15]); ma = fmaxf(ma, mb_); PIN(ma); }
      if constexpr (j == 20) { ma = fmaxf(ma, swap_other(ma)); mref = ma == -__builtin_inff() ? 0.f : ma; PIN(mref); }
      if constexpr (j >= 56) { constexpr int e = 2 * (j - 56); float v = -mref; c0[e] = v; c0[e + 1] = v; c1[e] = v; c1[e + 1] = v; if constexpr (j == 63) { PIN(c0); PIN(c1); } }
    }
    if constexpr (j >= E0 && j < E0 + 32) { constexpr int e = j - E0;
      float v = e < 16 ? s0[e & 15] : s1[e & 15]; if constexpr (FIRST) v -= mref; v = __builtin_amdgcn_exp2f(v); PIN(v); pe[e] = v; }
    if constexpr (j >= E0 + 1 && j < E0 + 33) { constexpr int e = j - E0 - 1;
      if constexpr (e & 1) { rs1 += pe[e]; PIN(rs1); unsigned w = cvtpk(pe[e - 1], pe[e]); PIN(w); p[e >> 3][(e >> 1) & 3] = w; }
      else { rs0 += pe[e]; PIN(rs0); } }
    if constexpr (j == 0) st_pre<0>(S, S.y);
    if constexpr (j == 8) st_pre<4>(S, S.y);
    if constexpr (j == 16) st_prep(S, X);
    if constexpr (j == 17) st_prep(S, S.y);
    if constexpr (j == 50) st_pre<0>(S, X);
    if constexpr (j == 58) st_pre<4>(S, X);
    SBAR();
  });
}

DI void hand_step(const char* fh, const char* ml, const unsigned token, const int left, bool& late) {
  if (left == 2) {
    unsigned fw; asm volatile("v_accvgpr_read_b32 %0, a254" : "=v"(fw));
    late = rfl(fw) != token;
    asm volatile("global_load_dwordx2 a[252:253], %0, off sc1" ::"v"(ml) : "memory", "a252", "a253");
  }
  if (left == 3) asm volatile("global_load_dword a254, %0, off sc1" ::"v"(fh) : "memory", "a254");
}

DI void b_segment(f32x16 (&o)[8], const bf16x8 (&q)[16], float& mref, float& lsum, Stream& S, const int qb, const int t0, const int t1,
                  const unsigned lane, const unsigned wid, const char* fh, const char* ml, const unsigned token, const bool consume, float& mb, float& lb, bool& late TR_ARG) {
  TRC();
  const int qrow = qb * 128 + (int)wid * 32 + (int)(lane & 31) - 4 * (int)(lane >> 5);
  unsigned rb_; open_item(S, rb_, S.lane16);
  bf16x8 fr[8];
#pragma unroll
  for (int j = 0; j < 8; ++j) fr[j] = ldsr(rb_ + j * 1024);
  f32x16 c0, c1; float rs0 = 0.f, rs1 = 0.f;
  set_c(c0, c1, 0.f, qrow - t0 * 64);
  SBAR();
  b_tile<true>(o, q, fr, c0, c1, mref, rs0, rs1, S, rb_);
  const int tc = consume ? t1 : -8;
  for (int t = t0 + 1; t < t1; ++t) {
    TRC();
    hand_step(fh, ml, token, tc - t, late);
    if (t >= 2 * qb) set_c(c0, c1, -mref, qrow - t * 64);
    b_tile<false>(o, q, fr, c0, c1, mref, rs0, rs1, S, rb_);
  }
  SBAR();
  asm volatile("v_accvgpr_read_b32 %0, a252\n\tv_accvgpr_read_b32 %1, a253" : "=v"(mb), "=v"(lb));
  MFMA_DRAIN_O(o); SBAR(); TRC();
  unopen_item(S);
  const float l = rs0 + rs1; lsum = l + swap_other(l);
}

DI void a_finish(bf16x8 (&ca)[2], f32x16 o, const float g, const unsigned lane) {
  const bool up = lane >= 32;
  const float lo = swap_other(o[12]), l = up ? o[12] : lo;
  const float sc = g / l + l * 0.f;
#pragma unroll
  for (int e = 12; e < 16; ++e) o[e] = up ? 0.f : o[e];
  ca[0] = pack8(o, 0, sc); ca[1] = pack8(o, 1, sc);
}
DI void a_phase(bf16x8 (&ca)[2], Stream& S, const bf16x8 q0, const bf16x8 q1, const float g, const unsigned lane, char* f2_mine, const char* f2_need, const unsigned token TR_ARG) {
  TRC();
  unsigned rb_; open_item(S, rb_, S.lane16);
  bf16x8 kf[2][2], va[2], vb[2];
  kf[0][0] = ldsr(rb_); kf[0][1] = ldsr(rb_ + 1024); kf[1][0] = ldsr(rb_ + 4096); kf[1][1] = ldsr(rb_ + 5120);
  va[0] = ldsr(rb_ + 2048); vb[0] = ldsr(rb_ + 3072); va[1] = ldsr(rb_ + 4096 + 2048); vb[1] = vb[0];
  f32x16 s[2], o = {}, negm;
  { f32x16 z = {}; z = MFMA(kf[0][0], q0, z); z = MFMA(kf[0][1], q1, z); negm = splat16(-max16(z)); PIN(negm); }
  u32x4 p0, p1 = {0u, 0u, 0u, 0u}; float pt[4] = {0.f, 0.f, 0.f, 0.f};
  PIN(o); asm volatile("s_nop 1" ::: "memory");
  mfma_vc(s[0], kf[0][0], q0, negm); mfma_v(s[0], kf[0][1], q1);
  kf[0][0] = ldsr(rb_ + 8192); kf[0][1] = ldsr(rb_ + 9216);
  MFMA_DRAIN_V(s[0]);
  SBAR();
  for (int it = 0; it < 8; ++it) {
    TRC();
    unsigned rn_ = rb_;
    if (it == 6) {
      unsigned fw; asm volatile("v_accvgpr_read_b32 %0, a254" : "=v"(fw));
      if (!__all(fw == token)) flag_wait(f2_need, token); }
    if (it == 5) asm volatile("global_load_dword a254, %0, off sc1" ::"v"(f2_need) : "memory", "a254");
    sfor<8>([&](auto KB) __attribute__((always_inline)) { constexpr int kb = decltype(KB)::v, c = kb & 1, n = c ^ 1;
#define AADDR(blk, off) ((blk) < 8 ? rb_ + (blk) * 4096 + (off) : rn_ + ((blk) - 8) * 4096 + (off))
#define AEXP(e) do { pe[e] = __builtin_amdgcn_exp2f(s[c][e]); PIN(pe[e]); } while (0)
#define ACVT(P, i, a, b) do { unsigned w_ = cvtpk(a, b); PIN(w_); P[i] = w_; } while (0)
      if constexpr (kb == 5) { if (it >= 4) asm volatile("s_waitcnt vmcnt(8)" ::: "memory"); wait_bar<63, false>(); open_item(S, rn_, S.lane16); }
      float pe[16];
      wait_lgkm<4>();
      mfma_vc(s[n], kf[n][0], q0, negm);
      kf[n][0] = ldsr(AADDR(kb + 3, 0)); SBAR();
      AEXP(0); AEXP(1); ACVT(p1, 2, pt[0], pt[1]); AEXP(2); AEXP(3); ACVT(p1, 3, pt[2], pt[3]);
      const bool rq = kb < 4 ? it >= 3 : it == 7;
      if constexpr ((kb & 3) == 0) st_pre<0>(S, S.y);
      if constexpr ((kb & 3) == 2) st_pre<4>(S, S.y);
      SBAR();
      mfma_v(s[n], kf[n][1], q1);
      kf[n][1] = ldsr(AADDR(kb + 3, 1024));
      if (rq) st_go<2 * (kb & 3)>(S);
      SBAR();
      AEXP(4); AEXP(5); ACVT(p0, 0, pe[0], pe[1]); AEXP(6); AEXP(7); ACVT(p0, 1, pe[2], pe[3]);
      SBAR();
      mfma_v(o, vb[n], __builtin_bit_cast(bf16x8, p1));
      vb[n] = ldsr(AADDR(kb + 1, 3072)); SBAR();
      AEXP(8); ACVT(p0, 2, pe[4], pe[5]); AEXP(9); ACVT(p0, 3, pe[6], pe[7]); SBAR(); AEXP(10); AEXP(11);
      SBAR();
      mfma_v(o, va[c], __builtin_bit_cast(bf16x8, p0));
      va[c] = ldsr(AADDR(kb + 2, 2048));
      if (rq) st_go<2 * (kb & 3) + 1>(S);
      SBAR();
      AEXP(12); AEXP(13); ACVT(p1, 0, pe[8], pe[9]); AEXP(14); AEXP(15); ACVT(p1, 1, pe[10], pe[11]);
      pt[0] = pe[12]; pt[1] = pe[13]; pt[2] = pe[14]; pt[3] = pe[15];
      if constexpr ((kb & 3) == 3) { if (rq) { st_prep(S, S.y); asm volatile("s_nop 3" ::: "memory"); } }
      if constexpr (kb == 7) { if (it == 2) { DRAIN(); if (lane == 0) flag_set(f2_mine, token); } }
      SBAR();
#undef AADDR
#undef AEXP
#undef ACVT
    });
    rb_ = rn_;
  }
  p1[2] = cvtpk(pt[0], pt[1]); p1[3] = cvtpk(pt[2], pt[3]);
  mfma_vn(o, vb[1], __builtin_bit_cast(bf16x8, p1));
  MFMA_DRAIN_V(o);
  unopen_item(S);
  a_finish(ca, o, g, lane);
}

template <bool MERGE>
DI float out_proj(f32x16 (&o)[8], const bf16x8 (&ca)[2], const float fa, const float fb, Stream& S, const __amdgpu_buffer_rsrc_t rs, const unsigned og, const unsigned wid TR_ARG) {
  u32x4 pb[16], cb[16], ow; bf16x8 fr[8]; f32x16 acc[14]; unsigned rb_;
  if constexpr (MERGE) {
    open_item(S, rb_, wid * 8192 + S.lane16);
#pragma unroll
    for (int c = 0; c < 8; ++c) pb[c] = __builtin_bit_cast(u32x4, ldsr(rb_ + c * 1024));
    wait_bar<8, false>(); open_item(S, rb_, wid * 8192 + S.lane16);
#pragma unroll
    for (int c = 0; c < 8; ++c) pb[8 + c] = __builtin_bit_cast(u32x4, ldsr(rb_ + c * 1024));
    wait_bar<0, false>();
  }
  open_item(S, rb_, S.lane16);
#pragma unroll
  for (int j = 0; j < 8; ++j) fr[j] = ldsr(rb_ + j * 1024);
  SBAR();
  Desc X;
  sfor<252>([&](auto F) __attribute__((always_inline)) { constexpr int f = decltype(F)::v, f8 = f + 8, w = f >> 5, ph = f & 31;
    constexpr int nb = wf_nb(f), ks = wf_ks(f);
    if constexpr (ph == 24 && w < 7) { TRC();
      wait_bar<(MERGE && w == 0) ? 4 : (MERGE && w == 1) ? 12 : w < 5 ? 16 : 8, false>(); open_item(S, rb_, S.lane16); }
    if constexpr ((f & 3) == 0) wait_lgkm<4>();
    if constexpr (wf_isa(f)) { if constexpr (ks == 0) { const f32x16 z = {}; acc[nb] = MFMA(fr[f & 7], ca[0], z); } else acc[nb] = MFMA(fr[f & 7], ca[1], acc[nb]); }
    else acc[nb] = MFMA(fr[f & 7], __builtin_bit_cast(bf16x8, cb[ks]), acc[nb]);
    if constexpr (f8 < 252) fr[f & 7] = ldsr(rb_ + (f8 & 31) * 1024);
    constexpr bool r1 = MERGE ? w < 5 : w < 5, r2 = MERGE && w < 2;
    if constexpr (r1 && ph < 16 && (ph & 1)) st_go<(ph >> 1)>(S);
    if constexpr (r2 && ph >= 16 && (ph & 1)) st_go<((ph - 16) >> 1)>(S);
    if constexpr (r1 && (ph == 0 || ph == 8)) st_pre<ph / 2>(S, S.y);
    if constexpr (r2 && ph == 2) st_prep(S, X);
    if constexpr (r2 && (ph == 16 || ph == 24)) st_pre<(ph - 16) / 2>(S, X);
    if constexpr (r1 && ph == 18) st_prep(S, S.y);
    if constexpr (f < 96 && f % 6 < 4) { constexpr int c = f / 6, e = f % 6; const float x0 = ard(o[c >> 1][8 * (c & 1) + 2 * e]), x1 = ard(o[c >> 1][8 * (c & 1) + 2 * e + 1]);
      unsigned v = MERGE ? cvtpk(fmaf(x0, fa, bflo(pb[c][e]) * fb), fmaf(x1, fa, bfhi(pb[c][e]) * fb)) : cvtpk(x0 * fa, x1 * fa); PIN(v); cb[c][e] = v; }
    if constexpr (f >= 128 && f < 245 && (f - 128) % 9 < 8) { constexpr int t = (f - 128) / 9, u = (f - 128) % 9;
      unsigned v = cvtpk(ard(acc[t][2 * u]), ard(acc[t][2 * u + 1])); PIN(v); ow[u & 3] = v;
      if constexpr ((u & 3) == 3) st16(rs, S.lane16, og + (t * 2 + (u >> 2)) * 1024, __builtin_bit_cast(bf16x8, ow)); }
    SBAR();
  });
  st16(rs, S.lane16, og + 26 * 1024, pack8(acc[13], 0)); st16(rs, S.lane16, og + 27 * 1024, pack8(acc[13], 1));
  return acc[13][0];
}

DI bf16x8 ld16(__amdgpu_buffer_rsrc_t rs, unsigned voff, unsigned soff) { return __builtin_bit_cast(bf16x8, __builtin_amdgcn_raw_buffer_load_b128(rs, voff, soff, 16)); }
DI void safe_redo(const __amdgpu_buffer_rsrc_t rs, const unsigned og, const unsigned head, const unsigned tb, const float g, const unsigned lane) {
  const unsigned lane16 = lane * 16;
  bf16x8 ca[2], cb[16];
  {
    const unsigned qa = (unsigned)WS_QA + (head * 64 + tb) * 2048; const bf16x8 q0 = ld16(rs, lane16, qa), q1 = ld16(rs, lane16, qa + 1024);
    float m = -1e30f; f32x16 o = splat16(opaque0());
    for (int kb = 0; kb < 64; ++kb) {
      const unsigned p = (unsigned)WS_KVA + (head * 64 + kb) * 4096;
      f32x16 t = {}; t = MFMA(ld16(rs, lane16, p), q0, t); t = MFMA(ld16(rs, lane16, p + 1024), q1, t);
      const float mn = fmaxf(m, max16(t)), al = __builtin_amdgcn_exp2f(m - mn); m = mn;
#pragma unroll
      for (int e = 0; e < 16; ++e) t[e] = __builtin_amdgcn_exp2f(t[e] - mn);
      o *= al; o = MFMA(ld16(rs, lane16, p + 2048), pack8(t, 0), o); o = MFMA(ld16(rs, lane16, p + 3072), pack8(t, 1), o);
    }
    a_finish(ca, o, g, lane);
  }
  {
    const unsigned qg = (unsigned)WS_QB + (head * 64 + tb) * 16384;
    const int qrow = (int)tb * 32 + (int)(lane & 31) - 4 * (int)(lane >> 5);
    float m = -1e30f, ls = 0.f; f32x16 o[8];
#pragma unroll
    for (int d = 0; d < 8; ++d) o[d] = splat16(opaque0());
    for (int kb = 0; kb <= (int)tb; ++kb) {
      const unsigned kp = (unsigned)WS_KB + (head * 64 + kb) * 16384, vp = (unsigned)WS_VB + (head * 64 + kb) * 16384;
      f32x16 s = {};
#pragma unroll
      for (int j = 0; j < 16; ++j) s = MFMA(ld16(rs, lane16, kp + j * 1024), ld16(rs, lane16, qg + j * 1024), s);
#pragma unroll
      for (int e = 0; e < 16; ++e) s[e] = maskv(s[e], qrow - kb * 32, e);
      const float mn = fmaxf(m, max16(s)), al = __builtin_amdgcn_exp2f(m - mn); m = mn; ls *= al;
#pragma unroll
      for (int e = 0; e < 16; ++e) { s[e] = __builtin_amdgcn_exp2f(s[e] - mn); ls += s[e]; }
      const bf16x8 p0 = pack8(s, 0), p1 = pack8(s, 1);
#pragma unroll
      for (int d = 0; d < 8; ++d) { o[d] *= al; o[d] = MFMA(ld16(rs, lane16, vp + d * 1024), p0, o[d]); o[d] = MFMA(ld16(rs, lane16, vp + 8192 + d * 1024), p1, o[d]); }
    }
    const float inv = (1.f - g) / (ls + swap_other(ls));
#pragma unroll
    for (int c = 0; c < 16; ++c) cb[c] = pack8(o[c >> 1], c & 1, inv);
  }
  const unsigned wf = (unsigned)WS_WOF + head * 262144;
  DRAIN();
  for (int nb = 0; nb < 14; ++nb) {
    const int fa = nb < 7 ? 2 * nb : 126 + 18 * (nb - 7);
    f32x16 a = {}; a = MFMA(ld16(rs, lane16, wf + fa * 1024), ca[0], a); a = MFMA(ld16(rs, lane16, wf + (fa + 1) * 1024), ca[1], a);
#pragma unroll
    for (int ks = 0; ks < 16; ++ks) a = MFMA(ld16(rs, lane16, wf + (nb < 7 ? 14 + 7 * ks + nb : fa + 2 + ks) * 1024), cb[ks], a);
    st16(rs, lane16, og + (2 * nb) * 1024, pack8(a, 0)); st16(rs, lane16, og + (2 * nb + 1) * 1024, pack8(a, 1));
  }
}

__global__ __launch_bounds__(256, 1) void fused_fwd(const char* __restrict__ x, const char* __restrict__ wqkv, const char* __restrict__ ckv, const char* __restrict__ wq,
                                                    const char* __restrict__ wuk, const char* __restrict__ wuv, const char* __restrict__ gate_w,
                                                    const char* __restrict__ wo_a, const char* __restrict__ wo_b, char* ws, char* __restrict__ out) {
  extern __shared__ __attribute__((aligned(16))) char lds[];
  const unsigned tid = threadIdx.x, lane = tid & 63, wid = rfl(tid >> 6), lane16 = lane * 16;
  const unsigned bid = blockIdx.x, head = 2 * (bid & 7) + ((bid >> 3) & 1), pi = bid >> 5, roleb = (bid >> 4) & 1;
  STAMP_DECL STAMPRT(0); STAMPIT(1); TR_DECL TRC();
  Sync Y; Y.ws = ws; Y.lane = lane; Y.fv = (u32x4){0u, 0u, 0u, 0u};
  Y.token = flag_get(ws + WS_W0) + 1u;
  proj_phase(x, wqkv, ckv, wq, wuk, wuv, gate_w, wo_a, wo_b, ws, (unsigned)WS_KVA + head * 262144, Y, lane, wid TR_PASS);
  STAMPIT(2);
  const unsigned token = Y.token;
  if (bid == 0 && tid == 0) flag_set(ws + WS_W0, token);
  const int qlong = 15 - (int)pi, nt1 = 32 - 2 * (int)pi - TA;
  const int qb = roleb ? (int)pi : qlong, nt2 = roleb ? 2 * (int)pi + 2 : TA;
  const unsigned pw = (head * 8 + pi) * 4 + wid;
  const unsigned po = (unsigned)WS_PO + pw * 16384, tb = qb * 4 + wid;
  const __amdgpu_buffer_rsrc_t rs = __builtin_amdgcn_make_buffer_rsrc(ws, 0, (int)WS_END, 0x20000);
  Stream S;
  {
    const unsigned n1 = 8 + (roleb ? 2 * nt1 : 0), n2 = n1 + 2 * nt2, n3 = n2 + (roleb ? 0 : 2), j = lane;
    const unsigned jb = j < n1 ? j - 8 + 2 * TA : j - n1, kv = (unsigned)WS_KB + head * (unsigned)MB + (jb & 1) * (unsigned)(WS_VB - WS_KB) + (jb >> 1) * 32768;
    unsigned a = j >= 8 ? kv : (unsigned)WS_KVA + head * 262144 + j * 32768;
    a = j >= n3 ? (unsigned)WS_WOF + head * 262144 + (j - n3) * 32768 : a;
    a += wid * 8192;
    a = (j >= n2 && j < n3) ? po + (j - n2) * 8192 : a;
    S.tab = j < n3 + 8 ? a : 0x7ffffff0u; S.srd = mksrd(ws, (unsigned)WS_END); S.lane16 = lane16;
    S.dseq = 5; S.dslot = 65536 + wid * 8192; S.rslot = 65536;
  }
  st_prep(S, S.y);
  const bf16x8 qa0 = ld16(rs, lane16, (unsigned)WS_QA + (head * 64 + tb) * 2048), qa1 = ld16(rs, lane16, (unsigned)WS_QA + (head * 64 + tb) * 2048 + 1024);
  const float g = __uint_as_float(flag_get(ws + WS_G + (tb * 32 + (lane & 31)) * 4));
  bf16x8 q[16];
#pragma unroll
  for (int j = 0; j < 16; ++j) q[j] = ld16(rs, lane16, (unsigned)WS_QB + ((head * 64 + (roleb ? qlong * 4 + wid : tb)) * 16 + j) * 1024);
  wait_bar<51, false>();
  asm volatile("" ::"v"(Y.fv));
  STAMPIT(3);
  bf16x8 ca[2];
  { const unsigned xcd = bid & 7, loc = bid >> 3, rb = loc & 7, sel = loc >> 3;
    a_phase(ca, S, qa0, qa1, g, lane, ws + WS_F2 + (((2 * xcd + (sel & 1)) * 16 + (sel >> 1) * 8 + rb) * 4 + wid) * 4, ws + WS_F2 + (head * 64 + lane) * 4, token TR_PASS); }
  STAMPIT(4);
  char* fh = ws + WS_FH + pw * 64; char* ml = ws + WS_ML + pw * 512 + lane * 8;
  f32x16 o[8]; float mref, lsum, mb, lb; bool late = false;
  for (int pass = roleb ? 0 : 1; pass < 2; ++pass) {
    b_segment(o, q, mref, lsum, S, pass ? qb : qlong, pass ? 0 : TA, pass ? nt2 : TA + nt1, lane, wid, fh, ml, token, !roleb, mb, lb, late TR_PASS);
    if (pass == 0) {
#pragma unroll
      for (int j = 0; j < 16; ++j) q[j] = ld16(rs, lane16, (unsigned)WS_QB + ((head * 64 + tb) * 16 + j) * 1024);
      const float inv = lsum > 0.f ? 1.f / lsum : 0.f;
#pragma unroll
      for (int c = 0; c < 16; ++c) { if (!(c & 1)) PINA(o[c >> 1]); st16(rs, lane16, po + c * 1024, pack8(o[c >> 1], c & 1, inv)); SBAR(); }
      __hip_atomic_store((GAS unsigned long long*)ml, ((unsigned long long)__float_as_uint(lsum) << 32) | __float_as_uint(mref), __ATOMIC_RELAXED, __HIP_MEMORY_SCOPE_AGENT);
      DRAIN();
      if (lane == 0) flag_set(fh, token);
    }
  }
  STAMPIT(5);
  const unsigned og = (unsigned)WS_OP + (head * 64 + tb) * 28 * 1024;
  float chk;
  if (!roleb) {
    if (late) {
      flag_wait(fh, token);
      const unsigned long long w = __hip_atomic_load((GAS unsigned long long*)ml, __ATOMIC_RELAXED, __HIP_MEMORY_SCOPE_AGENT);
      mb = __uint_as_float((unsigned)w); lb = __uint_as_float((unsigned)(w >> 32));
      Desc D; D.so = po; D.m0 = S.rslot + wid * 8192;
      sfor<16>([&](auto Q) __attribute__((always_inline)) { constexpr int qq = decltype(Q)::v;
        if constexpr (qq == 8) { D.so = po + 8192; D.m0 = nslot(S.rslot) + wid * 8192; }
        st_pre<(qq & 7)>(S, D); if constexpr (!(qq & 3)) NOP0(); st_go<(qq & 7)>(S); });
      DRAIN();
    }
    const float m = lb > 0.f ? fmaxf(mref, mb) : mref, ea = __builtin_amdgcn_exp2f(mref - m), wb = lb > 0.f ? lb * __builtin_amdgcn_exp2f(mb - m) : lb * 0.f;
    const float den = lsum * ea + wb, inv = (1.f - g) / den;
    chk = den + out_proj<true>(o, ca, ea * inv, wb * inv, S, rs, og, wid TR_PASS);
  } else chk = lsum + out_proj<false>(o, ca, (1.f - g) / lsum, 0.f, S, rs, og, wid TR_PASS);
  STAMPIT(6);
  if (__builtin_expect(__any(!(chk * 0.f == 0.f)), 0)) safe_redo(rs, og, head, tb, g, lane);
  DRAIN();
  if (lane == 0) flag_set(ws + WS_F3 + (tb * 16 + head) * 4, token);
  STAMPIT(7);
  flag_wait(ws + WS_F3 + (tb * 16 + (lane & 15)) * 4, token);
  STAMPIT(8);
  {
    u32x4 v[2][16];
    const unsigned c0 = head * 112 + lane, c1 = lane < 48 ? c0 + 64 : c0;
#pragma unroll
    for (int h = 0; h < 16; ++h) { v[0][h] = __builtin_bit_cast(u32x4, ld16(rs, c0 * 16, (unsigned)WS_OP + (h * 64 + tb) * 28 * 1024)); v[1][h] = __builtin_bit_cast(u32x4, ld16(rs, c1 * 16, (unsigned)WS_OP + (h * 64 + tb) * 28 * 1024)); }
    SBAR();
#pragma unroll
    for (int k = 0; k < 2; ++k) { float a[8] = {};
#pragma unroll
      for (int h = 0; h < 16; ++h)
#pragma unroll
        for (int e = 0; e < 4; ++e) { a[2 * e] += bflo(v[k][h][e]); a[2 * e + 1] += bfhi(v[k][h][e]); }
      const unsigned c = k ? c1 : c0;
      char* op = out + (size_t)(tb * 32 + (c & 31)) * 896 + ((c >> 6) * 16 + 4 * ((c >> 5) & 1)) * 2;
      if (k == 0 || lane < 48) { *(u32x2*)op = (u32x2){cvtpk(a[0], a[1]), cvtpk(a[2], a[3])}; *(u32x2*)(op + 16) = (u32x2){cvtpk(a[4], a[5]), cvtpk(a[6], a[7])}; } }
  }
  STAMPIT(9); STAMPRT(10); STAMP_OUT(1, 11); TRC(); TR_OUT();
}

extern "C" void kernel_launch(void* const* d_in, const int* in_sizes, int n_in, void* d_out, int out_size, void* d_ws, size_t ws_size, hipStream_t stream) {
  (void)in_sizes; (void)n_in; (void)out_size; (void)ws_size;
  (void)hipFuncSetAttribute((const void*)fused_fwd, hipFuncAttributeMaxDynamicSharedMemorySize, 163840);
  fused_fwd<<<256, 256, 163840, stream>>>((const char*)d_in[0], (const char*)d_in[1], (const char*)d_in[4], (const char*)d_in[5], (const char*)d_in[6], (const char*)d_in[7], (const char*)d_in[9],
                                          (const char*)d_in[2], (const char*)d_in[8], (char*)d_ws, (char*)d_out);
}
```

```cpp
#include <hip/hip_runtime.h>
#include <stdint.h>
#include <utility>

typedef short bf16x8 __attribute__((ext_vector_type(8)));
typedef short s16x4 __attribute__((ext_vector_type(4)));
typedef float f32x16 __attribute__((ext_vector_type(16)));
typedef float f32x2 __attribute__((ext_vector_type(2)));
typedef __bf16 bf16x2_t __attribute__((ext_vector_type(2)));
typedef unsigned u32x4 __attribute__((ext_vector_type(4)));
typedef unsigned u32x2 __attribute__((ext_vector_type(2)));
#define LAS __attribute__((address_space(3)))
#define GAS __attribute__((address_space(1)))
#define DI __device__ __forceinline__
#define SBAR() __builtin_amdgcn_sched_barrier(0)
#define MFMA(a, b, c) __builtin_amdgcn_mfma_f32_32x32x16_bf16((a), (b), (c), 0, 0, 0)
#define PIN(x) asm volatile("" : "+v"(x))

constexpr size_t MB = 1u << 20;
constexpr size_t WS_QB = 0, WS_KB = 16 * MB, WS_VB = 32 * MB, WS_QA = 48 * MB, WS_KVA = 50 * MB, WS_G = 54 * MB, WS_W0 = 54 * MB + 65536, WS_F1 = WS_W0 + 4096, WS_F2 = WS_W0 + 8192, WS_F3 = WS_W0 + 16384, WS_FH = WS_W0 + 32768,
                 WS_ML = 55 * MB, WS_PO = 56 * MB, WS_OP = 64 * MB, WS_WOF = 92 * MB, WS_END = 96 * MB;
#define TA_ 18
constexpr int TA = TA_;
constexpr float LOG2E = 1.4426950408889634f;
constexpr float QSCALE_B = 0.0625f * LOG2E;
constexpr float QSCALE_A = 0.18898223650461362f * LOG2E;

template <int I> struct IC { static constexpr int v = I; };
template <class F, int... Is> DI void sfor_(F&& f, std::integer_sequence<int, Is...>) { (f(IC<Is>{}), ...); }
template <int N, class F> DI void sfor(F&& f) { sfor_(f, std::make_integer_sequence<int, N>{}); }
DI void mfma_vc(f32x16& d, const bf16x8& a, const bf16x8& b, const f32x16& c) { asm volatile("v_mfma_f32_32x32x16_bf16 %0, %1, %2, %3" : "=&v"(d) : "v"(a), "v"(b), "v"(c)); }
DI void mfma_v(f32x16& d, const bf16x8& a, const bf16x8& b) { asm volatile("v_mfma_f32_32x32x16_bf16 %0, %1, %2, %0" : "+v"(d) : "v"(a), "v"(b)); }
DI void mfma_vn(f32x16& d, const bf16x8& a, const bf16x8& b) { asm volatile("s_nop 1\n\tv_mfma_f32_32x32x16_bf16 %0, %1, %2, %0" : "+v"(d) : "v"(a), "v"(b)); }
DI void mfma_a(f32x16& d, const bf16x8& a, const bf16x8& b) { asm volatile("v_mfma_f32_32x32x16_bf16 %0, %1, %2, %0" : "+a"(d) : "v"(a), "v"(b)); }
#define MFMA_DRAIN_V(x) asm volatile("s_nop 7\n\ts_nop 7" : "+v"(x))
#define MFMA_DRAIN_O(o) asm volatile("s_nop 7\n\ts_nop 7" : "+a"(o[0]), "+a"(o[1]), "+a"(o[2]), "+a"(o[3]), "+a"(o[4]), "+a"(o[5]), "+a"(o[6]), "+a"(o[7]))
DI float ard(float a) { float r; asm volatile("v_accvgpr_read_b32 %0, %1" : "=v"(r) : "a"(a)); return r; }
DI float opaque0() { float z; asm volatile("v_mov_b32 %0, 0" : "=v"(z)); return z; }
DI unsigned rfl(unsigned x) { return __builtin_amdgcn_readfirstlane(x); }
DI unsigned cvtpk(float lo, float hi) { f32x2 v = {lo, hi}; bf16x2_t b = __builtin_convertvector(v, bf16x2_t); return __builtin_bit_cast(unsigned, b); }
DI float bflo(unsigned w) { return __uint_as_float(w << 16); }
DI float bfhi(unsigned w) { return __uint_as_float(w & 0xffff0000u); }
DI u32x4 mksrd(const void* p, unsigned bytes) { u32x4 s = {(unsigned)(uintptr_t)p, (unsigned)((uintptr_t)p >> 32) & 0xffffu, bytes, 0x20000u}; return s; }
DI bf16x8 ldsr(unsigned a) { return *(const LAS bf16x8*)(uintptr_t)a; }
DI bf16x8 ldstr(unsigned a, unsigned a2) {
  s16x4 x = __builtin_amdgcn_ds_read_tr16_b64_v4i16((LAS s16x4*)(uintptr_t)a);
  s16x4 y = __builtin_amdgcn_ds_read_tr16_b64_v4i16((LAS s16x4*)(uintptr_t)a2);
  return __builtin_shufflevector(x, y, 0, 1, 2, 3, 4, 5, 6, 7);
}
DI void set_m0(unsigned v) { asm volatile("s_mov_b32 m0, %0" ::"s"(v) : "memory"); }
template <int OFF, int POL = 1> DI void dma16(unsigned voff, u32x4 srd, unsigned soff) {
  if constexpr (POL == 1) asm volatile("buffer_load_dwordx4 %0, %1, %2 offen offset:%3 sc1 lds" ::"v"(voff), "s"(srd), "s"(soff), "i"(OFF) : "memory");
  else if constexpr (POL == 2) asm volatile("buffer_load_dwordx4 %0, %1, %2 offen offset:%3 nt lds" ::"v"(voff), "s"(srd), "s"(soff), "i"(OFF) : "memory");
  else asm volatile("buffer_load_dwordx4 %0, %1, %2 offen offset:%3 lds" ::"v"(voff), "s"(srd), "s"(soff), "i"(OFF) : "memory");
}
DI void touch(const char* p) { asm volatile("global_load_dword a255, %0, off" ::"v"(p) : "memory", "a255"); }
#define LABEL(n) asm volatile(".Lgm_" #n ":" ::: "memory")
#define LOFF(n) ({ unsigned o_; asm volatile("s_mov_b32 %0, .Lgm_" #n "-.Lgm_base" : "=s"(o_)); o_; })
DI const char* code_line(const char* pc, unsigned from, unsigned to, unsigned line) { const unsigned o = from + line * 64; return pc + (o < to ? o : from); }
#define PINS(x) asm volatile("" : "+s"(x))
#define PINA(x) asm volatile("" : "+a"(x))
#define NOP0() asm volatile("s_nop 0" ::: "memory")
template <int V, bool L0> DI void wait_bar() {
  asm volatile("" ::: "memory");
  __builtin_amdgcn_s_waitcnt((V & 15) | ((V >> 4) << 14) | (7 << 4) | (L0 ? 0 : (15 << 8)));
  __builtin_amdgcn_s_barrier();
  asm volatile("" ::: "memory");
  SBAR();
}
DI bf16x8 pack8(const f32x16& a, int s, float sc) {
  u32x4 w = {cvtpk(a[8 * s] * sc, a[8 * s + 1] * sc), cvtpk(a[8 * s + 2] * sc, a[8 * s + 3] * sc), cvtpk(a[8 * s + 4] * sc, a[8 * s + 5] * sc), cvtpk(a[8 * s + 6] * sc, a[8 * s + 7] * sc)};
  return __builtin_bit_cast(bf16x8, w);
}
DI bf16x8 pack8(const f32x16& a, int s) {
  u32x4 w = {cvtpk(a[8 * s], a[8 * s + 1]), cvtpk(a[8 * s + 2], a[8 * s + 3]), cvtpk(a[8 * s + 4], a[8 * s + 5]), cvtpk(a[8 * s + 6], a[8 * s + 7])};
  return __builtin_bit_cast(bf16x8, w);
}
DI float swap_other(float v) {
  auto r = __builtin_amdgcn_permlane32_swap(__float_as_uint(v), __float_as_uint(v), false, false);
  return (threadIdx.x & 32) ? __uint_as_float(r[0]) : __uint_as_float(r[1]);
}

#define STAMP_DECL
#define STAMPIT(i)
#define STAMPRT(i)
#define STAMP_OUT(k, n)
#define TR_DECL
#define TR_ARG
#define TR_PASS
#define TRC()
#define TR_OUT()
__host__ __device__ constexpr bool wf_isa(int f) { return f < 14 || (f >= 126 && (f - 126) % 18 < 2); }
__host__ __device__ constexpr int wf_nb(int f) { return f < 14 ? f >> 1 : f < 126 ? (f - 14) % 7 : 7 + (f - 126) / 18; }
__host__ __device__ constexpr int wf_ks(int f) { return f < 14 ? f & 1 : f < 126 ? (f - 14) / 7 : (f - 126) % 18 < 2 ? (f - 126) % 18 : (f - 126) % 18 - 2; }
constexpr unsigned XRING = 65536;

#define WPOL 0
#define XPOL 0
struct Prov { u32x4 srd; unsigned voff, soff, pstr, kstr; };
DI void prov_next(Prov& p) { p.soff += p.kstr; }
struct K1State { unsigned xs, ws, xd; };

template <int Q> DI void k1_dma(Prov& p, unsigned m0b) { set_m0(m0b + Q * 4096); NOP0(); dma16<0, 1>(p.voff, p.srd, p.soff + Q * p.pstr); }

#define ST_AUX 16
DI void st16(__amdgpu_buffer_rsrc_t rs, unsigned voff, unsigned soff, bf16x8 v) {
  __builtin_amdgcn_raw_buffer_store_b128(__builtin_bit_cast(u32x4, v), rs, voff, soff, ST_AUX);
  asm volatile("s_nop 1" ::"v"(v) : "memory");
}

struct Sync { char* ws; unsigned token, lane; u32x4 fv;
  __amdgpu_buffer_rsrc_t rs; bf16x8 sq[24], wq[4]; unsigned sqe, sql;
  const char* pc; unsigned pfd[2];
};
DI void flag_set(char* p, unsigned token) { __hip_atomic_store((GAS unsigned*)p, token, __ATOMIC_RELAXED, __HIP_MEMORY_SCOPE_AGENT); }
DI unsigned flag_get(const char* p) { return __hip_atomic_load((GAS unsigned*)p, __ATOMIC_RELAXED, __HIP_MEMORY_SCOPE_AGENT); }
constexpr unsigned SPIN_CAP = 1u << 20;
DI void flag_wait(const char* p, unsigned token) {
#pragma unroll 1
  for (unsigned i = 0; i < SPIN_CAP; ++i) { if (__all(flag_get(p) == token)) break; __builtin_amdgcn_s_sleep(1); }
}
#define DRAIN() do { asm volatile("s_waitcnt vmcnt(0)" ::: "memory"); __builtin_amdgcn_s_waitcnt(0x0f70); } while (0)

template <bool SWAP, int NTL, bool FIRST, int QT>
DI void k1_ktile(f32x16 (&acc)[4][NTL], bf16x8 (&xa)[2][4], bf16x8 (&wf)[2][4], unsigned (&xr)[4], unsigned (&wr)[4], K1State& st, Prov& xp, Prov& wp,
                 const Prov& xn, const Prov& wn, const int t, const int NKT, const unsigned wid, Sync& Y) {
  constexpr int NM = 4 * NTL;
  sfor<4>([&](auto KS) __attribute__((always_inline)) { constexpr int ks = decltype(KS)::v, cur = ks & 1, nx = cur ^ 1, kn = (ks + 1) & 3;
    unsigned m0b;
    constexpr int VW = (FIRST && NTL == 3) ? 49 : (QT >= 1 && QT < 6) ? 20 : QT == 13 ? (ks == 1 ? 18 : 22) : QT > 13 ? 24 : 16;
    if constexpr (ks == 0) { if constexpr (NTL == 4 && QT == -1) { if (t == 7) DRAIN(); } m0b = (st.ws ^ 32768u) + 16384 + wid * 1024; }
    if constexpr (ks == 1) { wait_bar<VW, true>(); m0b = XRING + st.xd + wid * 1024 + 16384;
      if constexpr (NTL == 4 && QT == -1) { if (t == 7 && wid == 0) flag_set(Y.ws + WS_F1 + blockIdx.x * 4, Y.token); } }
    if constexpr (ks == 2) { if (t == NKT - 2) wp = wn; m0b = st.ws + wid * 1024; }
    if constexpr (ks == 3) {
      if constexpr (QT == 13) {
        if (wid == 0) { asm volatile("s_waitcnt vmcnt(18)" : "+v"(Y.fv));
          if (!__all(Y.fv[0] == Y.token && Y.fv[1] == Y.token && Y.fv[2] == Y.token && Y.fv[3] == Y.token))
#pragma unroll 1
            for (int i = 0; i < 4; ++i) flag_wait(Y.ws + WS_F1 + Y.lane * 16 + i * 4, Y.token); }
      }
      wait_bar<VW, true>();
      prov_next(xp); if (t == NKT - 3) xp = xn;
      st.xd = st.xs;
      m0b = XRING + st.xd + wid * 1024;
      const unsigned xs2 = st.xs == 65536 ? 0u : st.xs + 32768u, dx = xs2 - st.xs, dw = st.ws ? (unsigned)-32768 : 32768u;
      st.xs = xs2; st.ws ^= 32768u;
#pragma unroll
      for (int i = 0; i < 4; ++i) { xr[i] += dx; PIN(xr[i]); }
#pragma unroll
      for (int i = 0; i < NTL; ++i) { wr[i] += dw; PIN(wr[i]); }
    }
    Prov& pp = (ks & 1) ? xp : wp;
    unsigned so = pp.soff + (ks == 1 ? 4 * pp.pstr : 0), qso = 0;
    sfor<NM>([&](auto I) __attribute__((always_inline)) { constexpr int i = decltype(I)::v, m = i / NTL, n = i % NTL;
      if constexpr (FIRST && ks == 0) { const f32x16 z = {}; acc[m][n] = SWAP ? MFMA(wf[cur][n], xa[cur][m], z) : MFMA(xa[cur][m], wf[cur][n], z); }
      else acc[m][n] = SWAP ? MFMA(wf[cur][n], xa[cur][m], acc[m][n]) : MFMA(xa[cur][m], wf[cur][n], acc[m][n]);
      if constexpr (i == 0) xa[nx][0] = ldsr(xr[kn]);
      else if constexpr (i <= NTL) wf[nx][i - 1] = ldstr(wr[i - 1] + (kn & 1) * 8192 + (kn >> 1) * 16384, wr[i - 1] + (kn & 1) * 8192 + (kn >> 1) * 16384 + 2048);
      else if constexpr (i <= NTL + 3) xa[nx][i - NTL] = ldsr(xr[kn] + (i - NTL) * 4096);
      constexpr int D0 = NM - 8;
      if constexpr (QT >= 0 && QT < 6 && i == 5) { qso = __builtin_amdgcn_readlane(Y.sqe, 4 * QT + ks); PINS(qso); }
      if constexpr (QT >= 0 && QT < 6 && i == D0) st16(Y.rs, Y.lane * 16, qso, Y.sq[4 * QT + ks]);
      if constexpr (QT >= 13 && (i == 2 || i == 9)) { qso = __builtin_amdgcn_readlane(Y.sql, 8 * (QT - 13) + 2 * ks + (i == 9)); PINS(qso); }
      if constexpr (QT >= 13 && (i == 6 || i == 12)) st16(Y.rs, Y.lane * 16, qso, Y.sq[8 * (QT - 13) + 2 * ks + (i == 12)]);
      if constexpr (i >= D0) {
        if constexpr (((i - D0) & 1) == 0) { set_m0(m0b + ((i - D0) >> 1) * 4096); if constexpr (i > D0) so += pp.pstr; PINS(so); }
        else dma16<0, QT >= 13 ? 1 : (ks & 1) ? XPOL : WPOL>(pp.voff, pp.srd, so);
      }
      SBAR();
    });
    if constexpr (!(ks & 1)) prov_next(wp);
  });
}

template <bool SWAP, int NTL>
DI void k1_gemm(f32x16 (&acc)[4][NTL], K1State& st, Prov& xp, Prov& wp, const Prov& xn, const Prov& wn, const int NKT, const unsigned wid, const unsigned lane, Sync& Y) {
  const unsigned r = lane & 31, hh = lane >> 5, q = (lane >> 2) & 3, wrow = wid >> 1, wc = wid & 1;
  unsigned xr[4], wr[4];
#pragma unroll
  for (int ks = 0; ks < 4; ++ks) { xr[ks] = XRING + st.xs + (128 * wrow + r) * 128 + 16 * ((2 * ks + hh) ^ ((r >> 1) & 7)); PIN(xr[ks]); }
#pragma unroll
  for (int n = 0; n < NTL; ++n) { wr[n] = st.ws + (8 * hh + q) * 512 + 64 * ((NTL * wc + n) ^ q) + 32 * ((lane >> 4) & 1) + 8 * (lane & 3); PIN(wr[n]); }
  bf16x8 xa[2][4], wf[2][4];
#pragma unroll
  for (int m = 0; m < 4; ++m) xa[0][m] = ldsr(xr[0] + m * 4096);
#pragma unroll
  for (int n = 0; n < NTL; ++n) wf[0][n] = ldstr(wr[n], wr[n] + 2048);
  SBAR();
  constexpr int NP = NTL == 4 ? 6 : 1;
  sfor<NP>([&](auto T) __attribute__((always_inline)) { constexpr int t = decltype(T)::v; k1_ktile<SWAP, NTL, t == 0, NTL == 4 ? t : -1>(acc, xa, wf, xr, wr, st, xp, wp, xn, wn, t, NKT, wid, Y); });
  if constexpr (NTL == 4) {
    for (int t = NP; t < 13; ++t) k1_ktile<SWAP, NTL, false, -1>(acc, xa, wf, xr, wr, st, xp, wp, xn, wn, t, NKT, wid, Y);
    asm volatile("global_load_dword %0, %2, off\n\tglobal_load_dword %1, %3, off" : "=&v"(Y.pfd[0]), "=&v"(Y.pfd[1])
                 : "v"(code_line(Y.pc, LOFF(a), LOFF(b), wid * 64 + lane)), "v"(code_line(Y.pc, LOFF(a), LOFF(b), 256 + wid * 64 + lane)) : "memory");
    if (wid == 0) asm volatile("global_load_dwordx4 %0, %1, off sc1" : "=v"(Y.fv) : "v"(Y.ws + WS_F1 + Y.lane * 16) : "memory");
    sfor<3>([&](auto T) __attribute__((always_inline)) { constexpr int t = 13 + decltype(T)::v; k1_ktile<SWAP, NTL, false, t>(acc, xa, wf, xr, wr, st, xp, wp, xn, wn, t, 16, wid, Y); });
    asm volatile("" ::"v"(Y.pfd[0]), "v"(Y.pfd[1]));
  } else
    for (int t = NP; t < NKT; ++t) k1_ktile<SWAP, NTL, false, -1>(acc, xa, wf, xr, wr, st, xp, wp, xn, wn, t, NKT, wid, Y);
}

DI Prov mk_xprov(const char* X, unsigned ld, unsigned bytes, unsigned rb, unsigned wid, unsigned lane) {
  Prov p; p.srd = mksrd(X, bytes);
  p.voff = (lane >> 3) * ld + 16 * ((lane & 7) ^ (4 * (wid & 1) + (lane >> 4)));
  p.soff = (256 * rb + 8 * wid) * ld; p.pstr = 32 * ld; p.kstr = 128; return p;
}
template <class F> DI Prov mk_wprov(const char* W, unsigned ld, unsigned bytes, unsigned wid, unsigned lane, F colf) {
  Prov p; p.srd = mksrd(W, bytes);
  const unsigned ch = (lane & 31) ^ (((2 * (wid & 1) + (lane >> 5)) & 3) << 2), col = colf(ch);
  p.voff = col < 0x10000u ? (lane >> 5) * ld + 2 * col : 0x7ffffff0u;
  p.soff = 2 * wid * ld; p.pstr = 8 * ld; p.kstr = 32 * ld; return p;
}
DI void proj_phase(f32x16 (&acc4)[4][4], const char* x, const char* wqkv, const char* ckv, const char* wq, const char* wuk, const char* wuv, const char* gate_w,
                   const char* wo_a, const char* wo_b, char* ws, const unsigned kva_off, Sync& Y, const unsigned lane, const unsigned wid TR_ARG) {
  const unsigned tid = threadIdx.x, r = lane & 31, hh = lane >> 5, wrow = wid >> 1, wc = wid & 1, lane16 = lane * 16;
  const unsigned bid = blockIdx.x, xcd = bid & 7, loc = bid >> 3, rb = loc & 7, sel = loc >> 3;
  const unsigned cb = xcd + 8 * sel;
  const unsigned head2 = 2 * xcd + (sel & 1), isV = sel >> 1;
  const Prov xkv = mk_xprov(ckv, 2048, 2048 * 2048, rb, wid, lane);
  const Prov wkv = mk_wprov(isV ? wuv : wuk, 8192, 1024 * 8192, wid, lane, [&](unsigned ch) { return head2 * 256 + 8 * ch; });
  Prov xp = cb < 31 ? mk_xprov(x, 896, 2048 * 896, rb, wid, lane) : xkv;
  Prov wp = cb < 22 ? mk_wprov(wq, 8192, 448 * 8192, wid, lane, [&](unsigned ch) { return ch < 24 ? cb * 192 + 8 * ch : 0x10000u; })
          : cb < 31 ? mk_wprov(wqkv, 2688, 448 * 2688, wid, lane, [&](unsigned ch) { const unsigned vc = (cb < 28 ? cb - 22 : cb - 28) * 24 + ch, slot = vc >> 2;
                         return (ch < 24 && slot < (cb < 28 ? 32u : 16u)) ? (cb < 28 ? 0u : 896u) + 28 * slot + 8 * (vc & 3) : 0x10000u; })
          : wkv;
  K1State st = cb < 31 ? K1State{32768u, 32768u, 0u} : K1State{65536u, 0u, 32768u};
  {
    const unsigned x0 = XRING + st.xs + wid * 1024, x1 = XRING + (st.xs == 65536u ? 0u : st.xs + 32768u) + wid * 1024, x2 = XRING + st.xd + wid * 1024, w0 = st.ws + wid * 1024, w2 = (st.ws ^ 32768u) + wid * 1024;
    k1_dma<0>(xp, x0); k1_dma<1>(xp, x0); k1_dma<2>(xp, x0); k1_dma<3>(xp, x0); k1_dma<4>(xp, x0); k1_dma<5>(xp, x0); k1_dma<6>(xp, x0); k1_dma<7>(xp, x0); prov_next(xp);
    k1_dma<0>(wp, w0); k1_dma<1>(wp, w0); k1_dma<2>(wp, w0); k1_dma<3>(wp, w0); prov_next(wp);
    k1_dma<0>(xp, x1); k1_dma<1>(xp, x1); k1_dma<2>(xp, x1); k1_dma<3>(xp, x1);
    k1_dma<0>(wp, w0 + 16384); k1_dma<1>(wp, w0 + 16384); k1_dma<2>(wp, w0 + 16384); k1_dma<3>(wp, w0 + 16384); prov_next(wp);
    k1_dma<4>(xp, x1); k1_dma<5>(xp, x1); k1_dma<6>(xp, x1); k1_dma<7>(xp, x1); prov_next(xp);
    k1_dma<0>(wp, w2); k1_dma<1>(wp, w2); k1_dma<2>(wp, w2); k1_dma<3>(wp, w2); prov_next(wp);
    k1_dma<0>(xp, x2); k1_dma<1>(xp, x2); k1_dma<2>(xp, x2); k1_dma<3>(xp, x2);
  }
  const unsigned gw = bid * 4 + wid, hd = gw >> 6;
  unsigned short v[4][8];
#pragma unroll
  for (int i = 0; i < 4; ++i) { int f = (gw & 63) * 4 + i; f = f < 252 ? f : 251;
    const unsigned nb = wf_nb(f), ks = wf_ks(f); const bool isa = wf_isa(f);
    const char* src = (isa ? wo_a + hd * 28 * 896 : wo_b + hd * 256 * 896) + (32 * nb + r) * 2;
#pragma unroll
    for (int j = 0; j < 8; ++j) { unsigned d = 16 * ks + 8 * (j >> 2) + 4 * hh + (j & 3); if (isa && d >= 28) d = 0; v[i][j] = *(const unsigned short*)(src + d * 896); } }
  touch(code_line(Y.pc, 0, LOFF(end), (rb * 4 + wid) * 64 + lane));
  wait_bar<53, true>();
  TRC();
  const unsigned tb0 = rb * 8 + wrow * 4;
  {
    const unsigned n = lane >> 3, m = (lane >> 1) & 3, s = lane & 1, c32 = (cb < 22 ? cb : cb < 28 ? cb - 22 : cb - 28) * 6 + wc * 3 + n;
    unsigned d = cb < 22 ? (unsigned)WS_QB + (((c32 >> 3) * 64 + tb0 + m) * 16 + (c32 & 7) * 2 + s) * 1024
               : cb >= 28 ? (unsigned)WS_KVA + (c32 * 64 + tb0 + m) * 4096 + 2048 + s * 1024
               : c32 < 16 ? (unsigned)WS_QA + (c32 * 64 + tb0 + m) * 2048 + s * 1024 : (unsigned)WS_KVA + ((c32 - 16) * 64 + tb0 + m) * 4096 + s * 1024;
    if (cb == 31 || c32 >= (cb < 22 ? 128u : cb < 28 ? 32u : 16u)) d = 0x7ffffff0u;
    Y.sqe = cb >= 22 ? d : 0x7ffffff0u; Y.sql = cb < 22 ? d : 0x7ffffff0u;
  }
  if (cb < 31) {
    f32x16 acc[4][3];
    if (cb < 28) {
      k1_gemm<true, 3>(acc, st, xp, wp, xkv, wkv, 7, wid, lane, Y);
      TRC();
      const bool pad = cb >= 22 && hh;
#pragma unroll
      for (int n = 0; n < 3; ++n) { const float sc = cb < 22 ? QSCALE_B : (cb - 22) * 6 + wc * 3 + n < 16 ? QSCALE_A : 1.f;
#pragma unroll
        for (int m = 0; m < 4; ++m) { PINA(acc[m][n]); u32x4 w1 = __builtin_bit_cast(u32x4, pack8(acc[m][n], 1, sc)); if (pad) { w1[2] = 0u; w1[3] = 0u; }
          Y.sq[8 * n + 2 * m] = pack8(acc[m][n], 0, sc); Y.sq[8 * n + 2 * m + 1] = __builtin_bit_cast(bf16x8, w1); PIN(Y.sq[8 * n + 2 * m]); PIN(Y.sq[8 * n + 2 * m + 1]); SBAR(); } }
    } else {
      k1_gemm<false, 3>(acc, st, xp, wp, xkv, wkv, 7, wid, lane, Y);
      TRC();
      const unsigned fill = r == 28 ? 0x3f803f80u : 0u;
#pragma unroll
      for (int n = 0; n < 3; ++n)
#pragma unroll
        for (int m = 0; m < 4; ++m) { PINA(acc[m][n]);
#pragma unroll
          for (int s = 0; s < 2; ++s) { u32x4 w = __builtin_bit_cast(u32x4, pack8(acc[m][n], s)); if (r >= 28) w = (u32x4){fill, fill, fill, fill};
            Y.sq[8 * n + 2 * m + s] = __builtin_bit_cast(bf16x8, w); PIN(Y.sq[8 * n + 2 * m + s]); }
          SBAR(); }
    }
  } else {
    const unsigned row = rb * 256 + tid;
    const u32x4* xr_ = (const u32x4*)(x + row * 896); const u32x4* gw_ = (const u32x4*)gate_w;
    float s = 0.f;
#pragma unroll 14
    for (int i = 0; i < 56; ++i) { const u32x4 a = xr_[i], b = gw_[i];
#pragma unroll
      for (int e = 0; e < 4; ++e) { s = fmaf(bflo(a[e]), bflo(b[e]), s); s = fmaf(bfhi(a[e]), bfhi(b[e]), s); } }
    flag_set(ws + WS_G + row * 4, __float_as_uint(1.f / (1.f + __expf(-s))));
#pragma unroll
    for (int e = 0; e < 24; ++e) { Y.sq[e] = bf16x8{}; PIN(Y.sq[e]); }
    TRC();
  }
#pragma unroll
  for (int i = 0; i < 4; ++i) { u32x4 w;
#pragma unroll
    for (int e = 0; e < 4; ++e) w[e] = (unsigned)v[i][2 * e] | ((unsigned)v[i][2 * e + 1] << 16);
    Y.wq[i] = __builtin_bit_cast(bf16x8, w); PIN(Y.wq[i]); }
  Y.token = rfl(Y.token);
  TRC();
  Prov xa_, wa_; xa_.srd = mksrd(ws, (unsigned)WS_END); xa_.voff = lane16; xa_.soff = kva_off + wid * 1024; xa_.pstr = 4096; xa_.kstr = 32768;
  wa_ = xa_; wa_.soff += 3 * 32768; wa_.kstr = 16384;
  if (!isV) k1_gemm<true, 4>(acc4, st, xp, wp, xa_, wa_, 16, wid, lane, Y);
  else k1_gemm<false, 4>(acc4, st, xp, wp, xa_, wa_, 16, wid, lane, Y);
  const unsigned mw = (st.ws ^ 32768u) + 16384 + wid * 1024, mx = XRING + st.xd + wid * 1024;
  k1_dma<0>(wp, mw); k1_dma<1>(wp, mw); k1_dma<2>(wp, mw); k1_dma<3>(wp, mw); k1_dma<4>(xp, mx); k1_dma<5>(xp, mx); k1_dma<6>(xp, mx); k1_dma<7>(xp, mx);
}

struct Desc { unsigned so, m0; };
struct Stream {
  u32x4 srd; unsigned tab, lane16;
  unsigned dseq, dslot, rslot, so;
  Desc y;
};
DI unsigned nslot(unsigned s) { return s >= 131072u ? s - 131072u : s + 32768u; }
DI void st_prep(Stream& S, Desc& D) { D.so = __builtin_amdgcn_readlane(S.tab, S.dseq); PINS(D.so); D.m0 = S.dslot; S.dslot = nslot(S.dslot); S.dseq += 1; PINS(S.dslot); }
template <int Q> DI void st_pre(Stream& S, const Desc& D) {
  if constexpr (Q == 0) { set_m0(D.m0); S.so = D.so; } else if constexpr (Q == 4) { set_m0(D.m0 + 4096u); S.so = D.so + 4096u; PINS(S.so); }
}
template <int Q> DI void st_go(Stream& S) { dma16<(Q & 3) * 1024, 1>(S.lane16, S.srd, S.so); }
DI void st_item(Stream& S) {
  sfor<8>([&](auto Q) __attribute__((always_inline)) { st_pre<decltype(Q)::v>(S, S.y); if constexpr (!(decltype(Q)::v & 3)) NOP0(); st_go<decltype(Q)::v>(S); });
  st_prep(S, S.y); asm volatile("s_nop 4" ::: "memory");
}
DI float maskv(float v, int dq, int rr) { return dq >= ((rr & 3) + 8 * (rr >> 2)) ? v : -__builtin_inff(); }
DI float max16(const f32x16& s) {
  float a = fmaxf(fmaxf(s[0], s[1]), s[2]), b = fmaxf(fmaxf(s[3], s[4]), s[5]);
  a = fmaxf(fmaxf(a, s[6]), s[7]); b = fmaxf(fmaxf(b, s[8]), s[9]); a = fmaxf(fmaxf(a, s[10]), s[11]); b = fmaxf(fmaxf(b, s[12]), s[13]);
  a = fmaxf(fmaxf(a, s[14]), s[15]); a = fmaxf(a, b);
  return fmaxf(a, swap_other(a));
}
DI float max3f(float a, float b, float c) { float r; asm("v_max3_f32 %0, %1, %2, %3" : "=v"(r) : "v"(a), "v"(b), "v"(c)); return r; }
DI f32x16 splat16(float v) { f32x16 r; for (int i = 0; i < 16; ++i) r[i] = v; return r; }
DI void open_item(Stream& S, unsigned& rb_, unsigned off) { rb_ = S.rslot + off; PIN(rb_); S.rslot = nslot(S.rslot); PINS(S.rslot); }
DI void unopen_item(Stream& S) { S.rslot = S.rslot == 0u ? 131072u : S.rslot - 32768u; }
template <int N> DI void wait_lgkm() { __builtin_amdgcn_s_waitcnt(15 | (3 << 14) | (7 << 4) | (N << 8)); }
DI void set_c(f32x16& c0, f32x16& c1, float v, int dq) {
#pragma unroll
  for (int e = 0; e < 16; ++e) { c0[e] = maskv(v, dq, e); c1[e] = maskv(v, dq - 32, e); }
  PIN(c0); PIN(c1); asm volatile("s_nop 1" ::: "memory");
}

template <bool FIRST>
DI void b_tile(f32x16 (&o)[8], const bf16x8 (&q)[16], bf16x8 (&fr)[8], f32x16& c0, f32x16& c1, float& mref, float& rs0, float& rs1, Stream& S, unsigned& rb_) {
  f32x16 s0, s1; u32x4 p[4]; float pe[32], ma, mb_; Desc X;
  constexpr int E0 = FIRST ? 22 : 18;
  sfor<64>([&](auto J) __attribute__((always_inline)) { constexpr int j = decltype(J)::v;
    if constexpr (j == 24) { wait_bar<16, false>(); open_item(S, rb_, S.lane16); }
    if constexpr (j == 56) { wait_bar<11, false>(); open_item(S, rb_, S.lane16); }
    if constexpr ((j & 3) == 0) wait_lgkm<4>();
    if constexpr (j == 0) mfma_vc(s0, fr[0], q[0], c0);
    else if constexpr (j < 16) mfma_v(s0, fr[j & 7], q[j]);
    else if constexpr (j == 16) mfma_vc(s1, fr[0], q[0], c1);
    else if constexpr (j < 32) mfma_v(s1, fr[j & 7], q[j - 16]);
    else if constexpr (FIRST && j < 40) asm volatile("v_mfma_f32_32x32x16_bf16 %0, %1, %2, 0" : "=&a"(o[j & 7]) : "v"(fr[j & 7]), "v"(p[0]));
    else mfma_a(o[j & 7], fr[j & 7], __builtin_bit_cast(bf16x8, p[(j - 32) >> 3]));
    fr[j & 7] = ldsr(rb_ + ((j + 8) & 31) * 1024);
    if constexpr (j < 16 && (j & 1)) st_go<(j >> 1)>(S);
    if constexpr (j == 51 || j == 53 || j == 55 || j == 57 || j == 59 || j == 61) st_go<((j - 51) >> 1)>(S);
    if constexpr (j == 62) st_go<6>(S);
    if constexpr (j == 63) st_go<7>(S);
    if constexpr (FIRST) {
      if constexpr (j == 18) { ma = max3f(s0[0], s0[1], s0[2]); mb_ = max3f(s0[3], s0[4], s0[5]); ma = max3f(ma, s0[6], s0[7]); mb_ = max3f(mb_, s0[8], s0[9]); PIN(ma); PIN(mb_); }
      if constexpr (j == 19) { ma = max3f(ma, s0[10], s0[11]); mb_ = max3f(mb_, s0[12], s0[13]); ma = max3f(ma, s0[14], s0[15]); ma = fmaxf(ma, mb_); PIN(ma); }
      if constexpr (j == 20) { ma = fmaxf(ma, swap_other(ma)); mref = ma == -__builtin_inff() ? 0.f : ma; PIN(mref); }
      if constexpr (j >= 56) { constexpr int e = 2 * (j - 56); float v = -mref; c0[e] = v; c0[e + 1] = v; c1[e] = v; c1[e + 1] = v; if constexpr (j == 63) { PIN(c0); PIN(c1); } }
    }
    if constexpr (j >= E0 && j < E0 + 32) { constexpr int e = j - E0;
      float v = e < 16 ? s0[e & 15] : s1[e & 15]; if constexpr (FIRST) v -= mref; v = __builtin_amdgcn_exp2f(v); PIN(v); pe[e] = v; }
    if constexpr (j >= E0 + 1 && j < E0 + 33) { constexpr int e = j - E0 - 1;
      if constexpr (e & 1) { rs1 += pe[e]; PIN(rs1); unsigned w = cvtpk(pe[e - 1], pe[e]); PIN(w); p[e >> 3][(e >> 1) & 3] = w; }
      else { rs0 += pe[e]; PIN(rs0); } }
    if constexpr (j == 0) st_pre<0>(S, S.y);
    if constexpr (j == 8) st_pre<4>(S, S.y);
    if constexpr (j == 16) st_prep(S, X);
    if constexpr (j == 17) st_prep(S, S.y);
    if constexpr (j == 50) st_pre<0>(S, X);
    if constexpr (j == 58) st_pre<4>(S, X);
    SBAR();
  });
}

DI void hand_step(const char* fh, const char* ml, const unsigned token, const int left, bool& late) {
  if (left == 2) {
    unsigned fw; asm volatile("v_accvgpr_read_b32 %0, a254" : "=v"(fw));
    late = rfl(fw) != token;
    asm volatile("global_load_dwordx2 a[252:253], %0, off sc1" ::"v"(ml) : "memory", "a252", "a253");
  }
  if (left == 3) asm volatile("global_load_dword a254, %0, off sc1" ::"v"(fh) : "memory", "a254");
}

DI void b_segment(f32x16 (&o)[8], const bf16x8 (&q)[16], float& mref, float& lsum, Stream& S, const int qb, const int t0, const int t1,
                  const unsigned lane, const unsigned wid, const char* fh, const char* ml, const unsigned token, const bool consume, float& mb, float& lb, bool& late, const unsigned pj TR_ARG) {
  TRC();
  const int qrow = qb * 128 + (int)wid * 32 + (int)(lane & 31) - 4 * (int)(lane >> 5);
  unsigned rb_; open_item(S, rb_, S.lane16);
  bf16x8 fr[8];
#pragma unroll
  for (int j = 0; j < 8; ++j) fr[j] = ldsr(rb_ + j * 1024);
  f32x16 c0, c1; float rs0 = 0.f, rs1 = 0.f;
  set_c(c0, c1, 0.f, qrow - t0 * 64);
  SBAR();
  b_tile<true>(o, q, fr, c0, c1, mref, rs0, rs1, S, rb_);
  const int tc = consume ? t1 : -8;
  for (int t = t0 + 1; t < t1; ++t) {
    TRC();
    hand_step(fh, ml, token, tc - t, late);
    if (tc - t == 2 && late) S.tab = lane - pj < 2u ? 0x7ffffff0u : S.tab;
    if (t >= 2 * qb) set_c(c0, c1, -mref, qrow - t * 64);
    b_tile<false>(o, q, fr, c0, c1, mref, rs0, rs1, S, rb_);
  }
  SBAR();
  asm volatile("v_accvgpr_read_b32 %0, a252\n\tv_accvgpr_read_b32 %1, a253" : "=v"(mb), "=v"(lb));
  MFMA_DRAIN_O(o); SBAR(); TRC();
  unopen_item(S);
  const float l = rs0 + rs1; lsum = l + swap_other(l);
}

DI void a_finish(bf16x8 (&ca)[2], f32x16 o, const float g, const unsigned lane) {
  const bool up = lane >= 32;
  const float lo = swap_other(o[12]), l = up ? o[12] : lo;
  const float sc = g / l + l * 0.f;
#pragma unroll
  for (int e = 12; e < 16; ++e) o[e] = up ? 0.f : o[e];
  ca[0] = pack8(o, 0, sc); ca[1] = pack8(o, 1, sc);
}
DI bf16x8 ld16(__amdgpu_buffer_rsrc_t rs, unsigned voff, unsigned soff) { return __builtin_bit_cast(bf16x8, __builtin_amdgcn_raw_buffer_load_b128(rs, voff, soff, 16)); }
struct KvOut { __amdgpu_buffer_rsrc_t rs; unsigned dst, ns, ss, wof; };
DI void a_phase(bf16x8 (&ca)[2], bf16x8 (&q)[16], const unsigned qoff, const f32x16 (&acc)[4][4], const bf16x8 (&wq)[4], const KvOut& KO, Stream& S, const bf16x8 q0, const bf16x8 q1, const float g, const unsigned lane,
                char* f2_mine, const char* f2_need, const unsigned token, const char* pc, const unsigned line, const unsigned wid TR_ARG) {
  TRC();
  unsigned rb_, rn_; open_item(S, rb_, S.lane16); rn_ = rb_;
  bf16x8 kf[2][2], va[2], vb[2];
  kf[0][0] = ldsr(rb_); kf[0][1] = ldsr(rb_ + 1024); kf[1][0] = ldsr(rb_ + 4096); kf[1][1] = ldsr(rb_ + 5120);
  va[0] = ldsr(rb_ + 2048); vb[0] = ldsr(rb_ + 3072); va[1] = ldsr(rb_ + 4096 + 2048); vb[1] = vb[0];
  f32x16 s[2], o = {}, negm;
  { f32x16 z; asm volatile("v_mfma_f32_32x32x16_bf16 %0, %1, %2, 0" : "=&v"(z) : "v"(kf[0][0]), "v"(q0)); mfma_v(z, kf[0][1], q1); MFMA_DRAIN_V(z);
    negm = splat16(-max16(z)); PIN(negm); }
  u32x4 p0, p1 = {0u, 0u, 0u, 0u}, ow; float pt[4] = {0.f, 0.f, 0.f, 0.f}; unsigned fw;
  PIN(o); asm volatile("s_nop 1" ::: "memory");
  mfma_vc(s[0], kf[0][0], q0, negm); mfma_v(s[0], kf[0][1], q1);
  kf[0][0] = ldsr(rb_ + 8192); kf[0][1] = ldsr(rb_ + 9216);
  MFMA_DRAIN_V(s[0]);
  SBAR();
  sfor<64>([&](auto ST) __attribute__((always_inline)) { constexpr int st = decltype(ST)::v, it = st >> 3, kb = st & 7, c = kb & 1, n = c ^ 1;
#define AADDR(blk, off) ((blk) < 8 ? rb_ + (blk) * 4096 + (off) : rn_ + ((blk) - 8) * 4096 + (off))
#define AEXP(e) do { pe[e] = __builtin_amdgcn_exp2f(s[c][e]); PIN(pe[e]); } while (0)
#define ACVT(P, i, a, b) do { unsigned w_ = cvtpk(a, b); PIN(w_); P[i] = w_; } while (0)
    constexpr int se = (4 * st + 4) / 5, has_st = se < 32 && (5 * se) / 4 == st, sm = se >> 3, sn = (se >> 1) & 3, sh = se & 1;
    constexpr bool rq = it >= 2 && it <= 4 && kb < 4;
    constexpr int rb0 = st >= 53 && st < 61 ? 3 * (st - 53) : -1;
    if constexpr (kb == 0) { TRC(); rb_ = rn_; }
    if constexpr (st == 53) { if (wid == 0) { DRAIN(); asm volatile("" : "+v"(fw)); if (!__all(fw == token)) flag_wait(f2_need, token); } }
    if constexpr (kb == 5) { wait_bar<it == 4 ? 24 : it == 7 ? 24 : 63, false>(); open_item(S, rn_, S.lane16); }
    if constexpr (st == 42) DRAIN();
    if constexpr (st == 45) { if (wid == 0) flag_set(f2_mine, token); }
#define BPRE(p) do { if constexpr (((p) & 3) == 0) { st_pre<(p) & 4>(S, S.y); NOP0(); } } while (0)
#define BGO(p) do { st_go<(p) & 7>(S); if constexpr (((p) & 7) == 7) st_prep(S, S.y); } while (0)
    float pe[16];
    if constexpr (st < 62) wait_lgkm<4>();
    if constexpr (st < 63) mfma_vc(s[n], kf[n][0], q0, negm);
    if constexpr (st < 61) kf[n][0] = ldsr(AADDR(kb + 3, 0));
    if constexpr (rb0 >= 0) { BPRE(rb0); BGO(rb0); q[2 * (st - 53)] = ld16(KO.rs, S.lane16, qoff + 2 * (st - 53) * 1024); }
    SBAR();
    AEXP(0); AEXP(1); ACVT(p1, 2, pt[0], pt[1]); AEXP(2); AEXP(3); ACVT(p1, 3, pt[2], pt[3]);
    if constexpr (has_st) { ACVT(ow, 0, ard(acc[sm][sn][8 * sh]), ard(acc[sm][sn][8 * sh + 1])); ACVT(ow, 1, ard(acc[sm][sn][8 * sh + 2]), ard(acc[sm][sn][8 * sh + 3])); }
    if constexpr (rq && (kb & 1) == 0) st_pre<(kb & 2) * 2>(S, S.y);
    SBAR();
    if constexpr (st < 63) mfma_v(s[n], kf[n][1], q1);
    if constexpr (st < 61) kf[n][1] = ldsr(AADDR(kb + 3, 1024));
    if constexpr (rq) st_go<2 * (kb & 3)>(S);
    if constexpr (st == 41) touch(code_line(pc, LOFF(b), LOFF(c), line));
    if constexpr (rb0 >= 0) { BPRE(rb0 + 1); BGO(rb0 + 1); }
    if constexpr (st == 49) { if (wid == 0) asm volatile("global_load_dword %0, %1, off sc1" : "=v"(fw) : "v"(f2_need) : "memory"); }
    SBAR();
    AEXP(4); AEXP(5); ACVT(p0, 0, pe[0], pe[1]); AEXP(6); AEXP(7); ACVT(p0, 1, pe[2], pe[3]);
    if constexpr (has_st) { ACVT(ow, 2, ard(acc[sm][sn][8 * sh + 4]), ard(acc[sm][sn][8 * sh + 5])); ACVT(ow, 3, ard(acc[sm][sn][8 * sh + 6]), ard(acc[sm][sn][8 * sh + 7])); }
    SBAR();
    mfma_v(o, vb[n], __builtin_bit_cast(bf16x8, p1));
    if constexpr (st < 63) vb[n] = ldsr(AADDR(kb + 1, 3072));
    if constexpr (has_st) st16(KO.rs, S.lane16, KO.dst + sm * 16384 + sn * KO.ns + sh * KO.ss, __builtin_bit_cast(bf16x8, ow));
    if constexpr (st < 20 && !has_st) st16(KO.rs, S.lane16, KO.wof + (st / 5) * 1024, wq[st / 5]);
    if constexpr (rb0 >= 0) q[2 * (st - 53) + 1] = ld16(KO.rs, S.lane16, qoff + (2 * (st - 53) + 1) * 1024);
    SBAR();
    AEXP(8); ACVT(p0, 2, pe[4], pe[5]); AEXP(9); ACVT(p0, 3, pe[6], pe[7]); SBAR(); AEXP(10); AEXP(11);
    SBAR();
    mfma_v(o, va[c], __builtin_bit_cast(bf16x8, p0));
    if constexpr (st < 62) va[c] = ldsr(AADDR(kb + 2, 2048));
    if constexpr (rq) st_go<2 * (kb & 3) + 1>(S);
    if constexpr (rb0 >= 0) { BPRE(rb0 + 2); BGO(rb0 + 2); }
    SBAR();
    AEXP(12); AEXP(13); ACVT(p1, 0, pe[8], pe[9]); AEXP(14); AEXP(15); ACVT(p1, 1, pe[10], pe[11]);
    pt[0] = pe[12]; pt[1] = pe[13]; pt[2] = pe[14]; pt[3] = pe[15];
    if constexpr (rq && (kb & 3) == 3) st_prep(S, S.y);
    SBAR();
#undef AADDR
#undef AEXP
#undef ACVT
#undef BPRE
#undef BGO
  });
  asm volatile("" ::"v"(negm));
  DRAIN();
  p1[2] = cvtpk(pt[0], pt[1]); p1[3] = cvtpk(pt[2], pt[3]);
  mfma_vn(o, vb[1], __builtin_bit_cast(bf16x8, p1));
  MFMA_DRAIN_V(o);
  unopen_item(S);
  a_finish(ca, o, g, lane);
}

template <bool MERGE>
DI float out_proj(f32x16 (&o)[8], const bf16x8 (&ca)[2], const float fa, const float fb, Stream& S, const __amdgpu_buffer_rsrc_t rs, const unsigned og, const unsigned wid TR_ARG) {
  u32x4 pb[16], cb[16], ow; bf16x8 fr[8]; f32x16 acc[14]; unsigned rb_;
  if constexpr (MERGE) {
    open_item(S, rb_, wid * 8192 + S.lane16);
#pragma unroll
    for (int c = 0; c < 8; ++c) pb[c] = __builtin_bit_cast(u32x4, ldsr(rb_ + c * 1024));
    wait_bar<8, false>(); open_item(S, rb_, wid * 8192 + S.lane16);
#pragma unroll
    for (int c = 0; c < 8; ++c) pb[8 + c] = __builtin_bit_cast(u32x4, ldsr(rb_ + c * 1024));
    wait_bar<0, false>();
  }
  open_item(S, rb_, S.lane16);
#pragma unroll
  for (int j = 0; j < 8; ++j) fr[j] = ldsr(rb_ + j * 1024);
  SBAR();
  Desc X;
  sfor<252>([&](auto F) __attribute__((always_inline)) { constexpr int f = decltype(F)::v, f8 = f + 8, w = f >> 5, ph = f & 31;
    constexpr int nb = wf_nb(f), ks = wf_ks(f);
    if constexpr (ph == 24 && w < 7) { TRC();
      wait_bar<(MERGE && w == 0) ? 4 : (MERGE && w == 1) ? 12 : w < 5 ? 16 : 8, false>(); open_item(S, rb_, S.lane16); }
    if constexpr ((f & 3) == 0) wait_lgkm<4>();
    if constexpr (wf_isa(f)) { if constexpr (ks == 0) { const f32x16 z = {}; acc[nb] = MFMA(fr[f & 7], ca[0], z); } else acc[nb] = MFMA(fr[f & 7], ca[1], acc[nb]); }
    else acc[nb] = MFMA(fr[f & 7], __builtin_bit_cast(bf16x8, cb[ks]), acc[nb]);
    if constexpr (f8 < 252) fr[f & 7] = ldsr(rb_ + (f8 & 31) * 1024);
    constexpr bool r1 = MERGE ? w < 5 : w < 5, r2 = MERGE && w < 2;
    if constexpr (r1 && ph < 16 && (ph & 1)) st_go<(ph >> 1)>(S);
    if constexpr (r2 && ph >= 16 && (ph & 1)) st_go<((ph - 16) >> 1)>(S);
    if constexpr (r1 && (ph == 0 || ph == 8)) st_pre<ph / 2>(S, S.y);
    if constexpr (r2 && ph == 2) st_prep(S, X);
    if constexpr (r2 && (ph == 16 || ph == 24)) st_pre<(ph - 16) / 2>(S, X);
    if constexpr (r1 && ph == 18) st_prep(S, S.y);
    if constexpr (f < 96 && f % 6 < 4) { constexpr int c = f / 6, e = f % 6; const float x0 = ard(o[c >> 1][8 * (c & 1) + 2 * e]), x1 = ard(o[c >> 1][8 * (c & 1) + 2 * e + 1]);
      unsigned v = MERGE ? cvtpk(fmaf(x0, fa, bflo(pb[c][e]) * fb), fmaf(x1, fa, bfhi(pb[c][e]) * fb)) : cvtpk(x0 * fa, x1 * fa); PIN(v); cb[c][e] = v; }
    if constexpr (f >= 128 && f < 245 && (f - 128) % 9 < 8) { constexpr int t = (f - 128) / 9, u = (f - 128) % 9;
      unsigned v = cvtpk(ard(acc[t][2 * u]), ard(acc[t][2 * u + 1])); PIN(v); ow[u & 3] = v;
      if constexpr ((u & 3) == 3) st16(rs, S.lane16, og + (t * 2 + (u >> 2)) * 1024, __builtin_bit_cast(bf16x8, ow)); }
    SBAR();
  });
  st16(rs, S.lane16, og + 26 * 1024, pack8(acc[13], 0)); st16(rs, S.lane16, og + 27 * 1024, pack8(acc[13], 1));
  return acc[13][0];
}

DI void safe_redo(const __amdgpu_buffer_rsrc_t rs, const unsigned og, const unsigned head, const unsigned tb, const float g, const unsigned lane) {
  const unsigned lane16 = lane * 16;
  bf16x8 ca[2], cb[16];
  {
    const unsigned qa = (unsigned)WS_QA + (head * 64 + tb) * 2048; const bf16x8 q0 = ld16(rs, lane16, qa), q1 = ld16(rs, lane16, qa + 1024);
    float m = -1e30f; f32x16 o = splat16(opaque0());
    for (int kb = 0; kb < 64; ++kb) {
      const unsigned p = (unsigned)WS_KVA + (head * 64 + kb) * 4096;
      f32x16 t = {}; t = MFMA(ld16(rs, lane16, p), q0, t); t = MFMA(ld16(rs, lane16, p + 1024), q1, t);
      const float mn = fmaxf(m, max16(t)), al = __builtin_amdgcn_exp2f(m - mn); m = mn;
#pragma unroll
      for (int e = 0; e < 16; ++e) t[e] = __builtin_amdgcn_exp2f(t[e] - mn);
      o *= al; o = MFMA(ld16(rs, lane16, p + 2048), pack8(t, 0), o); o = MFMA(ld16(rs, lane16, p + 3072), pack8(t, 1), o);
    }
    a_finish(ca, o, g, lane);
  }
  {
    const unsigned qg = (unsigned)WS_QB + (head * 64 + tb) * 16384;
    const int qrow = (int)tb * 32 + (int)(lane & 31) - 4 * (int)(lane >> 5);
    float m = -1e30f, ls = 0.f; f32x16 o[8];
#pragma unroll
    for (int d = 0; d < 8; ++d) o[d] = splat16(opaque0());
    for (int kb = 0; kb <= (int)tb; ++kb) {
      const unsigned kp = (unsigned)WS_KB + (head * 64 + kb) * 16384, vp = (unsigned)WS_VB + (head * 64 + kb) * 16384;
      f32x16 s = {};
#pragma unroll
      for (int j = 0; j < 16; ++j) s = MFMA(ld16(rs, lane16, kp + j * 1024), ld16(rs, lane16, qg + j * 1024), s);
#pragma unroll
      for (int e = 0; e < 16; ++e) s[e] = maskv(s[e], qrow - kb * 32, e);
      const float mn = fmaxf(m, max16(s)), al = __builtin_amdgcn_exp2f(m - mn); m = mn; ls *= al;
#pragma unroll
      for (int e = 0; e < 16; ++e) { s[e] = __builtin_amdgcn_exp2f(s[e] - mn); ls += s[e]; }
      const bf16x8 p0 = pack8(s, 0), p1 = pack8(s, 1);
#pragma unroll
      for (int d = 0; d < 8; ++d) { o[d] *= al; o[d] = MFMA(ld16(rs, lane16, vp + d * 1024), p0, o[d]); o[d] = MFMA(ld16(rs, lane16, vp + 8192 + d * 1024), p1, o[d]); }
    }
    const float inv = (1.f - g) / (ls + swap_other(ls));
#pragma unroll
    for (int c = 0; c < 16; ++c) cb[c] = pack8(o[c >> 1], c & 1, inv);
  }
  const unsigned wf = (unsigned)WS_WOF + head * 262144;
  DRAIN();
  for (int nb = 0; nb < 14; ++nb) {
    const int fa = nb < 7 ? 2 * nb : 126 + 18 * (nb - 7);
    f32x16 a = {}; a = MFMA(ld16(rs, lane16, wf + fa * 1024), ca[0], a); a = MFMA(ld16(rs, lane16, wf + (fa + 1) * 1024), ca[1], a);
#pragma unroll
    for (int ks = 0; ks < 16; ++ks) a = MFMA(ld16(rs, lane16, wf + (nb < 7 ? 14 + 7 * ks + nb : fa + 2 + ks) * 1024), cb[ks], a);
    st16(rs, lane16, og + (2 * nb) * 1024, pack8(a, 0)); st16(rs, lane16, og + (2 * nb + 1) * 1024, pack8(a, 1));
  }
}

__global__ __launch_bounds__(256, 1) void fused_fwd(const char* __restrict__ x, const char* __restrict__ wqkv, const char* __restrict__ ckv, const char* __restrict__ wq,
                                                    const char* __restrict__ wuk, const char* __restrict__ wuv, const char* __restrict__ gate_w,
                                                    const char* __restrict__ wo_a, const char* __restrict__ wo_b, char* ws, char* __restrict__ out) {
  extern __shared__ __attribute__((aligned(16))) char lds[];
  const unsigned tid = threadIdx.x, lane = tid & 63, wid = rfl(tid >> 6), lane16 = lane * 16;
  const unsigned bid = blockIdx.x, head = 2 * (bid & 7) + ((bid >> 3) & 1), pi = bid >> 5, roleb = (bid >> 4) & 1;
  STAMP_DECL STAMPRT(0); STAMPIT(1); TR_DECL TRC();
  Sync Y; asm volatile("s_getpc_b64 %0\n.Lgm_base:" : "=s"(Y.pc)); Y.ws = ws; Y.lane = lane;
  Y.token = flag_get(ws + WS_W0) + 1u;
  const __amdgpu_buffer_rsrc_t rs = __builtin_amdgcn_make_buffer_rsrc(ws, 0, (int)WS_END, 0x20000);
  Y.rs = rs;
  f32x16 acc4[4][4];
  proj_phase(acc4, x, wqkv, ckv, wq, wuk, wuv, gate_w, wo_a, wo_b, ws, (unsigned)WS_KVA + head * 262144, Y, lane, wid TR_PASS);
  STAMPIT(2);
  LABEL(a);
  const unsigned token = Y.token;
  if (bid == 0 && wid == 0) flag_set(ws + WS_W0, token);
  const int qlong = 15 - (int)pi, nt1 = 32 - 2 * (int)pi - TA;
  const int qb = roleb ? (int)pi : qlong, nt2 = roleb ? 2 * (int)pi + 2 : TA;
  const bool produce = roleb && nt1 > 0, consume = !roleb && nt1 > 0;
  const unsigned pw = (head * 8 + pi) * 4 + wid;
  const unsigned po = (unsigned)WS_PO + pw * 16384, tb = qb * 4 + wid;
  Stream S; unsigned pj;
  {
    const unsigned n1 = 8 + (roleb ? 2 * nt1 : 0), n2 = n1 + 2 * nt2, n3 = n2 + (consume ? 2 : 0), j = lane;
    const unsigned jb = j < n1 ? j - 8 + 2 * TA : j - n1, kv = (unsigned)WS_KB + head * (unsigned)MB + (jb & 1) * (unsigned)(WS_VB - WS_KB) + (jb >> 1) * 32768;
    unsigned a = j >= 8 ? kv : (unsigned)WS_KVA + head * 262144 + j * 32768;
    a = j >= n3 ? (unsigned)WS_WOF + head * 262144 + (j - n3) * 32768 : a;
    a += wid * 8192;
    a = (j >= n2 && j < n3) ? po + (j - n2) * 8192 : a; pj = n2;
    S.tab = j < n3 + 8 ? a : 0x7ffffff0u; S.srd = mksrd(ws, (unsigned)WS_END); S.lane16 = lane16;
    S.dseq = 5; S.dslot = 65536 + wid * 8192; S.rslot = 65536;
  }
  st_prep(S, S.y);
  const bf16x8 qa0 = ld16(rs, lane16, (unsigned)WS_QA + (head * 64 + tb) * 2048), qa1 = ld16(rs, lane16, (unsigned)WS_QA + (head * 64 + tb) * 2048 + 1024);
  const float g = __uint_as_float(flag_get(ws + WS_G + (tb * 32 + (lane & 31)) * 4));
  wait_bar<0, false>();
  STAMPIT(3);
  bf16x8 ca[2], q[16];
  { const unsigned xcd = bid & 7, loc = bid >> 3, rb = loc & 7, sel = loc >> 3, head2 = 2 * xcd + (sel & 1), isV = sel >> 1, tb0 = rb * 8 + (wid >> 1) * 4;
    KvOut KO; KO.rs = rs; KO.dst = (isV ? (unsigned)WS_VB : (unsigned)WS_KB) + ((head2 * 64 + tb0) * 16 + (wid & 1) * (isV ? 4 : 8)) * 1024; KO.ns = isV ? 1024 : 2048; KO.ss = isV ? 8192 : 1024; KO.wof = (unsigned)WS_WOF + (bid * 4 + wid) * 4096;
    const unsigned cq = (256 * head + (lane & 1) * 255) / 192, rq_ = ((lane & 2) ? qlong : qb) >> 1;
    const unsigned need = lane < 16 ? (head >> 1) + 8 * ((lane & 7) + 8 * ((head & 1) + 2 * (lane >> 3))) : lane < 20 ? (cq & 7) + 8 * (rq_ + 8 * (cq >> 3)) : 16 * head + ((lane - 20) & 15);
    a_phase(ca, q, (unsigned)WS_QB + (head * 64 + (produce ? qlong * 4 + wid : tb)) * 16384, acc4, Y.wq, KO, S, qa0, qa1, g, lane,
            ws + WS_F2 + bid * 4, ws + WS_F2 + need * 4, token, Y.pc, tid, wid TR_PASS); }
  STAMPIT(4);
  LABEL(b);
  char* fh = ws + WS_FH + pw * 64; char* ml = ws + WS_ML + pw * 512 + lane * 8;
  f32x16 o[8]; float mref, lsum, mb, lb; bool late = false;
  for (int pass = produce ? 0 : 1; pass < 2; ++pass) {
    b_segment(o, q, mref, lsum, S, pass ? qb : qlong, pass ? 0 : TA, pass ? nt2 : TA + nt1, lane, wid, fh, ml, token, consume, mb, lb, late, pj TR_PASS);
    if (pass == 0) {
#pragma unroll
      for (int j = 0; j < 16; ++j) q[j] = ld16(rs, lane16, (unsigned)WS_QB + ((head * 64 + tb) * 16 + j) * 1024);
      const float inv = lsum > 0.f ? 1.f / lsum : 0.f;
#pragma unroll
      for (int c = 0; c < 16; ++c) { if (!(c & 1)) PINA(o[c >> 1]); st16(rs, lane16, po + c * 1024, pack8(o[c >> 1], c & 1, inv)); SBAR(); }
      __hip_atomic_store((GAS unsigned long long*)ml, ((unsigned long long)__float_as_uint(lsum) << 32) | __float_as_uint(mref), __ATOMIC_RELAXED, __HIP_MEMORY_SCOPE_AGENT);
      DRAIN();
      flag_set(fh, token);
    }
  }
  STAMPIT(5);
  LABEL(c);
  const unsigned og = (unsigned)WS_OP + (head * 64 + tb) * 28 * 1024;
  float chk;
  if (consume) {
    if (late) {
      flag_wait(fh, token);
      const unsigned long long w = __hip_atomic_load((GAS unsigned long long*)ml, __ATOMIC_RELAXED, __HIP_MEMORY_SCOPE_AGENT);
      mb = __uint_as_float((unsigned)w); lb = __uint_as_float((unsigned)(w >> 32));
      Desc D; D.so = po; D.m0 = S.rslot + wid * 8192;
      sfor<16>([&](auto Q) __attribute__((always_inline)) { constexpr int qq = decltype(Q)::v;
        if constexpr (qq == 8) { D.so = po + 8192; D.m0 = nslot(S.rslot) + wid * 8192; }
        st_pre<(qq & 7)>(S, D); if constexpr (!(qq & 3)) NOP0(); st_go<(qq & 7)>(S); });
      DRAIN();
    }
    const float m = lb > 0.f ? fmaxf(mref, mb) : mref, ea = __builtin_amdgcn_exp2f(mref - m), wb = lb > 0.f ? lb * __builtin_amdgcn_exp2f(mb - m) : lb * 0.f;
    const float den = lsum * ea + wb, inv = (1.f - g) / den;
    chk = den + out_proj<true>(o, ca, ea * inv, wb * inv, S, rs, og, wid TR_PASS);
  } else chk = lsum + out_proj<false>(o, ca, (1.f - g) / lsum, 0.f, S, rs, og, wid TR_PASS);
  STAMPIT(6);
  if (__builtin_expect(__any(!(chk * 0.f == 0.f)), 0)) safe_redo(rs, og, head, tb, g, lane);
  DRAIN();
  flag_set(ws + WS_F3 + (tb * 16 + head) * 4, token);
  STAMPIT(7);
  flag_wait(ws + WS_F3 + (tb * 16 + (lane & 15)) * 4, token);
  STAMPIT(8);
  {
    u32x4 v[2][16];
    const unsigned c0 = head * 112 + lane, c1 = lane < 48 ? c0 + 64 : c0;
#pragma unroll
    for (int h = 0; h < 16; ++h) { v[0][h] = __builtin_bit_cast(u32x4, ld16(rs, c0 * 16, (unsigned)WS_OP + (h * 64 + tb) * 28 * 1024)); v[1][h] = __builtin_bit_cast(u32x4, ld16(rs, c1 * 16, (unsigned)WS_OP + (h * 64 + tb) * 28 * 1024)); }
    SBAR();
#pragma unroll
    for (int k = 0; k < 2; ++k) { float a[8] = {};
#pragma unroll
      for (int h = 0; h < 16; ++h)
#pragma unroll
        for (int e = 0; e < 4; ++e) { a[2 * e] += bflo(v[k][h][e]); a[2 * e + 1] += bfhi(v[k][h][e]); }
      const unsigned c = k ? c1 : c0;
      char* op = out + (size_t)(tb * 32 + (c & 31)) * 896 + ((c >> 6) * 16 + 4 * ((c >> 5) & 1)) * 2;
      if (k == 0 || lane < 48) { *(u32x2*)op = (u32x2){cvtpk(a[0], a[1]), cvtpk(a[2], a[3])}; *(u32x2*)(op + 16) = (u32x2){cvtpk(a[4], a[5]), cvtpk(a[6], a[7])}; } }
  }
  LABEL(end);
  STAMPIT(9); STAMPRT(10); STAMP_OUT(1, 11); TRC(); TR_OUT();
}

extern "C" void kernel_launch(void* const* d_in, const int* in_sizes, int n_in, void* d_out, int out_size, void* d_ws, size_t ws_size, hipStream_t stream) {
  (void)in_sizes; (void)n_in; (void)out_size; (void)ws_size;
  (void)hipFuncSetAttribute((const void*)fused_fwd, hipFuncAttributeMaxDynamicSharedMemorySize, 163840);
  fused_fwd<<<256, 256, 163840, stream>>>((const char*)d_in[0], (const char*)d_in[1], (const char*)d_in[4], (const char*)d_in[5], (const char*)d_in[6], (const char*)d_in[7], (const char*)d_in[9],
                                          (const char*)d_in[2], (const char*)d_in[8], (char*)d_ws, (char*)d_out);
}
```

```cpp
#include <hip/hip_runtime.h>
#include <stdint.h>
#include <utility>

typedef short bf16x8 __attribute__((ext_vector_type(8)));
typedef short s16x4 __attribute__((ext_vector_type(4)));
typedef float f32x16 __attribute__((ext_vector_type(16)));
typedef float f32x2 __attribute__((ext_vector_type(2)));
typedef __bf16 bf16x2_t __attribute__((ext_vector_type(2)));
typedef unsigned u32x4 __attribute__((ext_vector_type(4)));
typedef unsigned u32x2 __attribute__((ext_vector_type(2)));
#define LAS __attribute__((address_space(3)))
#define GAS __attribute__((address_space(1)))
#define DI __device__ __forceinline__
#define SBAR() __builtin_amdgcn_sched_barrier(0)
#define MFMA(a, b, c) __builtin_amdgcn_mfma_f32_32x32x16_bf16((a), (b), (c), 0, 0, 0)
#define PIN(x) asm volatile("" : "+v"(x))

constexpr size_t MB = 1u << 20;
constexpr size_t WS_QB = 0, WS_KB = 16 * MB, WS_VB = 32 * MB, WS_QA = 48 * MB, WS_KVA = 50 * MB, WS_G = 54 * MB, WS_W0 = 54 * MB + 65536, WS_F1 = WS_W0 + 4096, WS_F2 = WS_W0 + 8192, WS_F3 = WS_W0 + 16384, WS_FH = WS_W0 + 32768,
                 WS_ML = 55 * MB, WS_PO = 56 * MB, WS_OP = 64 * MB, WS_WOF = 92 * MB, WS_END = 96 * MB;
#define TA_ 18
constexpr int TA = TA_;
constexpr float LOG2E = 1.4426950408889634f;
constexpr float QSCALE_B = 0.0625f * LOG2E;
constexpr float QSCALE_A = 0.18898223650461362f * LOG2E;

template <int I> struct IC { static constexpr int v = I; };
template <class F, int... Is> DI void sfor_(F&& f, std::integer_sequence<int, Is...>) { (f(IC<Is>{}), ...); }
template <int N, class F> DI void sfor(F&& f) { sfor_(f, std::make_integer_sequence<int, N>{}); }
DI void mfma_vc(f32x16& d, const bf16x8& a, const bf16x8& b, const f32x16& c) { asm volatile("v_mfma_f32_32x32x16_bf16 %0, %1, %2, %3" : "=&v"(d) : "v"(a), "v"(b), "v"(c)); }
DI void mfma_v(f32x16& d, const bf16x8& a, const bf16x8& b) { asm volatile("v_mfma_f32_32x32x16_bf16 %0, %1, %2, %0" : "+v"(d) : "v"(a), "v"(b)); }
DI void mfma_vn(f32x16& d, const bf16x8& a, const bf16x8& b) { asm volatile("s_nop 1\n\tv_mfma_f32_32x32x16_bf16 %0, %1, %2, %0" : "+v"(d) : "v"(a), "v"(b)); }
DI void mfma_a(f32x16& d, const bf16x8& a, const bf16x8& b) { asm volatile("v_mfma_f32_32x32x16_bf16 %0, %1, %2, %0" : "+a"(d) : "v"(a), "v"(b)); }
#define MFMA_DRAIN_V(x) asm volatile("s_nop 7\n\ts_nop 7" : "+v"(x))
#define MFMA_DRAIN_O(o) asm volatile("s_nop 7\n\ts_nop 7" : "+a"(o[0]), "+a"(o[1]), "+a"(o[2]), "+a"(o[3]), "+a"(o[4]), "+a"(o[5]), "+a"(o[6]), "+a"(o[7]))
DI float ard(float a) { float r; asm volatile("v_accvgpr_read_b32 %0, %1" : "=v"(r) : "a"(a)); return r; }
DI float opaque0() { float z; asm volatile("v_mov_b32 %0, 0" : "=v"(z)); return z; }
DI unsigned rfl(unsigned x) { return __builtin_amdgcn_readfirstlane(x); }
DI unsigned cvtpk(float lo, float hi) { f32x2 v = {lo, hi}; bf16x2_t b = __builtin_convertvector(v, bf16x2_t); return __builtin_bit_cast(unsigned, b); }
DI float dot2(unsigned a, unsigned b, float c) { return __builtin_amdgcn_fdot2_f32_bf16(__builtin_bit_cast(bf16x2_t, a), __builtin_bit_cast(bf16x2_t, b), c, false); }
DI float bflo(unsigned w) { return __uint_as_float(w << 16); }
DI float bfhi(unsigned w) { return __uint_as_float(w & 0xffff0000u); }
DI u32x4 mksrd(const void* p, unsigned bytes) { u32x4 s = {(unsigned)(uintptr_t)p, (unsigned)((uintptr_t)p >> 32) & 0xffffu, bytes, 0x20000u}; return s; }
DI bf16x8 ldsr(unsigned a) { return *(const LAS bf16x8*)(uintptr_t)a; }
DI bf16x8 ldstr(unsigned a, unsigned a2) {
  s16x4 x = __builtin_amdgcn_ds_read_tr16_b64_v4i16((LAS s16x4*)(uintptr_t)a);
  s16x4 y = __builtin_amdgcn_ds_read_tr16_b64_v4i16((LAS s16x4*)(uintptr_t)a2);
  return __builtin_shufflevector(x, y, 0, 1, 2, 3, 4, 5, 6, 7);
}
DI void set_m0(unsigned v) { asm volatile("s_mov_b32 m0, %0" ::"s"(v) : "memory"); }
template <int OFF, int POL = 1> DI void dma16(unsigned voff, u32x4 srd, unsigned soff) {
  if constexpr (POL == 1) asm volatile("buffer_load_dwordx4 %0, %1, %2 offen offset:%3 sc1 lds" ::"v"(voff), "s"(srd), "s"(soff), "i"(OFF) : "memory");
  else if constexpr (POL == 2) asm volatile("buffer_load_dwordx4 %0, %1, %2 offen offset:%3 nt lds" ::"v"(voff), "s"(srd), "s"(soff), "i"(OFF) : "memory");
  else asm volatile("buffer_load_dwordx4 %0, %1, %2 offen offset:%3 lds" ::"v"(voff), "s"(srd), "s"(soff), "i"(OFF) : "memory");
}
DI void touch(const char* p) { asm volatile("global_load_dword a255, %0, off" ::"v"(p) : "memory", "a255"); }
#define LABEL(n) asm volatile(".Lgm_" #n ":" ::: "memory")
#define LOFF(n) ({ unsigned o_; asm volatile("s_mov_b32 %0, .Lgm_" #n "-.Lgm_base" : "=s"(o_)); o_; })
DI const char* code_line(const char* pc, unsigned from, unsigned to, unsigned line) { const unsigned o = from + line * 64; return pc + (o < to ? o : from); }
#define PINS(x) asm volatile("" : "+s"(x))
#define PINA(x) asm volatile("" : "+a"(x))
#define NOP0() asm volatile("s_nop 0" ::: "memory")
template <int V, bool L0> DI void wait_bar() {
  asm volatile("" ::: "memory");
  __builtin_amdgcn_s_waitcnt((V & 15) | ((V >> 4) << 14) | (7 << 4) | (L0 ? 0 : (15 << 8)));
  __builtin_amdgcn_s_barrier();
  asm volatile("" ::: "memory");
  SBAR();
}
DI bf16x8 pack8(const f32x16& a, int s, float sc) {
  u32x4 w = {cvtpk(a[8 * s] * sc, a[8 * s + 1] * sc), cvtpk(a[8 * s + 2] * sc, a[8 * s + 3] * sc), cvtpk(a[8 * s + 4] * sc, a[8 * s + 5] * sc), cvtpk(a[8 * s + 6] * sc, a[8 * s + 7] * sc)};
  return __builtin_bit_cast(bf16x8, w);
}
DI bf16x8 pack8(const f32x16& a, int s) {
  u32x4 w = {cvtpk(a[8 * s], a[8 * s + 1]), cvtpk(a[8 * s + 2], a[8 * s + 3]), cvtpk(a[8 * s + 4], a[8 * s + 5]), cvtpk(a[8 * s + 6], a[8 * s + 7])};
  return __builtin_bit_cast(bf16x8, w);
}
DI f32x2 halves(float v) {
  auto r = __builtin_amdgcn_permlane32_swap(__float_as_uint(v), __float_as_uint(v), false, false);
  return (f32x2){__uint_as_float(r[0]), __uint_as_float(r[1])};
}
DI float hmax(float v) { const f32x2 h = halves(v); return fmaxf(h[0], h[1]); }
DI float hsum(float v) { const f32x2 h = halves(v); return h[0] + h[1]; }

#define STAMP_DECL
#define STAMPIT(i)
#define STAMPRT(i)
#define STAMP_OUT(k, n)
#define TR_DECL
#define TR_ARG
#define TR_PASS
#define TRC()
#define TR_OUT()
__host__ __device__ constexpr bool wf_isa(int f) { return f < 14 || (f >= 126 && (f - 126) % 18 < 2); }
__host__ __device__ constexpr int wf_nb(int f) { return f < 14 ? f >> 1 : f < 126 ? (f - 14) % 7 : 7 + (f - 126) / 18; }
__host__ __device__ constexpr int wf_ks(int f) { return f < 14 ? f & 1 : f < 126 ? (f - 14) / 7 : (f - 126) % 18 < 2 ? (f - 126) % 18 : (f - 126) % 18 - 2; }
constexpr unsigned XRING = 65536;

#define WPOL 0
#define XPOL 0
struct Prov { u32x4 srd; unsigned voff, soff, pstr, kstr; };
DI void prov_next(Prov& p) { p.soff += p.kstr; }
struct K1State { unsigned xs, ws, xd; };

template <int Q, int POL = 1> DI void k1_dma(Prov& p, unsigned m0b) { set_m0(m0b + Q * 4096); unsigned so = p.soff + Q * p.pstr; PINS(so); NOP0(); dma16<0, POL>(p.voff, p.srd, so); }

#define ST_AUX 16
DI void st16(__amdgpu_buffer_rsrc_t rs, unsigned voff, unsigned soff, bf16x8 v) {
  __builtin_amdgcn_raw_buffer_store_b128(__builtin_bit_cast(u32x4, v), rs, voff, soff, ST_AUX);
  asm volatile("s_nop 1" ::"v"(v) : "memory");
}

struct Sync { char* ws; unsigned token, lane; u32x4 fv;
  __amdgpu_buffer_rsrc_t rs; bf16x8 sq[24]; u32x4 wq[4]; unsigned sqe, sql; bool stored; bf16x8 qa[2]; unsigned qao, tso[2], tm0[2];
  const char* woa; const char* wob; unsigned gtab, gv0, gt[2][8];
  const char* pc; unsigned pfd[2];
};
DI void flag_set(char* p, unsigned token) { __hip_atomic_store((GAS unsigned*)p, token, __ATOMIC_RELAXED, __HIP_MEMORY_SCOPE_AGENT); }
DI unsigned flag_get(const char* p) { return __hip_atomic_load((GAS unsigned*)p, __ATOMIC_RELAXED, __HIP_MEMORY_SCOPE_AGENT); }
constexpr unsigned SPIN_CAP = 1u << 20;
DI void flag_wait(const char* p, unsigned token) {
#pragma unroll 1
  for (unsigned i = 0; i < SPIN_CAP; ++i) { if (__all(flag_get(p) == token)) break; __builtin_amdgcn_s_sleep(1); }
}
#define DRAIN() do { asm volatile("s_waitcnt vmcnt(0)" ::: "memory"); __builtin_amdgcn_s_waitcnt(0x0f70); } while (0)

template <bool SWAP, int NTL, bool FIRST, int QT>
DI void k1_ktile(f32x16 (&acc)[4][NTL], bf16x8 (&xa)[2][4], bf16x8 (&wf)[2][4], unsigned (&xr)[4], unsigned (&wr)[4], K1State& st, Prov& xp, Prov& wp,
                 const Prov& xn, const Prov& wn, const int t, const int NKT, const unsigned wid, Sync& Y) {
  constexpr int NM = 4 * NTL;
  u32x4 gsrd; unsigned gso, gv1;
  if constexpr (QT >= 0 && QT < 4) { const unsigned e = __builtin_amdgcn_readlane(Y.gtab, QT); gsrd = (e & 0x40000000u) ? mksrd(Y.woa, 448 * 896) : mksrd(Y.wob, 4096 * 896); gso = e & 0x3fffffffu;
    gv1 = ((int)e < 0 && Y.lane >= 32) ? 0x7ffffff0u : Y.gv0 + 8 * 896; PINS(gsrd); PINS(gso); PIN(gv1); }
  sfor<4>([&](auto KS) __attribute__((always_inline)) { constexpr int ks = decltype(KS)::v, cur = ks & 1, nx = cur ^ 1, kn = (ks + 1) & 3;
    unsigned m0b;
    constexpr bool BOOT = FIRST && NTL == 3;
    constexpr int VW = (BOOT && ks == 1) ? 12 : (QT >= 1 && QT < 4) ? (ks == 1 ? 24 : 22) : QT == 4 ? (ks == 1 ? 22 : 16) : QT == 12 ? (ks == 1 ? 18 : 21) : QT > 12 ? 22 : 16;
#define K1_BAR() do { if constexpr (QT == 0) { if (!Y.stored) asm volatile("s_waitcnt vmcnt(16)" ::: "memory"); wait_bar<40, true>(); } else wait_bar<VW, true>(); } while (0)
    if constexpr (ks == 0) { if constexpr (NTL == 4 && QT == -1) { if (t == 7 && Y.stored) DRAIN(); } m0b = (st.ws ^ 32768u) + 16384 + wid * 1024; }
    if constexpr (ks == 1) { K1_BAR(); m0b = XRING + st.xd + wid * 1024 + 16384;
      if constexpr (NTL == 4 && QT == -1) { if (t == 7 && wid == 0) flag_set(Y.ws + WS_F1 + blockIdx.x * 4, Y.token); } }
    if constexpr (ks == 2) { if (t == NKT - 2) wp = wn; m0b = st.ws + wid * 1024; }
    if constexpr (ks == 3) {
      if constexpr (QT == 13) {
        if (wid == 0) { asm volatile("s_waitcnt vmcnt(39)" : "+v"(Y.fv));
          if (!__all(Y.fv[0] == Y.token && Y.fv[1] == Y.token && Y.fv[2] == Y.token && Y.fv[3] == Y.token))
#pragma unroll 1
            for (int i = 0; i < 4; ++i) flag_wait(Y.ws + WS_F1 + Y.lane * 16 + i * 4, Y.token); }
      }
      K1_BAR();
      prov_next(xp); if (t == NKT - 3) xp = xn;
      st.xd = st.xs;
      m0b = XRING + st.xd + wid * 1024;
      const unsigned xs2 = st.xs == 65536 ? 0u : st.xs + 32768u, dx = xs2 - st.xs, dw = st.ws ? (unsigned)-32768 : 32768u;
      st.xs = xs2; st.ws ^= 32768u;
#pragma unroll
      for (int i = 0; i < 4; ++i) { xr[i] += dx; PIN(xr[i]); }
#pragma unroll
      for (int i = 0; i < NTL; ++i) { wr[i] += dw; PIN(wr[i]); }
    }
    Prov& pp = (ks & 1) ? xp : wp;
    unsigned so = pp.soff + (ks == 1 ? 4 * pp.pstr : 0), qso = 0;
    sfor<NM>([&](auto I) __attribute__((always_inline)) { constexpr int i = decltype(I)::v, m = i / NTL, n = i % NTL;
      if constexpr (FIRST && ks == 0) { const f32x16 z = {}; acc[m][n] = SWAP ? MFMA(wf[cur][n], xa[cur][m], z) : MFMA(xa[cur][m], wf[cur][n], z); }
      else acc[m][n] = SWAP ? MFMA(wf[cur][n], xa[cur][m], acc[m][n]) : MFMA(xa[cur][m], wf[cur][n], acc[m][n]);
      if constexpr (i == 0) xa[nx][0] = ldsr(xr[kn]);
      else if constexpr (i <= NTL) wf[nx][i - 1] = ldstr(wr[i - 1] + (kn & 1) * 8192 + (kn >> 1) * 16384, wr[i - 1] + (kn & 1) * 8192 + (kn >> 1) * 16384 + 2048);
      else if constexpr (i <= NTL + 3) xa[nx][i - NTL] = ldsr(xr[kn] + (i - NTL) * 4096);
      constexpr int D0 = NM - 8;
      constexpr int le = 6 * (QT - 12) + 3 * (ks >> 1) + 2 * (ks & 1);
      if constexpr (QT >= 12 && (i == 2 || (i == 9 && !(ks & 1)))) { qso = __builtin_amdgcn_readlane(Y.sql, le + (i == 9)); PINS(qso); }
      if constexpr (QT >= 12 && (i == 6 || (i == 12 && !(ks & 1)))) st16(Y.rs, Y.lane * 16, qso, Y.sq[le + (i == 12)]);
      if constexpr (QT == 14 && ks == 0 && (i == 0 || i == 7))
        asm volatile("s_nop 4\n\tbuffer_load_dwordx4 %0, %1, %2, %3 offen offset:%4 sc1" : "=v"(Y.qa[i == 7]) : "v"(Y.lane * 16), "s"(xn.srd), "s"(Y.qao), "i"(i == 7 ? 1024 : 0) : "memory");
      if constexpr (QT >= 0 && QT < 4 && (i == 5 || i == 7)) {
        constexpr int j = 2 * ks + (i == 7);
        asm volatile("buffer_load_ushort %0, %1, %2, %3 offen offset:%4" : "=v"(Y.gt[QT & 1][j]) : "v"(j < 4 ? Y.gv0 : gv1), "s"(gsrd), "s"(gso), "i"((j & 3) * 896) : "memory");
      }
      if constexpr (QT >= 2 && QT < 6 && ks == 0 && i >= 1 && i < 5) {
        asm volatile("" : "+v"(Y.gt[QT & 1][2 * i - 2]), "+v"(Y.gt[QT & 1][2 * i - 1]));
        unsigned w = Y.gt[QT & 1][2 * i - 2] | (Y.gt[QT & 1][2 * i - 1] << 16); PIN(w); Y.wq[QT - 2][i - 1] = w; }
      if constexpr (BOOT && ks < 2) {
        const unsigned x1 = XRING + (st.xs == 65536u ? 0u : st.xs + 32768u) + wid * 1024, x2 = XRING + st.xd + wid * 1024, w2 = (st.ws ^ 32768u) + wid * 1024;
        if constexpr (ks == 0) { if constexpr (i < 8) k1_dma<i, 0>(xp, x1); else k1_dma<i - 8, 0>(wp, w2); if constexpr (i == 7) prov_next(xp); }
        else { if constexpr (i < 4) k1_dma<i, 0>(xp, x2); else if constexpr (i < 8) k1_dma<i - 4, 0>(wp, w2 + 16384); else k1_dma<i - 4, 0>(xp, x2); if constexpr (i == 7) prov_next(wp); }
      } else if constexpr (i >= D0) {
        if constexpr (((i - D0) & 1) == 0) { set_m0(m0b + ((i - D0) >> 1) * 4096); if constexpr (i > D0) so += pp.pstr; PINS(so); }
        else dma16<0, QT >= 13 ? 1 : (ks & 1) ? XPOL : WPOL>(pp.voff, pp.srd, so);
      }
      SBAR();
    });
    if constexpr (!(ks & 1)) prov_next(wp);
#undef K1_BAR
  });
}

template <bool SWAP, int NTL>
DI void k1_gemm(f32x16 (&acc)[4][NTL], K1State& st, Prov& xp, Prov& wp, const Prov& xn, const Prov& wn, const int NKT, const unsigned wid, const unsigned lane, Sync& Y) {
  const unsigned r = lane & 31, hh = lane >> 5, q = (lane >> 2) & 3, wrow = wid >> 1, wc = wid & 1;
  unsigned xr[4], wr[4];
#pragma unroll
  for (int ks = 0; ks < 4; ++ks) { xr[ks] = XRING + st.xs + (128 * wrow + r) * 128 + 16 * ((2 * ks + hh) ^ ((r >> 1) & 7)); PIN(xr[ks]); }
#pragma unroll
  for (int n = 0; n < NTL; ++n) { wr[n] = st.ws + (8 * hh + q) * 512 + 64 * ((NTL * wc + n) ^ q) + 32 * ((lane >> 4) & 1) + 8 * (lane & 3); PIN(wr[n]); }
  bf16x8 xa[2][4], wf[2][4];
  if constexpr (NTL == 3) wait_bar<5, false>();
#pragma unroll
  for (int m = 0; m < 4; ++m) xa[0][m] = ldsr(xr[0] + m * 4096);
#pragma unroll
  for (int n = 0; n < NTL; ++n) wf[0][n] = ldstr(wr[n], wr[n] + 2048);
  SBAR();
  constexpr int NP = NTL == 4 ? 6 : 1;
  sfor<NP>([&](auto T) __attribute__((always_inline)) { constexpr int t = decltype(T)::v; k1_ktile<SWAP, NTL, t == 0, NTL == 4 ? t : -1>(acc, xa, wf, xr, wr, st, xp, wp, xn, wn, t, NKT, wid, Y); });
  if constexpr (NTL == 4) {
    for (int t = NP; t < 12; ++t) k1_ktile<SWAP, NTL, false, -1>(acc, xa, wf, xr, wr, st, xp, wp, xn, wn, t, NKT, wid, Y);
    asm volatile("global_load_dword %0, %2, off\n\tglobal_load_dword %1, %3, off" : "=&v"(Y.pfd[0]), "=&v"(Y.pfd[1])
                 : "v"(code_line(Y.pc, LOFF(a), LOFF(b), wid * 64 + lane)), "v"(code_line(Y.pc, LOFF(a), LOFF(b), 256 + wid * 64 + lane)) : "memory");
    if (wid == 0) asm volatile("global_load_dwordx4 %0, %1, off sc1" : "=v"(Y.fv) : "v"(Y.ws + WS_F1 + Y.lane * 16) : "memory");
    sfor<4>([&](auto T) __attribute__((always_inline)) { constexpr int t = 12 + decltype(T)::v; k1_ktile<SWAP, NTL, false, t>(acc, xa, wf, xr, wr, st, xp, wp, xn, wn, t, 16, wid, Y); });
    asm volatile("" ::"v"(Y.pfd[0]), "v"(Y.pfd[1]));
  } else
    for (int t = NP; t < NKT; ++t) k1_ktile<SWAP, NTL, false, -1>(acc, xa, wf, xr, wr, st, xp, wp, xn, wn, t, NKT, wid, Y);
}

DI Prov mk_xprov(const char* X, unsigned ld, unsigned bytes, unsigned rb, unsigned wid, unsigned lane) {
  Prov p; p.srd = mksrd(X, bytes);
  p.voff = (lane >> 3) * ld + 16 * ((lane & 7) ^ (4 * (wid & 1) + (lane >> 4)));
  p.soff = (256 * rb + 8 * wid) * ld; p.pstr = 32 * ld; p.kstr = 128; return p;
}
template <class F> DI Prov mk_wprov(const char* W, unsigned ld, unsigned bytes, unsigned wid, unsigned lane, F colf) {
  Prov p; p.srd = mksrd(W, bytes);
  const unsigned ch = (lane & 31) ^ (((2 * (wid & 1) + (lane >> 5)) & 3) << 2), col = colf(ch);
  p.voff = col < 0x10000u ? (lane >> 5) * ld + 2 * col : 0x7ffffff0u;
  p.soff = 2 * wid * ld; p.pstr = 8 * ld; p.kstr = 32 * ld; return p;
}
DI void proj_phase(f32x16 (&acc4)[4][4], const char* x, const char* wqkv, const char* ckv, const char* wq, const char* wuk, const char* wuv, const char* gate_w,
                   const char* wo_a, const char* wo_b, char* ws, const unsigned kva_off, Sync& Y, const unsigned lane, const unsigned wid TR_ARG) {
  const unsigned r = lane & 31, hh = lane >> 5, wrow = wid >> 1, wc = wid & 1, lane16 = lane * 16;
  const unsigned bid = blockIdx.x, xcd = bid & 7, loc = bid >> 3, rb = loc & 7, sel = loc >> 3;
  const unsigned cb = xcd + 8 * sel;
  const unsigned head2 = 2 * xcd + (sel & 1), isV = sel >> 1;
  const Prov xkv = mk_xprov(ckv, 2048, 2048 * 2048, rb, wid, lane);
  const Prov wkv = mk_wprov(isV ? wuv : wuk, 8192, 1024 * 8192, wid, lane, [&](unsigned ch) { return head2 * 256 + 8 * ch; });
  Prov xp = cb < 31 ? mk_xprov(x, 896, 2048 * 896, rb, wid, lane) : xkv;
  Prov wp = cb < 22 ? mk_wprov(wq, 8192, 448 * 8192, wid, lane, [&](unsigned ch) { return ch < 24 ? cb * 192 + 8 * ch : 0x10000u; })
          : cb < 31 ? mk_wprov(wqkv, 2688, 448 * 2688, wid, lane, [&](unsigned ch) { const unsigned vc = (cb < 28 ? cb - 22 : cb - 28) * 24 + ch, slot = vc >> 2;
                         return (ch < 24 && slot < (cb < 28 ? 32u : 16u)) ? (cb < 28 ? 0u : 896u) + 28 * slot + 8 * (vc & 3) : 0x10000u; })
          : wkv;
  K1State st = cb < 31 ? K1State{32768u, 32768u, 0u} : K1State{65536u, 0u, 32768u};
  const unsigned x0 = XRING + st.xs + wid * 1024, w0 = st.ws + wid * 1024;
  k1_dma<0, 0>(xp, x0); k1_dma<1, 0>(xp, x0); k1_dma<2, 0>(xp, x0); k1_dma<3, 0>(xp, x0); k1_dma<4, 0>(xp, x0); k1_dma<5, 0>(xp, x0); k1_dma<6, 0>(xp, x0); k1_dma<7, 0>(xp, x0); prov_next(xp);
  k1_dma<0, 0>(wp, w0); k1_dma<1, 0>(wp, w0); k1_dma<2, 0>(wp, w0); k1_dma<3, 0>(wp, w0); prov_next(wp);
  k1_dma<0, 0>(wp, w0 + 16384); k1_dma<1, 0>(wp, w0 + 16384); k1_dma<2, 0>(wp, w0 + 16384); k1_dma<3, 0>(wp, w0 + 16384); prov_next(wp);
  touch(code_line(Y.pc, 0, LOFF(end), (rb * 4 + wid) * 64 + lane));
  const unsigned gw = bid * 4 + wid, hd = gw >> 6;
  { int f = (gw & 63) * 4 + (lane & 3); f = f < 252 ? f : 251;
    const unsigned nb = wf_nb(f), ks = wf_ks(f); const bool isa = wf_isa(f);
    Y.gtab = ((hd * (isa ? 28 : 256) + 16 * ks) * 896 + 64 * nb) | (isa ? 0x40000000u : 0u) | (isa && ks == 1 ? 0x80000000u : 0u); PIN(Y.gtab); }
  Y.woa = wo_a; Y.wob = wo_b; Y.gv0 = 4 * hh * 896 + 2 * r; Y.stored = cb >= 22;
  const unsigned tb0 = rb * 8 + wrow * 4;
  {
    const unsigned n = lane >> 3, m = (lane >> 1) & 3, s = lane & 1, c32 = (cb < 22 ? cb : cb < 28 ? cb - 22 : cb - 28) * 6 + wc * 3 + n;
    unsigned d = cb < 22 ? (unsigned)WS_QB + (((c32 >> 3) * 64 + tb0 + m) * 16 + (c32 & 7) * 2 + s) * 1024
               : cb >= 28 ? (unsigned)WS_KVA + (c32 * 64 + tb0 + m) * 4096 + 2048 + s * 1024
               : c32 < 16 ? (unsigned)WS_QA + (c32 * 64 + tb0 + m) * 2048 + s * 1024 : (unsigned)WS_KVA + ((c32 - 16) * 64 + tb0 + m) * 4096 + s * 1024;
    if (cb == 31 || c32 >= (cb < 22 ? 128u : cb < 28 ? 32u : 16u)) d = 0x7ffffff0u;
    Y.sqe = cb >= 22 ? d : 0x7ffffff0u; Y.sql = cb < 22 ? d : 0x7ffffff0u;
  }
  if (cb < 31) {
    f32x16 acc[4][3];
    if (cb < 28) {
      k1_gemm<true, 3>(acc, st, xp, wp, xkv, wkv, 7, wid, lane, Y);
      TRC();
      const bool pad = cb >= 22 && hh;
#pragma unroll
      for (int n = 0; n < 3; ++n) { const float sc = cb < 22 ? QSCALE_B : (cb - 22) * 6 + wc * 3 + n < 16 ? QSCALE_A : 1.f;
#pragma unroll
        for (int m = 0; m < 4; ++m) { PINA(acc[m][n]); u32x4 w1 = __builtin_bit_cast(u32x4, pack8(acc[m][n], 1, sc)); if (pad) { w1[2] = 0u; w1[3] = 0u; }
          Y.sq[8 * n + 2 * m] = pack8(acc[m][n], 0, sc); Y.sq[8 * n + 2 * m + 1] = __builtin_bit_cast(bf16x8, w1); PIN(Y.sq[8 * n + 2 * m]); PIN(Y.sq[8 * n + 2 * m + 1]); SBAR(); } }
    } else {
      k1_gemm<false, 3>(acc, st, xp, wp, xkv, wkv, 7, wid, lane, Y);
      TRC();
      const unsigned fill = r == 28 ? 0x3f803f80u : 0u;
#pragma unroll
      for (int n = 0; n < 3; ++n)
#pragma unroll
        for (int m = 0; m < 4; ++m) { PINA(acc[m][n]);
#pragma unroll
          for (int s = 0; s < 2; ++s) { u32x4 w = __builtin_bit_cast(u32x4, pack8(acc[m][n], s)); if (r >= 28) w = (u32x4){fill, fill, fill, fill};
            Y.sq[8 * n + 2 * m + s] = __builtin_bit_cast(bf16x8, w); PIN(Y.sq[8 * n + 2 * m + s]); }
          SBAR(); }
    }
  } else {
    { const unsigned x1 = XRING + wid * 1024, x2 = XRING + st.xd + wid * 1024, w2 = (st.ws ^ 32768u) + wid * 1024;
      sfor<8>([&](auto I) __attribute__((always_inline)) { k1_dma<decltype(I)::v, 0>(xp, x1); }); prov_next(xp);
      sfor<4>([&](auto I) __attribute__((always_inline)) { k1_dma<decltype(I)::v, 0>(wp, w2); }); prov_next(wp);
      sfor<4>([&](auto I) __attribute__((always_inline)) { k1_dma<decltype(I)::v, 0>(xp, x2); }); }
    u32x4 gw_[7];
#pragma unroll
    for (int i = 0; i < 7; ++i) gw_[i] = *(const u32x4*)(gate_w + (8 * i + (lane & 7)) * 16);
#pragma unroll 2
    for (int p = 0; p < 8; ++p) { const unsigned rw = rb * 256 + wid * 64 + p * 8 + (lane >> 3); float s = 0.f;
#pragma unroll
      for (int i = 0; i < 7; ++i) { const u32x4 a = *(const u32x4*)(x + rw * 896 + (8 * i + (lane & 7)) * 16);
#pragma unroll
        for (int e = 0; e < 4; ++e) s = dot2(a[e], gw_[i][e], s); }
      s += __shfl_xor(s, 1); s += __shfl_xor(s, 2); s += __shfl_xor(s, 4);
      flag_set(ws + WS_G + rw * 4, __float_as_uint(1.f / (1.f + __expf(-s)))); }
#pragma unroll
    for (int e = 0; e < 24; ++e) { Y.sq[e] = bf16x8{}; PIN(Y.sq[e]); }
    wait_bar<0, false>();
    TRC();
  }
  if (Y.stored) sfor<24>([&](auto E) __attribute__((always_inline)) { constexpr int e = decltype(E)::v;
    unsigned so = __builtin_amdgcn_readlane(Y.sqe, e); asm volatile("s_nop 4" : "+s"(so)); st16(Y.rs, lane16, so, Y.sq[e]); });
  asm volatile("" : "+v"(Y.token)); Y.token = rfl(Y.token) + 1u;
  TRC();
  Prov xa_, wa_; xa_.srd = mksrd(ws, (unsigned)WS_END); xa_.voff = lane16; xa_.soff = kva_off + wid * 1024; xa_.pstr = 4096; xa_.kstr = 32768;
  wa_ = xa_; wa_.soff += 3 * 32768; wa_.kstr = 16384;
  if (!isV) k1_gemm<true, 4>(acc4, st, xp, wp, xa_, wa_, 16, wid, lane, Y);
  else k1_gemm<false, 4>(acc4, st, xp, wp, xa_, wa_, 16, wid, lane, Y);
  Y.tso[0] = xp.soff + 16384; Y.tm0[0] = XRING + st.xd + wid * 1024 + 16384; Y.tso[1] = wp.soff; Y.tm0[1] = (st.ws ^ 32768u) + 16384 + wid * 1024;
}

struct Desc { unsigned so, m0; };
struct Stream {
  u32x4 srd; unsigned tab, lane16;
  unsigned dseq, dslot, rslot, so;
  Desc y;
};
DI unsigned nslot(unsigned s) { return s >= 131072u ? s - 131072u : s + 32768u; }
DI void st_prep(Stream& S, Desc& D) { D.so = __builtin_amdgcn_readlane(S.tab, S.dseq); PINS(D.so); D.m0 = S.dslot; S.dslot = nslot(S.dslot); S.dseq += 1; PINS(S.dslot); }
template <int Q> DI void st_pre(Stream& S, const Desc& D) {
  if constexpr (Q == 0) { set_m0(D.m0); S.so = D.so; } else if constexpr (Q == 4) { set_m0(D.m0 + 4096u); S.so = D.so + 4096u; PINS(S.so); }
}
template <int Q> DI void st_go(Stream& S) { dma16<(Q & 3) * 1024, 1>(S.lane16, S.srd, S.so); }
DI void st_item(Stream& S) {
  sfor<8>([&](auto Q) __attribute__((always_inline)) { st_pre<decltype(Q)::v>(S, S.y); if constexpr (!(decltype(Q)::v & 3)) NOP0(); st_go<decltype(Q)::v>(S); });
  st_prep(S, S.y); asm volatile("s_nop 4" ::: "memory");
}
DI float maskv(float v, int dq, int rr) { return dq >= ((rr & 3) + 8 * (rr >> 2)) ? v : -__builtin_inff(); }
DI float max16(const f32x16& s) {
  float a = fmaxf(fmaxf(s[0], s[1]), s[2]), b = fmaxf(fmaxf(s[3], s[4]), s[5]);
  a = fmaxf(fmaxf(a, s[6]), s[7]); b = fmaxf(fmaxf(b, s[8]), s[9]); a = fmaxf(fmaxf(a, s[10]), s[11]); b = fmaxf(fmaxf(b, s[12]), s[13]);
  a = fmaxf(fmaxf(a, s[14]), s[15]); a = fmaxf(a, b);
  return hmax(a);
}
DI float max3f(float a, float b, float c) { float r; asm("v_max3_f32 %0, %1, %2, %3" : "=v"(r) : "v"(a), "v"(b), "v"(c)); return r; }
DI f32x16 splat16(float v) { f32x16 r; for (int i = 0; i < 16; ++i) r[i] = v; return r; }
DI void open_item(Stream& S, unsigned& rb_, unsigned off) { rb_ = S.rslot + off; PIN(rb_); S.rslot = nslot(S.rslot); PINS(S.rslot); }
DI void unopen_item(Stream& S) { S.rslot = S.rslot == 0u ? 131072u : S.rslot - 32768u; }
template <int N> DI void wait_lgkm() { __builtin_amdgcn_s_waitcnt(15 | (3 << 14) | (7 << 4) | (N << 8)); }
DI void set_c(f32x16& c0, f32x16& c1, float v, int dq) {
#pragma unroll
  for (int e = 0; e < 16; ++e) { c0[e] = maskv(v, dq, e); c1[e] = maskv(v, dq - 32, e); }
  PIN(c0); PIN(c1); asm volatile("s_nop 1" ::: "memory");
}

template <bool FIRST>
DI void b_tile(f32x16 (&o)[8], const bf16x8 (&q)[16], bf16x8 (&fr)[8], f32x16& c0, f32x16& c1, float& mref, float& rs0, float& rs1, Stream& S, unsigned& rb_) {
  f32x16 s0, s1; u32x4 p[4]; float pe[32], ma, mb_; Desc X;
  constexpr int E0 = FIRST ? 22 : 18;
  sfor<64>([&](auto J) __attribute__((always_inline)) { constexpr int j = decltype(J)::v;
    if constexpr (j == 24) { wait_bar<16, false>(); open_item(S, rb_, S.lane16); }
    if constexpr (j == 56) { wait_bar<11, false>(); open_item(S, rb_, S.lane16); }
    if constexpr ((j & 3) == 0) wait_lgkm<4>();
    if constexpr (j == 0) mfma_vc(s0, fr[0], q[0], c0);
    else if constexpr (j < 16) mfma_v(s0, fr[j & 7], q[j]);
    else if constexpr (j == 16) mfma_vc(s1, fr[0], q[0], c1);
    else if constexpr (j < 32) mfma_v(s1, fr[j & 7], q[j - 16]);
    else if constexpr (FIRST && j < 40) asm volatile("v_mfma_f32_32x32x16_bf16 %0, %1, %2, 0" : "=&a"(o[j & 7]) : "v"(fr[j & 7]), "v"(p[0]));
    else mfma_a(o[j & 7], fr[j & 7], __builtin_bit_cast(bf16x8, p[(j - 32) >> 3]));
    fr[j & 7] = ldsr(rb_ + ((j + 8) & 31) * 1024);
    if constexpr (j < 16 && (j & 1)) st_go<(j >> 1)>(S);
    if constexpr (j == 51 || j == 53 || j == 55 || j == 57 || j == 59 || j == 61) st_go<((j - 51) >> 1)>(S);
    if constexpr (j == 62) st_go<6>(S);
    if constexpr (j == 63) st_go<7>(S);
    if constexpr (FIRST) {
      if constexpr (j == 18) { ma = max3f(s0[0], s0[1], s0[2]); mb_ = max3f(s0[3], s0[4], s0[5]); ma = max3f(ma, s0[6], s0[7]); mb_ = max3f(mb_, s0[8], s0[9]); PIN(ma); PIN(mb_); }
      if constexpr (j == 19) { ma = max3f(ma, s0[10], s0[11]); mb_ = max3f(mb_, s0[12], s0[13]); ma = max3f(ma, s0[14], s0[15]); ma = fmaxf(ma, mb_); PIN(ma); }
      if constexpr (j == 20) { ma = hmax(ma); mref = ma == -__builtin_inff() ? 0.f : ma; PIN(mref); }
      if constexpr (j >= 56) { constexpr int e = 2 * (j - 56); float v = -mref; c0[e] = v; c0[e + 1] = v; c1[e] = v; c1[e + 1] = v; if constexpr (j == 63) { PIN(c0); PIN(c1); } }
    }
    if constexpr (j >= E0 && j < E0 + 32) { constexpr int e = j - E0;
      float v = e < 16 ? s0[e & 15] : s1[e & 15]; if constexpr (FIRST) v -= mref; v = __builtin_amdgcn_exp2f(v); PIN(v); pe[e] = v; }
    if constexpr (j >= E0 + 1 && j < E0 + 33) { constexpr int e = j - E0 - 1;
      if constexpr (e & 1) { rs1 += pe[e]; PIN(rs1); unsigned w = cvtpk(pe[e - 1], pe[e]); PIN(w); p[e >> 3][(e >> 1) & 3] = w; }
      else { rs0 += pe[e]; PIN(rs0); } }
    if constexpr (j == 0) st_pre<0>(S, S.y);
    if constexpr (j == 8) st_pre<4>(S, S.y);
    if constexpr (j == 16) st_prep(S, X);
    if constexpr (j == 17) st_prep(S, S.y);
    if constexpr (j == 50) st_pre<0>(S, X);
    if constexpr (j == 58) st_pre<4>(S, X);
    SBAR();
  });
}

DI void hand_step(const char* fh, const char* ml, const unsigned token, const int left, bool& late) {
  if (left == 2) {
    unsigned fw; asm volatile("v_accvgpr_read_b32 %0, a254" : "=v"(fw));
    late = rfl(fw) != token;
    asm volatile("global_load_dwordx2 a[252:253], %0, off sc1" ::"v"(ml) : "memory", "a252", "a253");
  }
  if (left == 3) asm volatile("global_load_dword a254, %0, off sc1" ::"v"(fh) : "memory", "a254");
}

DI void b_segment(f32x16 (&o)[8], const bf16x8 (&q)[16], float& mref, float& lsum, Stream& S, const int qb, const int t0, const int t1,
                  const unsigned lane, const unsigned wid, const char* fh, const char* ml, const unsigned token, const bool consume, float& mb, float& lb, bool& late, const unsigned pj TR_ARG) {
  TRC();
  const int qrow = qb * 128 + (int)wid * 32 + (int)(lane & 31) - 4 * (int)(lane >> 5);
  unsigned rb_; open_item(S, rb_, S.lane16);
  bf16x8 fr[8];
#pragma unroll
  for (int j = 0; j < 8; ++j) fr[j] = ldsr(rb_ + j * 1024);
  f32x16 c0, c1; float rs0 = 0.f, rs1 = 0.f;
  set_c(c0, c1, 0.f, qrow - t0 * 64);
  SBAR();
  b_tile<true>(o, q, fr, c0, c1, mref, rs0, rs1, S, rb_);
  const int tc = consume ? t1 : -8;
  for (int t = t0 + 1; t < t1; ++t) {
    TRC();
    hand_step(fh, ml, token, tc - t, late);
    if (tc - t == 2 && late) S.tab = lane - pj < 2u ? 0x7ffffff0u : S.tab;
    if (t >= 2 * qb) set_c(c0, c1, -mref, qrow - t * 64);
    b_tile<false>(o, q, fr, c0, c1, mref, rs0, rs1, S, rb_);
  }
  SBAR();
  asm volatile("v_accvgpr_read_b32 %0, a252\n\tv_accvgpr_read_b32 %1, a253" : "=v"(mb), "=v"(lb));
  MFMA_DRAIN_O(o); SBAR(); TRC();
  unopen_item(S);
  lsum = hsum(rs0 + rs1);
}

DI void a_finish(bf16x8 (&ca)[2], f32x16 o, const float g, const unsigned lane) {
  const bool up = lane >= 32;
  const float l = halves(o[12])[1];
  const float sc = g / l + l * 0.f;
#pragma unroll
  for (int e = 12; e < 16; ++e) o[e] = up ? 0.f : o[e];
  ca[0] = pack8(o, 0, sc); ca[1] = pack8(o, 1, sc);
}
DI bf16x8 ld16(__amdgpu_buffer_rsrc_t rs, unsigned voff, unsigned soff) { return __builtin_bit_cast(bf16x8, __builtin_amdgcn_raw_buffer_load_b128(rs, voff, soff, 16)); }
struct KvOut { __amdgpu_buffer_rsrc_t rs; unsigned dst, ns, ss, wof; };
DI void a_phase(bf16x8 (&ca)[2], bf16x8 (&q)[16], const unsigned qoff, const f32x16 (&acc)[4][4], const Sync& Y, const KvOut& KO, Stream& S, float& g, const char* gp, const unsigned lane,
                char* f2_mine, const char* f2_need, const unsigned token, const char* pc, const unsigned line, const unsigned wid TR_ARG) {
  TRC();
  const bf16x8 q0 = Y.qa[0], q1 = Y.qa[1];
  unsigned rb_, rn_; open_item(S, rb_, S.lane16); rn_ = rb_;
  bf16x8 kf[2][2], va[2], vb[2];
  kf[0][0] = ldsr(rb_); kf[0][1] = ldsr(rb_ + 1024); kf[1][0] = ldsr(rb_ + 4096); kf[1][1] = ldsr(rb_ + 5120);
  va[0] = ldsr(rb_ + 2048); vb[0] = ldsr(rb_ + 3072); va[1] = ldsr(rb_ + 4096 + 2048); vb[1] = vb[0];
  f32x16 s[2], o = {}, negm;
  { f32x16 z; asm volatile("v_mfma_f32_32x32x16_bf16 %0, %1, %2, 0" : "=&v"(z) : "v"(kf[0][0]), "v"(q0)); mfma_v(z, kf[0][1], q1); MFMA_DRAIN_V(z);
    negm = splat16(-max16(z)); PIN(negm); }
  u32x4 p0, p1 = {0u, 0u, 0u, 0u}, ow; float pt[4] = {0.f, 0.f, 0.f, 0.f}; unsigned fw;
  PIN(o); asm volatile("s_nop 1" ::: "memory");
  mfma_vc(s[0], kf[0][0], q0, negm); mfma_v(s[0], kf[0][1], q1);
  kf[0][0] = ldsr(rb_ + 8192); kf[0][1] = ldsr(rb_ + 9216);
  MFMA_DRAIN_V(s[0]);
  SBAR();
  sfor<64>([&](auto ST) __attribute__((always_inline)) { constexpr int st = decltype(ST)::v, it = st >> 3, kb = st & 7, c = kb & 1, n = c ^ 1;
#define AADDR(blk, off) ((blk) < 8 ? rb_ + (blk) * 4096 + (off) : rn_ + ((blk) - 8) * 4096 + (off))
#define AEXP(e) do { pe[e] = __builtin_amdgcn_exp2f(s[c][e]); PIN(pe[e]); } while (0)
#define ACVT(P, i, a, b) do { unsigned w_ = cvtpk(a, b); PIN(w_); P[i] = w_; } while (0)
    constexpr int se = (4 * st + 4) / 5, has_st = se < 32 && (5 * se) / 4 == st, sm = se >> 3, sn = (se >> 1) & 3, sh = se & 1;
    constexpr bool rq = it >= 2 && it <= 4 && kb < 4;
    constexpr int rb0 = st >= 53 && st < 61 ? 3 * (st - 53) : -1;
    if constexpr (kb == 0) { TRC(); rb_ = rn_; }
    if constexpr (st == 53) { if (wid == 0) { DRAIN(); asm volatile("" : "+v"(fw)); if (!__all(fw == token)) flag_wait(f2_need, token); } }
    if constexpr (kb == 5) { wait_bar<it == 0 ? 13 : it == 1 ? 8 : it == 4 ? 24 : it == 7 ? 24 : 63, false>(); open_item(S, rn_, S.lane16); }
    if constexpr (st == 42) DRAIN();
    if constexpr (st == 45) { if (wid == 0) flag_set(f2_mine, token); }
#define TGO(p) do { set_m0(Y.tm0[(p) >> 2] + ((p) & 3) * 4096); unsigned so_ = Y.tso[(p) >> 2] + ((p) & 3) * 4096; PINS(so_); NOP0(); dma16<0, 1>(S.lane16, S.srd, so_); } while (0)
#define BPRE(p) do { if constexpr (((p) & 3) == 0) { st_pre<(p) & 4>(S, S.y); NOP0(); } } while (0)
#define BGO(p) do { st_go<(p) & 7>(S); if constexpr (((p) & 7) == 7) st_prep(S, S.y); } while (0)
    float pe[16];
    if constexpr (st < 62) wait_lgkm<4>();
    if constexpr (st < 63) mfma_vc(s[n], kf[n][0], q0, negm);
    if constexpr (st < 61) kf[n][0] = ldsr(AADDR(kb + 3, 0));
    if constexpr (rb0 >= 0) { BPRE(rb0); BGO(rb0); q[2 * (st - 53)] = ld16(KO.rs, S.lane16, qoff + 2 * (st - 53) * 1024); }
    SBAR();
    AEXP(0); AEXP(1); ACVT(p1, 2, pt[0], pt[1]); AEXP(2); AEXP(3); ACVT(p1, 3, pt[2], pt[3]);
    if constexpr (has_st) { ACVT(ow, 0, ard(acc[sm][sn][8 * sh]), ard(acc[sm][sn][8 * sh + 1])); ACVT(ow, 1, ard(acc[sm][sn][8 * sh + 2]), ard(acc[sm][sn][8 * sh + 3])); }
    if constexpr (rq && (kb & 1) == 0) st_pre<(kb & 2) * 2>(S, S.y);
    SBAR();
    if constexpr (st < 63) mfma_v(s[n], kf[n][1], q1);
    if constexpr (st < 61) kf[n][1] = ldsr(AADDR(kb + 3, 1024));
    if constexpr (rq) st_go<2 * (kb & 3)>(S);
    if constexpr (st < 4) TGO(2 * st);
    if constexpr (st == 61) g = __uint_as_float(flag_get(gp));
    if constexpr (st == 41) touch(code_line(pc, LOFF(b), LOFF(c), line));
    if constexpr (rb0 >= 0) { BPRE(rb0 + 1); BGO(rb0 + 1); }
    if constexpr (st == 49) { if (wid == 0) asm volatile("global_load_dword %0, %1, off sc1" : "=v"(fw) : "v"(f2_need) : "memory"); }
    SBAR();
    AEXP(4); AEXP(5); ACVT(p0, 0, pe[0], pe[1]); AEXP(6); AEXP(7); ACVT(p0, 1, pe[2], pe[3]);
    if constexpr (has_st) { ACVT(ow, 2, ard(acc[sm][sn][8 * sh + 4]), ard(acc[sm][sn][8 * sh + 5])); ACVT(ow, 3, ard(acc[sm][sn][8 * sh + 6]), ard(acc[sm][sn][8 * sh + 7])); }
    SBAR();
    mfma_v(o, vb[n], __builtin_bit_cast(bf16x8, p1));
    if constexpr (st < 63) vb[n] = ldsr(AADDR(kb + 1, 3072));
    if constexpr (has_st) st16(KO.rs, S.lane16, KO.dst + sm * 16384 + sn * KO.ns + sh * KO.ss, __builtin_bit_cast(bf16x8, ow));
    if constexpr (st < 20 && !has_st) st16(KO.rs, S.lane16, KO.wof + (st / 5) * 1024, __builtin_bit_cast(bf16x8, Y.wq[st / 5]));
    if constexpr (rb0 >= 0) q[2 * (st - 53) + 1] = ld16(KO.rs, S.lane16, qoff + (2 * (st - 53) + 1) * 1024);
    SBAR();
    AEXP(8); ACVT(p0, 2, pe[4], pe[5]); AEXP(9); ACVT(p0, 3, pe[6], pe[7]); SBAR(); AEXP(10); AEXP(11);
    SBAR();
    mfma_v(o, va[c], __builtin_bit_cast(bf16x8, p0));
    if constexpr (st < 62) va[c] = ldsr(AADDR(kb + 2, 2048));
    if constexpr (rq) st_go<2 * (kb & 3) + 1>(S);
    if constexpr (st < 4) TGO(2 * st + 1);
    if constexpr (rb0 >= 0) { BPRE(rb0 + 2); BGO(rb0 + 2); }
    SBAR();
    AEXP(12); AEXP(13); ACVT(p1, 0, pe[8], pe[9]); AEXP(14); AEXP(15); ACVT(p1, 1, pe[10], pe[11]);
    pt[0] = pe[12]; pt[1] = pe[13]; pt[2] = pe[14]; pt[3] = pe[15];
    if constexpr (rq && (kb & 3) == 3) st_prep(S, S.y);
    SBAR();
#undef AADDR
#undef AEXP
#undef ACVT
#undef BPRE
#undef TGO
#undef BGO
  });
  asm volatile("" ::"v"(negm));
  DRAIN();
  p1[2] = cvtpk(pt[0], pt[1]); p1[3] = cvtpk(pt[2], pt[3]);
  mfma_vn(o, vb[1], __builtin_bit_cast(bf16x8, p1));
  MFMA_DRAIN_V(o);
  unopen_item(S);
  a_finish(ca, o, g, lane);
}

template <bool MERGE>
DI float out_proj(f32x16 (&o)[8], const bf16x8 (&ca)[2], const float fa, const float fb, Stream& S, const __amdgpu_buffer_rsrc_t rs, const unsigned og, const unsigned wid, unsigned& f3v, const char* f3p TR_ARG) {
  u32x4 pb[16], cb[16], ow; bf16x8 fr[8]; f32x16 acc[14]; unsigned rb_;
  if constexpr (MERGE) {
    open_item(S, rb_, wid * 8192 + S.lane16);
#pragma unroll
    for (int c = 0; c < 8; ++c) pb[c] = __builtin_bit_cast(u32x4, ldsr(rb_ + c * 1024));
    wait_bar<8, false>(); open_item(S, rb_, wid * 8192 + S.lane16);
#pragma unroll
    for (int c = 0; c < 8; ++c) pb[8 + c] = __builtin_bit_cast(u32x4, ldsr(rb_ + c * 1024));
    wait_bar<0, false>();
  }
  open_item(S, rb_, S.lane16);
#pragma unroll
  for (int j = 0; j < 8; ++j) fr[j] = ldsr(rb_ + j * 1024);
  SBAR();
  Desc X;
  sfor<252>([&](auto F) __attribute__((always_inline)) { constexpr int f = decltype(F)::v, f8 = f + 8, w = f >> 5, ph = f & 31;
    constexpr int nb = wf_nb(f), ks = wf_ks(f);
    if constexpr (ph == 24 && w < 7) { TRC();
      wait_bar<(MERGE && w == 0) ? 4 : (MERGE && w == 1) ? 12 : w < 5 ? 16 : 8, false>(); open_item(S, rb_, S.lane16); }
    if constexpr ((f & 3) == 0) wait_lgkm<4>();
    if constexpr (wf_isa(f)) { if constexpr (ks == 0) { const f32x16 z = {}; acc[nb] = MFMA(fr[f & 7], ca[0], z); } else acc[nb] = MFMA(fr[f & 7], ca[1], acc[nb]); }
    else acc[nb] = MFMA(fr[f & 7], __builtin_bit_cast(bf16x8, cb[ks]), acc[nb]);
    if constexpr (f8 < 252) fr[f & 7] = ldsr(rb_ + (f8 & 31) * 1024);
    constexpr bool r1 = MERGE ? w < 5 : w < 5, r2 = MERGE && w < 2;
    if constexpr (r1 && ph < 16 && (ph & 1)) st_go<(ph >> 1)>(S);
    if constexpr (r2 && ph >= 16 && (ph & 1)) st_go<((ph - 16) >> 1)>(S);
    if constexpr (r1 && (ph == 0 || ph == 8)) st_pre<ph / 2>(S, S.y);
    if constexpr (r2 && ph == 2) st_prep(S, X);
    if constexpr (r2 && (ph == 16 || ph == 24)) st_pre<(ph - 16) / 2>(S, X);
    if constexpr (r1 && ph == 18) st_prep(S, S.y);
    if constexpr (f == 236) asm volatile("global_load_dword %0, %1, off sc1" : "=v"(f3v) : "v"(f3p) : "memory");
    if constexpr (f < 96 && f % 6 < 4) { constexpr int c = f / 6, e = f % 6; const float x0 = ard(o[c >> 1][8 * (c & 1) + 2 * e]), x1 = ard(o[c >> 1][8 * (c & 1) + 2 * e + 1]);
      unsigned v = MERGE ? cvtpk(fmaf(x0, fa, bflo(pb[c][e]) * fb), fmaf(x1, fa, bfhi(pb[c][e]) * fb)) : cvtpk(x0 * fa, x1 * fa); PIN(v); cb[c][e] = v; }
    if constexpr (f >= 128 && f < 245 && (f - 128) % 9 < 8) { constexpr int t = (f - 128) / 9, u = (f - 128) % 9;
      unsigned v = cvtpk(ard(acc[t][2 * u]), ard(acc[t][2 * u + 1])); PIN(v); ow[u & 3] = v;
      if constexpr ((u & 3) == 3) st16(rs, S.lane16, og + (t * 2 + (u >> 2)) * 1024, __builtin_bit_cast(bf16x8, ow)); }
    SBAR();
  });
  st16(rs, S.lane16, og + 26 * 1024, pack8(acc[13], 0)); st16(rs, S.lane16, og + 27 * 1024, pack8(acc[13], 1));
  return acc[13][0];
}

DI void safe_redo(const __amdgpu_buffer_rsrc_t rs, const unsigned og, const unsigned head, const unsigned tb, const float g, const unsigned lane) {
  const unsigned lane16 = lane * 16;
  bf16x8 ca[2], cb[16];
  {
    const unsigned qa = (unsigned)WS_QA + (head * 64 + tb) * 2048; const bf16x8 q0 = ld16(rs, lane16, qa), q1 = ld16(rs, lane16, qa + 1024);
    float m = -1e30f; f32x16 o = splat16(opaque0());
    for (int kb = 0; kb < 64; ++kb) {
      const unsigned p = (unsigned)WS_KVA + (head * 64 + kb) * 4096;
      f32x16 t = {}; t = MFMA(ld16(rs, lane16, p), q0, t); t = MFMA(ld16(rs, lane16, p + 1024), q1, t);
      const float mn = fmaxf(m, max16(t)), al = __builtin_amdgcn_exp2f(m - mn); m = mn;
#pragma unroll
      for (int e = 0; e < 16; ++e) t[e] = __builtin_amdgcn_exp2f(t[e] - mn);
      o *= al; o = MFMA(ld16(rs, lane16, p + 2048), pack8(t, 0), o); o = MFMA(ld16(rs, lane16, p + 3072), pack8(t, 1), o);
    }
    a_finish(ca, o, g, lane);
  }
  {
    const unsigned qg = (unsigned)WS_QB + (head * 64 + tb) * 16384;
    const int qrow = (int)tb * 32 + (int)(lane & 31) - 4 * (int)(lane >> 5);
    float m = -1e30f, ls = 0.f; f32x16 o[8];
#pragma unroll
    for (int d = 0; d < 8; ++d) o[d] = splat16(opaque0());
    for (int kb = 0; kb <= (int)tb; ++kb) {
      const unsigned kp = (unsigned)WS_KB + (head * 64 + kb) * 16384, vp = (unsigned)WS_VB + (head * 64 + kb) * 16384;
      f32x16 s = {};
#pragma unroll
      for (int j = 0; j < 16; ++j) s = MFMA(ld16(rs, lane16, kp + j * 1024), ld16(rs, lane16, qg + j * 1024), s);
#pragma unroll
      for (int e = 0; e < 16; ++e) s[e] = maskv(s[e], qrow - kb * 32, e);
      const float mn = fmaxf(m, max16(s)), al = __builtin_amdgcn_exp2f(m - mn); m = mn; ls *= al;
#pragma unroll
      for (int e = 0; e < 16; ++e) { s[e] = __builtin_amdgcn_exp2f(s[e] - mn); ls += s[e]; }
      const bf16x8 p0 = pack8(s, 0), p1 = pack8(s, 1);
#pragma unroll
      for (int d = 0; d < 8; ++d) { o[d] *= al; o[d] = MFMA(ld16(rs, lane16, vp + d * 1024), p0, o[d]); o[d] = MFMA(ld16(rs, lane16, vp + 8192 + d * 1024), p1, o[d]); }
    }
    const float inv = (1.f - g) / hsum(ls);
#pragma unroll
    for (int c = 0; c < 16; ++c) cb[c] = pack8(o[c >> 1], c & 1, inv);
  }
  const unsigned wf = (unsigned)WS_WOF + head * 262144;
  DRAIN();
  for (int nb = 0; nb < 14; ++nb) {
    const int fa = nb < 7 ? 2 * nb : 126 + 18 * (nb - 7);
    f32x16 a = {}; a = MFMA(ld16(rs, lane16, wf + fa * 1024), ca[0], a); a = MFMA(ld16(rs, lane16, wf + (fa + 1) * 1024), ca[1], a);
#pragma unroll
    for (int ks = 0; ks < 16; ++ks) a = MFMA(ld16(rs, lane16, wf + (nb < 7 ? 14 + 7 * ks + nb : fa + 2 + ks) * 1024), cb[ks], a);
    st16(rs, lane16, og + (2 * nb) * 1024, pack8(a, 0)); st16(rs, lane16, og + (2 * nb + 1) * 1024, pack8(a, 1));
  }
}

__global__ __launch_bounds__(256, 1) void fused_fwd(const char* __restrict__ x, const char* __restrict__ wqkv, const char* __restrict__ ckv, const char* __restrict__ wq,
                                                    const char* __restrict__ wuk, const char* __restrict__ wuv, const char* __restrict__ gate_w,
                                                    const char* __restrict__ wo_a, const char* __restrict__ wo_b, char* ws, char* __restrict__ out) {
  extern __shared__ __attribute__((aligned(16))) char lds[];
  const unsigned wid = rfl(threadIdx.x >> 6);
  STAMP_DECL STAMPRT(0); STAMPIT(1); TR_DECL TRC();
  Sync Y; asm volatile("s_getpc_b64 %0\n.Lgm_base:" : "=s"(Y.pc)); Y.ws = ws; Y.lane = threadIdx.x & 63;
  asm volatile("global_load_dword %0, %1, off sc1" : "=v"(Y.token) : "v"(ws + WS_W0) : "memory");
  const __amdgpu_buffer_rsrc_t rs = __builtin_amdgcn_make_buffer_rsrc(ws, 0, (int)WS_END, 0x20000);
  Y.rs = rs;
  { const unsigned b_ = blockIdx.x, p_ = b_ >> 5; Y.qao = (unsigned)WS_QA + ((2 * (b_ & 7) + ((b_ >> 3) & 1)) * 64 + (((b_ >> 4) & 1) ? p_ : 15 - p_) * 4 + wid) * 2048; }
  f32x16 acc4[4][4];
  proj_phase(acc4, x, wqkv, ckv, wq, wuk, wuv, gate_w, wo_a, wo_b, ws, (unsigned)WS_KVA + (2 * (blockIdx.x & 7) + ((blockIdx.x >> 3) & 1)) * 262144, Y, Y.lane, wid TR_PASS);
  STAMPIT(2);
  LABEL(a);
  unsigned lane, bid = blockIdx.x;
  asm volatile("v_mbcnt_lo_u32_b32 %0, -1, 0\n\tv_mbcnt_hi_u32_b32 %0, -1, %0" : "=v"(lane)); PINS(bid);
  const unsigned lane16 = lane * 16, tid = wid * 64 + lane, head = 2 * (bid & 7) + ((bid >> 3) & 1), pi = bid >> 5, roleb = (bid >> 4) & 1;
  const unsigned token = Y.token;
  if (bid == 0 && wid == 0) flag_set(ws + WS_W0, token);
  const int qlong = 15 - (int)pi, nt1 = 32 - 2 * (int)pi - TA;
  const int qb = roleb ? (int)pi : qlong, nt2 = roleb ? 2 * (int)pi + 2 : TA;
  const bool produce = roleb && nt1 > 0, consume = !roleb && nt1 > 0;
  const unsigned pw = (head * 8 + pi) * 4 + wid;
  const unsigned po = (unsigned)WS_PO + pw * 16384, tb = qb * 4 + wid;
  Stream S; unsigned pj;
  {
    const unsigned n1 = 8 + (roleb ? 2 * nt1 : 0), n2 = n1 + 2 * nt2, n3 = n2 + (consume ? 2 : 0), j = lane;
    const unsigned jb = j < n1 ? j - 8 + 2 * TA : j - n1, kv = (unsigned)WS_KB + head * (unsigned)MB + (jb & 1) * (unsigned)(WS_VB - WS_KB) + (jb >> 1) * 32768;
    unsigned a = j >= 8 ? kv : (unsigned)WS_KVA + head * 262144 + j * 32768;
    a = j >= n3 ? (unsigned)WS_WOF + head * 262144 + (j - n3) * 32768 : a;
    a += wid * 8192;
    a = (j >= n2 && j < n3) ? po + (j - n2) * 8192 : a; pj = n2;
    S.tab = j < n3 + 8 ? a : 0x7ffffff0u; S.srd = mksrd(ws, (unsigned)WS_END); S.lane16 = lane16;
    S.dseq = 5; S.dslot = 65536 + wid * 8192; S.rslot = 65536;
  }
  st_prep(S, S.y);
  wait_bar<24, false>();
  asm volatile("" : "+v"(Y.qa[0]), "+v"(Y.qa[1]));
  float g;
  STAMPIT(3);
  bf16x8 ca[2], q[16];
  { const unsigned xcd = bid & 7, loc = bid >> 3, rb = loc & 7, sel = loc >> 3, head2 = 2 * xcd + (sel & 1), isV = sel >> 1, tb0 = rb * 8 + (wid >> 1) * 4;
    KvOut KO; KO.rs = rs; KO.dst = (isV ? (unsigned)WS_VB : (unsigned)WS_KB) + ((head2 * 64 + tb0) * 16 + (wid & 1) * (isV ? 4 : 8)) * 1024; KO.ns = isV ? 1024 : 2048; KO.ss = isV ? 8192 : 1024; KO.wof = (unsigned)WS_WOF + (bid * 4 + wid) * 4096;
    const unsigned cq = (256 * head + (lane & 1) * 255) / 192, rq_ = ((lane & 2) ? qlong : qb) >> 1;
    const unsigned need = lane < 16 ? (head >> 1) + 8 * ((lane & 7) + 8 * ((head & 1) + 2 * (lane >> 3))) : lane < 20 ? (cq & 7) + 8 * (rq_ + 8 * (cq >> 3)) : 16 * head + ((lane - 20) & 15);
    a_phase(ca, q, (unsigned)WS_QB + (head * 64 + (produce ? qlong * 4 + wid : tb)) * 16384, acc4, Y, KO, S, g, ws + WS_G + (tb * 32 + (lane & 31)) * 4, lane,
            ws + WS_F2 + bid * 4, ws + WS_F2 + need * 4, token, Y.pc, tid, wid TR_PASS); }
  STAMPIT(4);
  LABEL(b);
  char* fh = ws + WS_FH + pw * 64; char* ml = ws + WS_ML + pw * 512 + lane * 8;
  f32x16 o[8]; float mref, lsum, mb, lb; bool late = false;
  for (int pass = produce ? 0 : 1; pass < 2; ++pass) {
    b_segment(o, q, mref, lsum, S, pass ? qb : qlong, pass ? 0 : TA, pass ? nt2 : TA + nt1, lane, wid, fh, ml, token, consume, mb, lb, late, pj TR_PASS);
    if (pass == 0) {
#pragma unroll
      for (int j = 0; j < 16; ++j) q[j] = ld16(rs, lane16, (unsigned)WS_QB + ((head * 64 + tb) * 16 + j) * 1024);
      const float inv = lsum > 0.f ? 1.f / lsum : 0.f;
#pragma unroll
      for (int c = 0; c < 16; ++c) { if (!(c & 1)) PINA(o[c >> 1]); st16(rs, lane16, po + c * 1024, pack8(o[c >> 1], c & 1, inv)); SBAR(); }
      __hip_atomic_store((GAS unsigned long long*)ml, ((unsigned long long)__float_as_uint(lsum) << 32) | __float_as_uint(mref), __ATOMIC_RELAXED, __HIP_MEMORY_SCOPE_AGENT);
      DRAIN();
      flag_set(fh, token);
    }
  }
  STAMPIT(5);
  LABEL(c);
  const unsigned og = (unsigned)WS_OP + (head * 64 + tb) * 28 * 1024;
  float chk; unsigned f3v; const char* f3p = ws + WS_F3 + (tb * 16 + (lane & 15)) * 4;
  if (consume) {
    if (late) {
      flag_wait(fh, token);
      const unsigned long long w = __hip_atomic_load((GAS unsigned long long*)ml, __ATOMIC_RELAXED, __HIP_MEMORY_SCOPE_AGENT);
      mb = __uint_as_float((unsigned)w); lb = __uint_as_float((unsigned)(w >> 32));
      Desc D; D.so = po; D.m0 = S.rslot + wid * 8192;
      sfor<16>([&](auto Q) __attribute__((always_inline)) { constexpr int qq = decltype(Q)::v;
        if constexpr (qq == 8) { D.so = po + 8192; D.m0 = nslot(S.rslot) + wid * 8192; }
        st_pre<(qq & 7)>(S, D); if constexpr (!(qq & 3)) NOP0(); st_go<(qq & 7)>(S); });
      DRAIN();
    }
    const float m = lb > 0.f ? fmaxf(mref, mb) : mref, ea = __builtin_amdgcn_exp2f(mref - m), wb = lb > 0.f ? lb * __builtin_amdgcn_exp2f(mb - m) : lb * 0.f;
    const float den = lsum * ea + wb, inv = (1.f - g) / den;
    chk = den + out_proj<true>(o, ca, ea * inv, wb * inv, S, rs, og, wid, f3v, f3p TR_PASS);
  } else chk = lsum + out_proj<false>(o, ca, (1.f - g) / lsum, 0.f, S, rs, og, wid, f3v, f3p TR_PASS);
  STAMPIT(6);
  DRAIN(); asm volatile("" : "+v"(f3v));
  if (__builtin_expect(__any(!(chk * 0.f == 0.f)), 0)) { safe_redo(rs, og, head, tb, g, lane); DRAIN(); }
  flag_set(ws + WS_F3 + (tb * 16 + head) * 4, token);
  STAMPIT(7);
  if (!__all(f3v == token || (lane & 15) == head)) flag_wait(f3p, token);
  STAMPIT(8);
  {
    u32x4 v[2][16]; unsigned elo = 0x00003f80u, ehi = 0x3f800000u; PIN(elo); PIN(ehi);
    const unsigned c0 = head * 112 + lane, c1 = lane < 48 ? c0 + 64 : c0;
#pragma unroll
    for (int h = 0; h < 16; ++h) { v[0][h] = __builtin_bit_cast(u32x4, ld16(rs, c0 * 16, (unsigned)WS_OP + (h * 64 + tb) * 28 * 1024)); v[1][h] = __builtin_bit_cast(u32x4, ld16(rs, c1 * 16, (unsigned)WS_OP + (h * 64 + tb) * 28 * 1024)); }
    SBAR();
#pragma unroll
    for (int k = 0; k < 2; ++k) { float a[8] = {};
#pragma unroll
      for (int h = 0; h < 16; ++h)
#pragma unroll
        for (int e = 0; e < 4; ++e) { a[2 * e] = dot2(v[k][h][e], elo, a[2 * e]); a[2 * e + 1] = dot2(v[k][h][e], ehi, a[2 * e + 1]); }
      const unsigned c = k ? c1 : c0;
      char* op = out + (size_t)(tb * 32 + (c & 31)) * 896 + ((c >> 6) * 16 + 4 * ((c >> 5) & 1)) * 2;
      if (k == 0 || lane < 48) { *(u32x2*)op = (u32x2){cvtpk(a[0], a[1]), cvtpk(a[2], a[3])}; *(u32x2*)(op + 16) = (u32x2){cvtpk(a[4], a[5]), cvtpk(a[6], a[7])}; } }
  }
  LABEL(end);
  STAMPIT(9); STAMPRT(10); STAMP_OUT(1, 11); TRC(); TR_OUT();
}

extern "C" void kernel_launch(void* const* d_in, const int* in_sizes, int n_in, void* d_out, int out_size, void* d_ws, size_t ws_size, hipStream_t stream) {
  (void)in_sizes; (void)n_in; (void)out_size; (void)ws_size;
  (void)hipFuncSetAttribute((const void*)fused_fwd, hipFuncAttributeMaxDynamicSharedMemorySize, 163840);
  fused_fwd<<<256, 256, 163840, stream>>>((const char*)d_in[0], (const char*)d_in[1], (const char*)d_in[4], (const char*)d_in[5], (const char*)d_in[6], (const char*)d_in[7], (const char*)d_in[9],
                                          (const char*)d_in[2], (const char*)d_in[8], (char*)d_ws, (char*)d_out);
}
```

```cpp
#include <hip/hip_runtime.h>
#include <stdint.h>
#include <utility>

typedef short bf16x8 __attribute__((ext_vector_type(8)));
typedef short s16x4 __attribute__((ext_vector_type(4)));
typedef float f32x16 __attribute__((ext_vector_type(16)));
typedef float f32x4 __attribute__((ext_vector_type(4)));
typedef float f32x2 __attribute__((ext_vector_type(2)));
typedef __bf16 bf16x2_t __attribute__((ext_vector_type(2)));
typedef unsigned u32x4 __attribute__((ext_vector_type(4)));
typedef unsigned u32x2 __attribute__((ext_vector_type(2)));
#define LAS __attribute__((address_space(3)))
#define GAS __attribute__((address_space(1)))
#define DI __device__ __forceinline__
#define SBAR() __builtin_amdgcn_sched_barrier(0)
#define MFMA(a, b, c) __builtin_amdgcn_mfma_f32_32x32x16_bf16((a), (b), (c), 0, 0, 0)
#define PIN(x) asm volatile("" : "+v"(x))

constexpr size_t MB = 1u << 20;
constexpr size_t WS_QB = 0, WS_KB = 16 * MB, WS_VB = 32 * MB, WS_QA = 48 * MB, WS_KVA = 50 * MB, WS_G = 54 * MB, WS_W0 = 54 * MB + 65536, WS_F1 = WS_W0 + 4096, WS_F2 = WS_W0 + 8192, WS_F3 = WS_W0 + 16384, WS_FH = WS_W0 + 32768,
                 WS_ML = 55 * MB, WS_PO = 56 * MB, WS_OP = 64 * MB, WS_WOF = 92 * MB, WS_END = 96 * MB;
#define TA_ 18
constexpr int TA = TA_;
constexpr float LOG2E = 1.4426950408889634f;
constexpr float QSCALE_B = 0.0625f * LOG2E;
constexpr float QSCALE_A = 0.18898223650461362f * LOG2E;

template <int I> struct IC { static constexpr int v = I; };
template <class F, int... Is> DI void sfor_(F&& f, std::integer_sequence<int, Is...>) { (f(IC<Is>{}), ...); }
template <int N, class F> DI void sfor(F&& f) { sfor_(f, std::make_integer_sequence<int, N>{}); }
DI void mfma_vc(f32x16& d, const bf16x8& a, const bf16x8& b, const f32x16& c) { asm volatile("v_mfma_f32_32x32x16_bf16 %0, %1, %2, %3" : "=&v"(d) : "v"(a), "v"(b), "v"(c)); }
DI void mfma_v(f32x16& d, const bf16x8& a, const bf16x8& b) { asm volatile("v_mfma_f32_32x32x16_bf16 %0, %1, %2, %0" : "+v"(d) : "v"(a), "v"(b)); }
DI void mfma_vn(f32x16& d, const bf16x8& a, const bf16x8& b) { asm volatile("s_nop 1\n\tv_mfma_f32_32x32x16_bf16 %0, %1, %2, %0" : "+v"(d) : "v"(a), "v"(b)); }
DI void mfma_a(f32x16& d, const bf16x8& a, const bf16x8& b) { asm volatile("v_mfma_f32_32x32x16_bf16 %0, %1, %2, %0" : "+a"(d) : "v"(a), "v"(b)); }
#define MFMA_DRAIN_V(x) asm volatile("s_nop 7\n\ts_nop 7" : "+v"(x))
#define MFMA_DRAIN_O(o) asm volatile("s_nop 7\n\ts_nop 7" : "+a"(o[0]), "+a"(o[1]), "+a"(o[2]), "+a"(o[3]), "+a"(o[4]), "+a"(o[5]), "+a"(o[6]), "+a"(o[7]))
DI float ard(float a) { float r; asm volatile("v_accvgpr_read_b32 %0, %1" : "=v"(r) : "a"(a)); return r; }
DI float opaque0() { float z; asm volatile("v_mov_b32 %0, 0" : "=v"(z)); return z; }
DI unsigned wf_src(unsigned f, unsigned hd) { const bool isa = f < 32;
  const unsigned nb = isa ? (f < 28 ? f >> 1 : 0) : f < 64 ? (f & 1) * 13 : (f - 48) >> 4, ks = isa ? f & 1 : f < 64 ? (f - 32) >> 1 : f & 15;
  return ((hd * (isa ? 28 : 256) + 16 * ks) * 896 + 64 * nb) | (isa ? 0x40000000u : 0u) | (isa && ks == 1 ? 0x80000000u : 0u); }
DI unsigned rfl(unsigned x) { return __builtin_amdgcn_readfirstlane(x); }
DI unsigned cvtpk(float lo, float hi) { f32x2 v = {lo, hi}; bf16x2_t b = __builtin_convertvector(v, bf16x2_t); return __builtin_bit_cast(unsigned, b); }
DI float dot2(unsigned a, unsigned b, float c) { return __builtin_amdgcn_fdot2_f32_bf16(__builtin_bit_cast(bf16x2_t, a), __builtin_bit_cast(bf16x2_t, b), c, false); }
DI float bflo(unsigned w) { return __uint_as_float(w << 16); }
DI float bfhi(unsigned w) { return __uint_as_float(w & 0xffff0000u); }
DI u32x4 mksrd(const void* p, unsigned bytes) { u32x4 s = {(unsigned)(uintptr_t)p, (unsigned)((uintptr_t)p >> 32) & 0xffffu, bytes, 0x20000u}; return s; }
DI bf16x8 ldsr(unsigned a) { return *(const LAS bf16x8*)(uintptr_t)a; }
DI bf16x8 ldstr(unsigned a, unsigned a2) {
  s16x4 x = __builtin_amdgcn_ds_read_tr16_b64_v4i16((LAS s16x4*)(uintptr_t)a);
  s16x4 y = __builtin_amdgcn_ds_read_tr16_b64_v4i16((LAS s16x4*)(uintptr_t)a2);
  return __builtin_shufflevector(x, y, 0, 1, 2, 3, 4, 5, 6, 7);
}
DI void set_m0(unsigned v) { asm volatile("s_mov_b32 m0, %0" ::"s"(v) : "memory"); }
template <int OFF, int POL = 1> DI void dma16(unsigned voff, u32x4 srd, unsigned soff) {
  if constexpr (POL == 1) asm volatile("buffer_load_dwordx4 %0, %1, %2 offen offset:%3 sc1 lds" ::"v"(voff), "s"(srd), "s"(soff), "i"(OFF) : "memory");
  else if constexpr (POL == 2) asm volatile("buffer_load_dwordx4 %0, %1, %2 offen offset:%3 nt lds" ::"v"(voff), "s"(srd), "s"(soff), "i"(OFF) : "memory");
  else asm volatile("buffer_load_dwordx4 %0, %1, %2 offen offset:%3 lds" ::"v"(voff), "s"(srd), "s"(soff), "i"(OFF) : "memory");
}
DI void touch(const char* p) { asm volatile("global_load_dword a255, %0, off" ::"v"(p) : "memory", "a255"); }
#define LABEL(n) asm volatile(".Lgm_" #n ":" ::: "memory")
#define LOFF(n) ({ unsigned o_; asm volatile("s_mov_b32 %0, .Lgm_" #n "-.Lgm_base" : "=s"(o_)); o_; })
DI const char* code_line(const char* pc, unsigned from, unsigned to, unsigned line) { const unsigned o = from + line * 64; return pc + (o < to ? o : from); }
#define PINS(x) asm volatile("" : "+s"(x))
#define PINA(x) asm volatile("" : "+a"(x))
#define NOP0() asm volatile("s_nop 0" ::: "memory")
template <int V, bool L0> DI void wait_bar() {
  asm volatile("" ::: "memory");
  __builtin_amdgcn_s_waitcnt((V & 15) | ((V >> 4) << 14) | (7 << 4) | (L0 ? 0 : (15 << 8)));
  __builtin_amdgcn_s_barrier();
  asm volatile("" ::: "memory");
  SBAR();
}
template <int N> DI void wait_lgkm() { __builtin_amdgcn_s_waitcnt(15 | (3 << 14) | (7 << 4) | (N << 8)); }
DI bf16x8 pack8(const f32x16& a, int s, float sc) {
  u32x4 w = {cvtpk(a[8 * s] * sc, a[8 * s + 1] * sc), cvtpk(a[8 * s + 2] * sc, a[8 * s + 3] * sc), cvtpk(a[8 * s + 4] * sc, a[8 * s + 5] * sc), cvtpk(a[8 * s + 6] * sc, a[8 * s + 7] * sc)};
  return __builtin_bit_cast(bf16x8, w);
}
DI bf16x8 pack8(const f32x16& a, int s) {
  u32x4 w = {cvtpk(a[8 * s], a[8 * s + 1]), cvtpk(a[8 * s + 2], a[8 * s + 3]), cvtpk(a[8 * s + 4], a[8 * s + 5]), cvtpk(a[8 * s + 6], a[8 * s + 7])};
  return __builtin_bit_cast(bf16x8, w);
}
DI f32x2 halves(float v) {
  auto r = __builtin_amdgcn_permlane32_swap(__float_as_uint(v), __float_as_uint(v), false, false);
  return (f32x2){__uint_as_float(r[0]), __uint_as_float(r[1])};
}
DI float hmax(float v) { const f32x2 h = halves(v); return fmaxf(h[0], h[1]); }
DI float hsum(float v) { const f32x2 h = halves(v); return h[0] + h[1]; }

#define STAMP_DECL
#define STAMPIT(i)
#define STAMPRT(i)
#define STAMP_OUT(k, n)
#define TR_DECL
#define TR_ARG
#define TR_PASS
#define TRC()
#define TR_OUT()
__host__ __device__ constexpr int wf_b(int nb, int ks) { return nb == 0 ? 32 + 2 * ks : nb == 13 ? 33 + 2 * ks : 48 + 16 * nb + ks; }
constexpr unsigned XRING = 65536;

#define SPOL 1
struct Prov { u32x4 srd; unsigned voff[2], soff, pstr, kstr; };
DI void prov_next(Prov& p) { p.soff += p.kstr; }
struct K1State { unsigned xs, ws, xd; };
struct K1Regs { unsigned xl, xr0, xr1, mw, mx; };

template <int Q, int POL = 1> DI void k1_dma(Prov& p, unsigned m0b) { set_m0(m0b + Q * 4096); unsigned so = p.soff + Q * p.pstr; PINS(so); NOP0(); dma16<0, POL>(p.voff[Q & 1], p.srd, so); }

#define ST_AUX 16
DI void st16(__amdgpu_buffer_rsrc_t rs, unsigned voff, unsigned soff, bf16x8 v) {
  __builtin_amdgcn_raw_buffer_store_b128(__builtin_bit_cast(u32x4, v), rs, voff, soff, ST_AUX);
  asm volatile("s_nop 1" ::"v"(v) : "memory");
}

struct Sync { char* ws; unsigned token, lane; u32x4 fv;
  __amdgpu_buffer_rsrc_t rs; bf16x8 sq[24]; unsigned sqe, sql, sv; bool stored; bf16x8 qa[2]; unsigned qao, tso, tm0;
  const char* pc; unsigned pfd[2];
};
DI void flag_set(char* p, unsigned token) { __hip_atomic_store((GAS unsigned*)p, token, __ATOMIC_RELAXED, __HIP_MEMORY_SCOPE_AGENT); }
DI unsigned flag_get(const char* p) { return __hip_atomic_load((GAS unsigned*)p, __ATOMIC_RELAXED, __HIP_MEMORY_SCOPE_AGENT); }
constexpr unsigned SPIN_CAP = 1u << 20;
DI void flag_wait(const char* p, unsigned token) {
#pragma unroll 1
  for (unsigned i = 0; i < SPIN_CAP; ++i) { if (__all(flag_get(p) == token)) break; __builtin_amdgcn_s_sleep(1); }
}
#define DRAIN() do { asm volatile("s_waitcnt vmcnt(0)" ::: "memory"); __builtin_amdgcn_s_waitcnt(0x0f70); } while (0)

constexpr int g_go(int N2, int kh, int i) {
  if (N2 == 8) { const int m = i >> 3, n = i & 7; return n == 7 ? (kh ? ((m & 1) ? -1 : m >> 1) : (m < 4 ? m : 2 * m - 3)) : (n == 3 && !kh && m >= 4) ? 2 * m - 4 : -1; }
  return (i & 3) != 3 ? -1 : kh ? (((i & 7) == 7 && i < 32) ? i >> 3 : -1) : i >> 2; }
constexpr int g_tr(int N2, int i) {
  if (N2 == 8) return ((i >> 3) < 4 && (i & 7) >= 1 && (i & 7) <= 4) ? 4 * (i >> 3) + (i & 7) - 1 : -1;
  if ((i & 3) > 1 || i % 6 == 0) return -1;
  int c = 0; for (int j = 0; j < i; ++j) c += (j & 3) <= 1 && j % 6 != 0; return c < 12 ? c : -1; }
DI unsigned xnext(unsigned s) { return s == 65536u ? 0u : s + 32768u; }
template <bool SWAP, int NTL, bool FIRST, int QT>
DI void k1_ktile(f32x4 (&acc)[8 * NTL][2], bf16x8 (&xa)[4], s16x4 (&wf)[2][4 * NTL], K1Regs& R, unsigned (&wr)[2 * NTL], K1State& st, Prov& xp, Prov& wp,
                 const Prov& xn, const Prov& wn, const int t, const int NKT, const unsigned wid, Sync& Y) {
  constexpr int N2 = 2 * NTL; constexpr bool BOOT = FIRST && NTL == 3;
  sfor<2>([&](auto KH) __attribute__((always_inline)) { constexpr int kh = decltype(KH)::v;
    constexpr int VW = BOOT ? 8 : QT == 12 ? (kh ? 19 : 16) : QT > 12 ? 22 : 16;
    if constexpr (kh == 0 && QT == 14) {
      if (wid == 0) { asm volatile("s_waitcnt vmcnt(44)" : "+v"(Y.fv));
        if (!__all(Y.fv[0] == Y.token && Y.fv[1] == Y.token && Y.fv[2] == Y.token && Y.fv[3] == Y.token))
#pragma unroll 1
          for (int i = 0; i < 4; ++i) flag_wait(Y.ws + WS_F1 + Y.lane * 16 + i * 4, Y.token); } }
    if constexpr (QT == 0) { if (!Y.stored) asm volatile("s_waitcnt vmcnt(16)" ::: "memory"); wait_bar<40, false>(); }
    else if constexpr (!(BOOT && kh == 0)) wait_bar<VW, false>();
    const unsigned mw = R.mw + kh * 16384, mx = R.mx;
    const unsigned bx1 = XRING + xnext(st.xs) + wid * 1024, bw2 = R.mw ^ 32768u;
    unsigned so = 0, qso = 0;
    sfor<8 * N2>([&](auto I) __attribute__((always_inline)) { constexpr int i = decltype(I)::v, m = i / N2, n = i % N2, pr = SWAP ? NTL * m + (n >> 1) : N2 * (m >> 1) + n, hf = SWAP ? n & 1 : m & 1;
      const bf16x8 wv = __builtin_shufflevector(wf[kh][2 * n], wf[kh][2 * n + 1], 0, 1, 2, 3, 4, 5, 6, 7);
      constexpr int go = BOOT ? -1 : g_go(N2, kh, i);
#define K1_OPS "v"(SWAP ? wv : xa[m & 3]), "v"(SWAP ? xa[m & 3] : wv)
      if constexpr (go >= 0) {
        const Prov& pp = go < 4 ? wp : xp;
        if constexpr (FIRST && kh == 0) asm volatile("v_mfma_f32_16x16x32_bf16 %0, %1, %2, 0\n\tbuffer_load_dwordx4 %3, %4, %5 offen lds" : "=a"(acc[pr][hf]) : K1_OPS, "v"(pp.voff[go & 1]), "s"(pp.srd), "s"(so) : "memory");
        else if constexpr (QT >= 14) asm volatile("v_mfma_f32_16x16x32_bf16 %0, %1, %2, %0\n\tbuffer_load_dwordx4 %3, %4, %5 offen sc1 lds" : "+a"(acc[pr][hf]) : K1_OPS, "v"(pp.voff[go & 1]), "s"(pp.srd), "s"(so) : "memory");
        else asm volatile("v_mfma_f32_16x16x32_bf16 %0, %1, %2, %0\n\tbuffer_load_dwordx4 %3, %4, %5 offen lds" : "+a"(acc[pr][hf]) : K1_OPS, "v"(pp.voff[go & 1]), "s"(pp.srd), "s"(so) : "memory");
      } else if constexpr (FIRST && kh == 0) asm volatile("v_mfma_f32_16x16x32_bf16 %0, %1, %2, 0" : "=a"(acc[pr][hf]) : K1_OPS);
      else asm volatile("v_mfma_f32_16x16x32_bf16 %0, %1, %2, %0" : "+a"(acc[pr][hf]) : K1_OPS);
#undef K1_OPS
      SBAR();
      if constexpr (n == 0) xa[(m + 2) & 3] = ldsr(m < 6 ? (kh ? R.xr1 : R.xr0) + (m + 2) * 2048 : (kh ? R.xr0 : R.xr1) + (m - 6) * 2048);
      constexpr int tr = g_tr(N2, i);
      if constexpr (tr >= 0) wf[kh ^ 1][tr] = __builtin_amdgcn_ds_read_tr16_b64_v4i16((LAS s16x4*)(uintptr_t)(wr[tr >> 1] + (kh ? 0 : 16384) + (tr & 1) * 2048));
      if constexpr (BOOT) {
        constexpr int p = (i & 1) ? i >> 1 : -1;
        if constexpr (kh == 0 && p >= 0 && p < 20) { if constexpr (p < 4) k1_dma<p, 0>(wp, bw2); else if constexpr (p < 12) k1_dma<p - 4, 0>(xp, bx1); else if constexpr (p < 16) k1_dma<p - 12, 0>(wp, bw2 + 16384); else k1_dma<p - 16, 0>(xp, mx);
          if constexpr (p == 3 || p == 15) prov_next(wp); if constexpr (p == 11) prov_next(xp); }
        if constexpr (kh == 1 && p >= 0 && (p & 1) && p < 24) { constexpr int r = p >> 1; if constexpr (r < 4) k1_dma<r + 4, 0>(xp, mx); else if constexpr (r < 8) k1_dma<r - 4, 0>(wp, mw - 16384); else k1_dma<r - 8, 0>(wp, mw);
          if constexpr (r == 3) prov_next(xp); if constexpr (r == 7) prov_next(wp); }
      } else {
        constexpr int pm = g_go(N2, kh, i + (N2 == 8 ? 2 : 1)), ps = g_go(N2, kh, i + 1);
        if constexpr (pm >= 0 && i + 1 < 8 * N2) set_m0(pm < 4 ? mw + pm * 4096 : mx + (pm - 4) * 4096);
        if constexpr (ps >= 0 && i + 1 < 8 * N2) { so = ps == 0 ? wp.soff : ps == 4 ? xp.soff : so + (ps < 4 ? wp.pstr : xp.pstr); PINS(so); }
      }
      constexpr int wx = kh ? -1 : N2 == 8 ? ((m >= 4 && (n & 3) == 0) ? 2 * (m - 4) + (n >> 2) : -1) : i == 29 ? 0 : i == 32 ? 1 : i == 33 ? 2 : i == 37 ? 3 : i == 40 ? 4 : i == 41 ? 5 : -1;
      if constexpr (wx >= 0) { wr[wx] ^= 32768u; PIN(wr[wx]); }
      if constexpr (!BOOT && n == 0 && m == 2) { prov_next(wp); PINS(wp.soff); }
      if constexpr (!BOOT && kh == 0 && n == 0 && m == (N2 == 8 ? 5 : 4)) { prov_next(xp); PINS(xp.soff); }
      if constexpr (kh == 1 && m == 4 && n == 1) { st.xs = xnext(st.xs); PINS(st.xs); }
      if constexpr (kh == 1 && m == 4 && n == 3) { R.xr0 = R.xl + st.xs; PIN(R.xr0); }
      if constexpr (kh == 1 && m == 6 && n == 1) { R.xr1 = R.xr0 ^ 64u; PIN(R.xr1); }
      if constexpr (kh == 1 && m == 5 && n == 3) { R.mx = R.mx >= XRING + 65536u ? R.mx - 65536u : R.mx + 32768u; PINS(R.mx); }
      if constexpr (kh == 1 && m == 7 && n == 1) { R.mw ^= 32768u; PINS(R.mw); }
      if constexpr (kh == 1 && m == 7 && n == 2) { if (t == NKT - 3) { wp = wn; xp = xn; } }
      if constexpr (NTL == 4 && QT == -1 && kh == 1 && m == 7 && n == 4) { if (t == 6 && Y.stored) DRAIN(); }
      if constexpr (NTL == 4 && QT == -1 && kh == 1 && m == 5 && n == 5) { if (t == 7 && wid == 0) flag_set(Y.ws + WS_F1 + blockIdx.x * 4, Y.token); }
      if constexpr (n == N2 - (N2 == 8 ? 4 : 2) && (N2 == 6 || m >= 4)) wait_lgkm<N2 == 8 ? 1 : 3>();
      if constexpr (QT >= 12) { constexpr int le = 6 * (QT - 12) + 3 * kh + (m >> 1) - 1;
        if constexpr (m >= 2 && !(m & 1) && n == 0) { qso = __builtin_amdgcn_readlane(Y.sql, le); PINS(qso); }
        if constexpr (m >= 2 && !(m & 1) && n == (m < 4 ? 5 : 4)) st16(Y.rs, Y.sv, qso, Y.sq[le]); }
      if constexpr (QT == 14 && kh == 1 && n == 3 && (m == 5 || m == 7))
        asm volatile("s_nop 4\n\tbuffer_load_dwordx4 %0, %1, %2, %3 offen offset:%4 sc1" : "=v"(Y.qa[m == 7]) : "v"(Y.lane * 16), "s"(xn.srd), "s"(Y.qao), "i"(m == 7 ? 1024 : 0) : "memory");
      SBAR();
    });
    if constexpr (BOOT && kh == 1) prov_next(wp);
  });
}

template <bool SWAP, int NTL>
DI void k1_gemm(f32x4 (&acc)[8 * NTL][2], K1State& st, Prov& xp, Prov& wp, const Prov& xn, const Prov& wn, const int NKT, const unsigned wid, const unsigned lane, Sync& Y) {
  const unsigned c = lane & 15, g = lane >> 4, q = (lane >> 2) & 3, wrow = wid >> 1, wc = wid & 1;
  K1Regs R; unsigned wr[2 * NTL];
  R.xl = XRING + (128 * wrow + c) * 128 + 16 * (g ^ (c >> 1)); R.xr0 = R.xl + st.xs; R.xr1 = R.xr0 ^ 64u; R.mw = st.ws + wid * 1024; R.mx = XRING + st.xd + wid * 1024; PIN(R.xl); PIN(R.xr0); PIN(R.xr1);
#pragma unroll
  for (int n = 0; n < 2 * NTL; ++n) { wr[n] = st.ws + (8 * g + q) * 512 + 32 * ((2 * NTL * wc + n) ^ (q | ((g & 1) << 2))) + 8 * (lane & 3); PIN(wr[n]); }
  bf16x8 xa[4]; s16x4 wf[2][4 * NTL];
  if constexpr (NTL == 3) wait_bar<2, false>();
  xa[0] = ldsr(R.xr0); xa[1] = ldsr(R.xr0 + 2048);
#pragma unroll
  for (int n = 0; n < 4 * NTL; ++n) wf[0][n] = __builtin_amdgcn_ds_read_tr16_b64_v4i16((LAS s16x4*)(uintptr_t)(wr[n >> 1] + (n & 1) * 2048));
  SBAR();
  k1_ktile<SWAP, NTL, true, NTL == 4 ? 0 : -1>(acc, xa, wf, R, wr, st, xp, wp, xn, wn, 0, NKT, wid, Y);
  if constexpr (NTL == 4) {
    for (int t = 1; t < 12; ++t) k1_ktile<SWAP, NTL, false, -1>(acc, xa, wf, R, wr, st, xp, wp, xn, wn, t, NKT, wid, Y);
    asm volatile("global_load_dword %0, %2, off\n\tglobal_load_dword %1, %3, off" : "=&v"(Y.pfd[0]), "=&v"(Y.pfd[1])
                 : "v"(code_line(Y.pc, LOFF(a), LOFF(b), wid * 64 + lane)), "v"(code_line(Y.pc, LOFF(a), LOFF(b), 256 + wid * 64 + lane)) : "memory");
    if (wid == 0) asm volatile("global_load_dwordx4 %0, %1, off sc1" : "=v"(Y.fv) : "v"(Y.ws + WS_F1 + Y.lane * 16) : "memory");
    sfor<4>([&](auto T) __attribute__((always_inline)) { constexpr int t = 12 + decltype(T)::v; k1_ktile<SWAP, NTL, false, t>(acc, xa, wf, R, wr, st, xp, wp, xn, wn, t, 16, wid, Y); });
    asm volatile("" ::"v"(Y.pfd[0]), "v"(Y.pfd[1]));
  } else
    for (int t = 1; t < NKT; ++t) k1_ktile<SWAP, NTL, false, -1>(acc, xa, wf, R, wr, st, xp, wp, xn, wn, t, NKT, wid, Y);
  st.ws = R.mw - wid * 1024; st.xd = R.mx - XRING - wid * 1024;
  asm volatile("s_nop 7" ::: "memory"); SBAR();
}

DI Prov mk_xprov(const char* X, unsigned ld, unsigned bytes, unsigned rb, unsigned wid, unsigned lane, bool vperm) {
  Prov p; p.srd = mksrd(X, bytes);
  p.voff[0] = p.voff[1] = (lane >> 3) * ld + 16 * ((lane & 7) ^ (4 * (wid & 1) + (lane >> 4)));
  p.soff = (256 * rb + 8 * (vperm ? ((wid & 1) << 1) | (wid >> 1) : wid)) * ld; p.pstr = 32 * ld; p.kstr = 128; return p;
}
template <class F> DI Prov mk_wprov(const char* W, unsigned ld, unsigned bytes, unsigned wid, unsigned lane, F colf) {
  Prov p; p.srd = mksrd(W, bytes);
#pragma unroll
  for (int o = 0; o < 2; ++o) { const unsigned ch = (lane & 31) ^ ((2 * (wid & 1) + (lane >> 5) + 4 * o) << 1), col = colf(ch);
    p.voff[o] = col < 0x10000u ? (lane >> 5) * ld + 2 * col : 0x7ffffff0u; }
  p.soff = 2 * wid * ld; p.pstr = 8 * ld; p.kstr = 32 * ld; return p;
}
DI u32x4 pack44(const f32x4& a, const f32x4& b, float sc) { return (u32x4){cvtpk(a[0] * sc, a[1] * sc), cvtpk(a[2] * sc, a[3] * sc), cvtpk(b[0] * sc, b[1] * sc), cvtpk(b[2] * sc, b[3] * sc)}; }
DI void proj_phase(f32x4 (&acc4)[32][2], const char* x, const char* wqkv, const char* ckv, const char* wq, const char* wuk, const char* wuv, const char* gate_w,
                   const char* wo_a, const char* wo_b, char* ws, const unsigned kva_off, Sync& Y, const unsigned lane, const unsigned wid TR_ARG) {
  const unsigned wrow = wid >> 1, wc = wid & 1, lane16 = lane * 16;
  const unsigned bid = blockIdx.x, xcd = bid & 7, loc = bid >> 3, rb = loc & 7, sel = loc >> 3;
  const unsigned cb = xcd + 8 * sel;
  const unsigned head2 = 2 * xcd + (sel & 1), isV = sel >> 1;
  const Prov xkv = mk_xprov(ckv, 2048, 2048 * 2048, rb, wid, lane, isV);
  const Prov wkv = mk_wprov(isV ? wuv : wuk, 8192, 1024 * 8192, wid, lane, [&](unsigned ch) { return head2 * 256 + 8 * ch; });
  Prov xp = cb < 31 ? mk_xprov(x, 896, 2048 * 896, rb, wid, lane, cb >= 28) : xkv;
  Prov wp = cb < 22 ? mk_wprov(wq, 8192, 448 * 8192, wid, lane, [&](unsigned ch) { return ch < 24 ? cb * 192 + 8 * ch : 0x10000u; })
          : cb < 31 ? mk_wprov(wqkv, 2688, 448 * 2688, wid, lane, [&](unsigned ch) { const unsigned vc = (cb < 28 ? cb - 22 : cb - 28) * 24 + ch, slot = vc >> 2;
                         return (ch < 24 && slot < (cb < 28 ? 32u : 16u)) ? (cb < 28 ? 0u : 896u) + 28 * slot + 8 * (vc & 3) : 0x10000u; })
          : wkv;
  K1State st = cb < 31 ? K1State{32768u, 32768u, 0u} : K1State{65536u, 0u, 32768u};
  const unsigned x0 = XRING + st.xs + wid * 1024, w0 = st.ws + wid * 1024;
  sfor<8>([&](auto I) __attribute__((always_inline)) { k1_dma<decltype(I)::v, 0>(xp, x0); }); prov_next(xp);
  sfor<4>([&](auto I) __attribute__((always_inline)) { k1_dma<decltype(I)::v, 0>(wp, w0); }); prov_next(wp);
  sfor<4>([&](auto I) __attribute__((always_inline)) { k1_dma<decltype(I)::v, 0>(wp, w0 + 16384); }); prov_next(wp);
  touch(code_line(Y.pc, 0, LOFF(end), (rb * 4 + wid) * 64 + lane));
  { const unsigned e = wf_src(((bid * 4 + wid) & 63) * 4 + (lane >> 4), bid >> 4), row = ((int)e < 0 && (lane & 15) >= 12) ? 0 : lane & 15;
    touch(((e & 0x40000000u) ? wo_a : wo_b) + (e & 0x3fffffffu) + row * 896); }
  Y.stored = cb >= 22; Y.sv = (lane >> 5) * 1024 + ((lane >> 4) & 1) * 512 + (lane & 15) * 16;
  const unsigned tb0 = rb * 8 + wrow * 4;
  {
    const bool vt = cb >= 28; const unsigned hi = vt ? lane / 6 : lane / 3, lo = lane - hi * (vt ? 6 : 3), tb = tb0 + (vt ? hi : hi >> 1), odd = (vt ? lo : hi) & 1;
    const unsigned c32 = (cb < 22 ? cb : cb < 28 ? cb - 22 : cb - 28) * 6 + wc * 3 + (vt ? lo >> 1 : lo);
    unsigned d = cb < 22 ? (unsigned)WS_QB + (((c32 >> 3) * 64 + tb) * 16 + (c32 & 7) * 2) * 1024
               : vt ? (unsigned)WS_KVA + (c32 * 64 + tb) * 4096 + 2048
               : c32 < 16 ? (unsigned)WS_QA + (c32 * 64 + tb) * 2048 : (unsigned)WS_KVA + ((c32 - 16) * 64 + tb) * 4096;
    d += odd * 256;
    if (cb == 31 || c32 >= (cb < 22 ? 128u : cb < 28 ? 32u : 16u)) d = 0x7ffffff0u;
    Y.sqe = cb >= 22 ? d : 0x7ffffff0u; Y.sql = cb < 22 ? d : 0x7ffffff0u;
  }
  if (cb < 31) {
    f32x4 acc[24][2];
    if (cb < 28) {
      k1_gemm<true, 3>(acc, st, xp, wp, xkv, wkv, 7, wid, lane, Y);
      TRC();
      const bool pad = cb >= 22 && lane >= 48;
#pragma unroll
      for (int e = 0; e < 24; ++e) { const float sc = cb < 22 ? QSCALE_B : (cb - 22) * 6 + wc * 3 + e % 3 < 16 ? QSCALE_A : 1.f;
        PINA(acc[e][0]); PINA(acc[e][1]); u32x4 w = pack44(acc[e][0], acc[e][1], sc); if (pad) { w[2] = 0u; w[3] = 0u; }
        Y.sq[e] = __builtin_bit_cast(bf16x8, w); PIN(Y.sq[e]); if (e & 1) SBAR(); }
    } else {
      k1_gemm<false, 3>(acc, st, xp, wp, xkv, wkv, 7, wid, lane, Y);
      TRC();
      const unsigned fill = (lane & 15) == 12 ? 0x3f803f80u : 0u;
#pragma unroll
      for (int e = 0; e < 24; ++e) { PINA(acc[e][0]); PINA(acc[e][1]); u32x4 w = pack44(acc[e][0], acc[e][1], 1.f); if ((e & 1) && (lane & 15) >= 12) w = (u32x4){fill, fill, fill, fill};
        Y.sq[e] = __builtin_bit_cast(bf16x8, w); PIN(Y.sq[e]); if (e & 1) SBAR(); }
    }
  } else {
    { const unsigned x1 = XRING + wid * 1024, w2 = (st.ws ^ 32768u) + wid * 1024;
      sfor<4>([&](auto I) __attribute__((always_inline)) { k1_dma<decltype(I)::v, 0>(wp, w2); }); prov_next(wp);
      sfor<8>([&](auto I) __attribute__((always_inline)) { k1_dma<decltype(I)::v, 0>(xp, x1); }); prov_next(xp);
      sfor<4>([&](auto I) __attribute__((always_inline)) { k1_dma<decltype(I)::v, 0>(wp, w2 + 16384); }); prov_next(wp); }
    u32x4 gw_[7];
#pragma unroll
    for (int i = 0; i < 7; ++i) gw_[i] = *(const u32x4*)(gate_w + (8 * i + (lane & 7)) * 16);
#pragma unroll 2
    for (int p = 0; p < 8; ++p) { const unsigned rw = rb * 256 + wid * 64 + p * 8 + (lane >> 3); float s = 0.f;
#pragma unroll
      for (int i = 0; i < 7; ++i) { const u32x4 a = *(const u32x4*)(x + rw * 896 + (8 * i + (lane & 7)) * 16);
#pragma unroll
        for (int e = 0; e < 4; ++e) s = dot2(a[e], gw_[i][e], s); }
      s += __shfl_xor(s, 1); s += __shfl_xor(s, 2); s += __shfl_xor(s, 4);
      flag_set(ws + WS_G + rw * 4, __float_as_uint(1.f / (1.f + __expf(-s)))); }
#pragma unroll
    for (int e = 0; e < 24; ++e) { Y.sq[e] = bf16x8{}; PIN(Y.sq[e]); }
    wait_bar<0, false>();
    TRC();
  }
  if (Y.stored) sfor<24>([&](auto E) __attribute__((always_inline)) { constexpr int e = decltype(E)::v;
    unsigned so = __builtin_amdgcn_readlane(Y.sqe, e); asm volatile("s_nop 4" : "+s"(so)); st16(Y.rs, Y.sv, so, Y.sq[e]); });
  asm volatile("" : "+v"(Y.token)); Y.token = rfl(Y.token) + 1u;
  TRC();
  Prov xa_, wa_; xa_.srd = mksrd(ws, (unsigned)WS_END); xa_.voff[0] = xa_.voff[1] = lane16; xa_.soff = kva_off + wid * 1024; xa_.pstr = 4096; xa_.kstr = 32768;
  wa_ = xa_; wa_.soff += 3 * 32768; wa_.kstr = 16384;
  if (!isV) k1_gemm<true, 4>(acc4, st, xp, wp, xa_, wa_, 16, wid, lane, Y);
  else k1_gemm<false, 4>(acc4, st, xp, wp, xa_, wa_, 16, wid, lane, Y);
  Y.tso = xp.soff; Y.tm0 = XRING + st.xd + wid * 1024;
}

struct Desc { unsigned so, m0; };
struct Stream {
  u32x4 srd; unsigned tab, lane16;
  unsigned dseq, dslot, rslot, so;
  Desc y;
};
DI unsigned nslot(unsigned s) { return s >= 131072u ? s - 131072u : s + 32768u; }
DI void st_prep(Stream& S, Desc& D) { D.so = __builtin_amdgcn_readlane(S.tab, S.dseq); PINS(D.so); D.m0 = S.dslot; S.dslot = nslot(S.dslot); S.dseq += 1; PINS(S.dslot); }
template <int Q> DI void st_pre(Stream& S, const Desc& D) {
  if constexpr (Q == 0) { set_m0(D.m0); S.so = D.so; } else if constexpr (Q == 4) { set_m0(D.m0 + 4096u); S.so = D.so + 4096u; PINS(S.so); }
}
template <int Q> DI void st_go(Stream& S) { dma16<(Q & 3) * 1024, SPOL>(S.lane16, S.srd, S.so); }
DI void st_item(Stream& S) {
  sfor<8>([&](auto Q) __attribute__((always_inline)) { st_pre<decltype(Q)::v>(S, S.y); if constexpr (!(decltype(Q)::v & 3)) NOP0(); st_go<decltype(Q)::v>(S); });
  st_prep(S, S.y); asm volatile("s_nop 4" ::: "memory");
}
DI float maskv(float v, int dq, int rr) { return dq >= ((rr & 3) + 8 * (rr >> 2)) ? v : -__builtin_inff(); }
DI float max16(const f32x16& s) {
  float a = fmaxf(fmaxf(s[0], s[1]), s[2]), b = fmaxf(fmaxf(s[3], s[4]), s[5]);
  a = fmaxf(fmaxf(a, s[6]), s[7]); b = fmaxf(fmaxf(b, s[8]), s[9]); a = fmaxf(fmaxf(a, s[10]), s[11]); b = fmaxf(fmaxf(b, s[12]), s[13]);
  a = fmaxf(fmaxf(a, s[14]), s[15]); a = fmaxf(a, b);
  return hmax(a);
}
DI float max3f(float a, float b, float c) { float r; asm("v_max3_f32 %0, %1, %2, %3" : "=v"(r) : "v"(a), "v"(b), "v"(c)); return r; }
DI f32x16 splat16(float v) { f32x16 r; for (int i = 0; i < 16; ++i) r[i] = v; return r; }
DI void open_item(Stream& S, unsigned& rb_, unsigned off) { rb_ = S.rslot + off; PIN(rb_); S.rslot = nslot(S.rslot); PINS(S.rslot); }
DI void unopen_item(Stream& S) { S.rslot = S.rslot == 0u ? 131072u : S.rslot - 32768u; }
DI void set_c(f32x16& c0, f32x16& c1, float v, int dq) {
#pragma unroll
  for (int e = 0; e < 16; ++e) { c0[e] = maskv(v, dq, e); c1[e] = maskv(v, dq - 32, e); }
  PIN(c0); PIN(c1); asm volatile("s_nop 1" ::: "memory");
}

template <bool FIRST>
DI void b_tile(f32x16 (&o)[8], const bf16x8 (&q)[16], bf16x8 (&fr)[8], f32x16& c0, f32x16& c1, float& mref, float& rs0, float& rs1, Stream& S, unsigned& rb_) {
  f32x16 s0, s1; u32x4 p[4]; float pe[32], ma, mb_; Desc X;
  constexpr int E0 = FIRST ? 22 : 18;
  sfor<64>([&](auto J) __attribute__((always_inline)) { constexpr int j = decltype(J)::v;
    if constexpr (j == 24) { wait_bar<16, false>(); open_item(S, rb_, S.lane16); }
    if constexpr (j == 56) { wait_bar<11, false>(); open_item(S, rb_, S.lane16); }
    if constexpr ((j & 3) == 0) wait_lgkm<4>();
    if constexpr (j == 0) mfma_vc(s0, fr[0], q[0], c0);
    else if constexpr (j < 16) mfma_v(s0, fr[j & 7], q[j]);
    else if constexpr (j == 16) mfma_vc(s1, fr[0], q[0], c1);
    else if constexpr (j < 32) mfma_v(s1, fr[j & 7], q[j - 16]);
    else if constexpr (FIRST && j < 40) asm volatile("v_mfma_f32_32x32x16_bf16 %0, %1, %2, 0" : "=&a"(o[j & 7]) : "v"(fr[j & 7]), "v"(p[0]));
    else mfma_a(o[j & 7], fr[j & 7], __builtin_bit_cast(bf16x8, p[(j - 32) >> 3]));
    fr[j & 7] = ldsr(rb_ + ((j + 8) & 31) * 1024);
    if constexpr (j < 16 && (j & 1)) st_go<(j >> 1)>(S);
    if constexpr (j == 51 || j == 53 || j == 55 || j == 57 || j == 59 || j == 61) st_go<((j - 51) >> 1)>(S);
    if constexpr (j == 62) st_go<6>(S);
    if constexpr (j == 63) st_go<7>(S);
    SBAR();
    if constexpr (FIRST) {
      if constexpr (j == 18) { ma = max3f(s0[0], s0[1], s0[2]); mb_ = max3f(s0[3], s0[4], s0[5]); ma = max3f(ma, s0[6], s0[7]); mb_ = max3f(mb_, s0[8], s0[9]); PIN(ma); PIN(mb_); }
      if constexpr (j == 19) { ma = max3f(ma, s0[10], s0[11]); mb_ = max3f(mb_, s0[12], s0[13]); ma = max3f(ma, s0[14], s0[15]); ma = fmaxf(ma, mb_); PIN(ma); }
      if constexpr (j == 20) { ma = hmax(ma); mref = ma == -__builtin_inff() ? 0.f : ma; PIN(mref); }
      if constexpr (j >= 56) { constexpr int e = 2 * (j - 56); float v = -mref; c0[e] = v; c0[e + 1] = v; c1[e] = v; c1[e + 1] = v; if constexpr (j == 63) { PIN(c0); PIN(c1); } }
    }
    if constexpr (j >= E0 && j < E0 + 32) { constexpr int e = j - E0;
      float v = e < 16 ? s0[e & 15] : s1[e & 15]; if constexpr (FIRST) v -= mref; v = __builtin_amdgcn_exp2f(v); PIN(v); pe[e] = v; }
    if constexpr (j >= E0 + 1 && j < E0 + 33) { constexpr int e = j - E0 - 1;
      if constexpr (e & 1) { rs1 += pe[e]; PIN(rs1); unsigned w = cvtpk(pe[e - 1], pe[e]); PIN(w); p[e >> 3][(e >> 1) & 3] = w; }
      else { rs0 += pe[e]; PIN(rs0); } }
    if constexpr (j == 0) st_pre<0>(S, S.y);
    if constexpr (j == 8) st_pre<4>(S, S.y);
    if constexpr (j == 16) st_prep(S, X);
    if constexpr (j == 17) st_prep(S, S.y);
    if constexpr (j == 50) st_pre<0>(S, X);
    if constexpr (j == 58) st_pre<4>(S, X);
    SBAR();
  });
}

DI void hand_step(const char* fh, const char* ml, const unsigned token, const int left, bool& late) {
  if (left == 2) {
    unsigned fw; asm volatile("v_accvgpr_read_b32 %0, a254" : "=v"(fw));
    late = rfl(fw) != token;
    asm volatile("global_load_dwordx2 a[252:253], %0, off sc1" ::"v"(ml) : "memory", "a252", "a253");
  }
  if (left == 3) asm volatile("global_load_dword a254, %0, off sc1" ::"v"(fh) : "memory", "a254");
}

DI void b_segment(f32x16 (&o)[8], const bf16x8 (&q)[16], float& mref, float& lsum, Stream& S, const int qb, const int t0, const int t1,
                  const unsigned lane, const unsigned wid, const char* fh, const char* ml, const unsigned token, const bool consume, float& mb, float& lb, bool& late, const unsigned pj TR_ARG) {
  TRC();
  const int qrow = qb * 128 + (int)wid * 32 + (int)(lane & 31) - 4 * (int)(lane >> 5);
  unsigned rb_; open_item(S, rb_, S.lane16);
  bf16x8 fr[8];
#pragma unroll
  for (int j = 0; j < 8; ++j) fr[j] = ldsr(rb_ + j * 1024);
  f32x16 c0, c1; float rs0 = 0.f, rs1 = 0.f;
  set_c(c0, c1, 0.f, qrow - t0 * 64);
  SBAR();
  b_tile<true>(o, q, fr, c0, c1, mref, rs0, rs1, S, rb_);
  const int tc = consume ? t1 : -8;
  for (int t = t0 + 1; t < t1; ++t) {
    TRC();
    hand_step(fh, ml, token, tc - t, late);
    if (tc - t == 2 && late) S.tab = lane - pj < 2u ? 0x7ffffff0u : S.tab;
    if (t >= 2 * qb) set_c(c0, c1, -mref, qrow - t * 64);
    b_tile<false>(o, q, fr, c0, c1, mref, rs0, rs1, S, rb_);
  }
  SBAR();
  asm volatile("v_accvgpr_read_b32 %0, a252\n\tv_accvgpr_read_b32 %1, a253" : "=v"(mb), "=v"(lb));
  MFMA_DRAIN_O(o); SBAR(); TRC();
  unopen_item(S);
  lsum = hsum(rs0 + rs1);
}

DI void a_finish(bf16x8 (&ca)[2], f32x16 o, const float g, const unsigned lane) {
  const bool up = lane >= 32;
  const float l = halves(o[12])[1];
  const float sc = g / l + l * 0.f;
#pragma unroll
  for (int e = 12; e < 16; ++e) o[e] = up ? 0.f : o[e];
  ca[0] = pack8(o, 0, sc); ca[1] = pack8(o, 1, sc);
}
DI bf16x8 ld16(__amdgpu_buffer_rsrc_t rs, unsigned voff, unsigned soff) { return __builtin_bit_cast(bf16x8, __builtin_amdgcn_raw_buffer_load_b128(rs, voff, soff, 16)); }
struct KvOut { __amdgpu_buffer_rsrc_t rs; unsigned dst, s2, s1, s0, sv, wof, gtab, gv0; const char* woa; const char* wob; };
constexpr int a_ops(int st) {
  return (st < 32) + (st < 4 ? 2 : 0) + (st < 16 ? 2 : 0) + (st >= 26 && st < 30) + (((st >= 16 && st < 20) || (st >= 24 && st < 28) || (st >= 38 && st < 42)) ? 2 : 0) + (st == 43) + (st == 52) + (st == 56) + (st >= 57 ? 6 : 0); }
constexpr int a_sum(int a, int b) { int n = 0; for (int i = a; i < b; ++i) n += a_ops(i); return n; }
DI void a_phase(bf16x8 (&ca)[2], bf16x8 (&q)[16], const unsigned qoff, const f32x4 (&acc)[32][2], const Sync& Y, const KvOut& KO, Stream& S, float& g, const char* gp, const unsigned lane,
                char* f2_mine, const char* f2_need, const unsigned token, const char* pc, const unsigned line, const unsigned wid TR_ARG) {
  TRC();
  const bf16x8 q0 = Y.qa[0], q1 = Y.qa[1];
  unsigned rb_, rn_; open_item(S, rb_, S.lane16); rn_ = rb_;
  bf16x8 kf[2][2], va[2], vb[2];
  kf[0][0] = ldsr(rb_); kf[0][1] = ldsr(rb_ + 1024); kf[1][0] = ldsr(rb_ + 4096); kf[1][1] = ldsr(rb_ + 5120);
  va[0] = ldsr(rb_ + 2048); vb[0] = ldsr(rb_ + 3072); va[1] = ldsr(rb_ + 4096 + 2048); vb[1] = vb[0];
  f32x16 s[2], o = {}, negm;
  { f32x16 z; asm volatile("v_mfma_f32_32x32x16_bf16 %0, %1, %2, 0" : "=&v"(z) : "v"(kf[0][0]), "v"(q0)); mfma_v(z, kf[0][1], q1); MFMA_DRAIN_V(z);
    negm = splat16(-max16(z)); PIN(negm); }
  u32x4 p0, p1 = {0u, 0u, 0u, 0u}, ow, gsrd; float pt[4] = {0.f, 0.f, 0.f, 0.f}; unsigned fw, gt[4][8], gso, gv1;
  PIN(o); asm volatile("s_nop 1" ::: "memory");
  mfma_vc(s[0], kf[0][0], q0, negm); mfma_v(s[0], kf[0][1], q1);
  kf[0][0] = ldsr(rb_ + 8192); kf[0][1] = ldsr(rb_ + 9216);
  MFMA_DRAIN_V(s[0]);
  SBAR();
#define AADDR(blk, off) ((blk) < 8 ? rb_ + (blk) * 4096 + (off) : rn_ + ((blk) - 8) * 4096 + (off))
#define AEXP(e) do { pe[e] = __builtin_amdgcn_exp2f(s[c][e]); PIN(pe[e]); } while (0)
#define ACVT(P, i, a, b) do { unsigned w_ = cvtpk(a, b); PIN(w_); P[i] = w_; } while (0)
#define TGO(p) do { set_m0(Y.tm0 + (p) * 4096); unsigned so_ = Y.tso + (p) * 4096; PINS(so_); NOP0(); dma16<0, 1>(S.lane16, S.srd, so_); } while (0)
#define BGO(p) do { if constexpr (((p) & 3) == 0) { st_pre<(p) & 4>(S, S.y); NOP0(); } st_go<(p) & 7>(S); if constexpr (((p) & 7) == 7) { st_prep(S, S.y); asm volatile("s_nop 3" ::: "memory"); } } while (0)
#define GDESC(f) do { const unsigned e_ = __builtin_amdgcn_readlane(KO.gtab, f); gsrd = (e_ & 0x40000000u) ? mksrd(KO.woa, 448 * 896) : mksrd(KO.wob, 4096 * 896); gso = e_ & 0x3fffffffu; \
    gv1 = ((int)e_ < 0 && lane >= 32) ? 0x7ffffff0u : KO.gv0 + 8 * 896; PINS(gsrd); PINS(gso); PIN(gv1); } while (0)
#define GLD(j) asm volatile("buffer_load_ushort %0, %1, %2, %3 offen offset:%4" : "=v"(gt[st >> 2][j]) : "v"((j) < 4 ? KO.gv0 : gv1), "s"(gsrd), "s"(gso), "i"(((j) & 3) * 896) : "memory")
#define TAIL(u) do { constexpr int u_ = 6 * (st - 57) + (u);     \
    if constexpr (u_ < 24) { if constexpr (u_ % 3 == 2) BGO(u_ / 3); else asm volatile("buffer_load_dwordx4 %0, %1, %2, %3 offen sc1" : "=v"(q[2 * (u_ / 3) + u_ % 3]) : "v"(S.lane16), "s"(S.srd), "s"(qoff + (2 * (u_ / 3) + u_ % 3) * 1024) : "memory"); } \
    else if constexpr (u_ < 40) BGO(u_ - 16); } while (0)
  GDESC(0); asm volatile("s_nop 4" ::: "memory");
  sfor<64>([&](auto ST) __attribute__((always_inline)) { constexpr int st = decltype(ST)::v, it = st >> 3, kb = st & 7, c = kb & 1, n = c ^ 1;
    constexpr bool has_st = st < 32; constexpr int se = st & 31;
    constexpr int rq = (st >= 16 && st < 20) ? st - 16 : (st >= 24 && st < 28) ? st - 24 : (st >= 38 && st < 42) ? st - 38 : -1;
    if constexpr (kb == 0) { TRC(); rb_ = rn_; }
    if constexpr (st == 36) DRAIN();
    if constexpr (kb == 5 && it < 7) { wait_bar<it == 0 ? a_sum(0, 5) : it == 1 ? a_sum(4, 13) : it == 6 ? a_sum(42, 53) : 63, false>(); open_item(S, rn_, S.lane16); }
    if constexpr (st == 37) { if (wid == 0) flag_set(f2_mine, token); }
    if constexpr (st == 26) asm volatile("s_waitcnt vmcnt(%0)" ::"i"(a_sum(16, 26)) : "memory");
    if constexpr (st == 57) { asm volatile("s_waitcnt vmcnt(1)" : "+v"(fw)); if (!__all(fw == token)) flag_wait(f2_need, token); }
    float pe[16];
    if constexpr (st < 62) wait_lgkm<4>();
    if constexpr (st < 63) mfma_vc(s[n], kf[n][0], q0, negm);
    if constexpr (st < 61) kf[n][0] = ldsr(AADDR(kb + 3, 0));
    if constexpr (st < 16) GLD(2 * (st & 3));
    if constexpr (st >= 57) { TAIL(0); TAIL(1); }
    SBAR();
    AEXP(0); AEXP(1); ACVT(p1, 2, pt[0], pt[1]); AEXP(2); AEXP(3); ACVT(p1, 3, pt[2], pt[3]);
    if constexpr (has_st) { ACVT(ow, 0, ard(acc[se][0][0]), ard(acc[se][0][1])); ACVT(ow, 1, ard(acc[se][0][2]), ard(acc[se][0][3])); }
    if constexpr (rq >= 0 && (rq & 1) == 0) st_pre<(rq & 2) * 2>(S, S.y);
    SBAR();
    if constexpr (st < 63) mfma_v(s[n], kf[n][1], q1);
    if constexpr (st < 61) kf[n][1] = ldsr(AADDR(kb + 3, 1024));
    if constexpr (rq >= 0) st_go<2 * (rq & 3)>(S);
    if constexpr (st < 4) TGO(2 * st);
    if constexpr (st == 43) touch(code_line(pc, LOFF(b), LOFF(c), line));
    if constexpr (st == 52) asm volatile("global_load_dword %0, %1, off sc1" : "=v"(fw) : "v"(f2_need) : "memory");
    if constexpr (st == 56) asm volatile("global_load_dword %0, %1, off sc1" : "=v"(g) : "v"(gp) : "memory");
    if constexpr (st >= 57) TAIL(2);
    SBAR();
    AEXP(4); AEXP(5); ACVT(p0, 0, pe[0], pe[1]); AEXP(6); AEXP(7); ACVT(p0, 1, pe[2], pe[3]);
    if constexpr (has_st) { ACVT(ow, 2, ard(acc[se][1][0]), ard(acc[se][1][1])); ACVT(ow, 3, ard(acc[se][1][2]), ard(acc[se][1][3])); }
    SBAR();
    mfma_v(o, vb[n], __builtin_bit_cast(bf16x8, p1));
    if constexpr (st < 63) vb[n] = ldsr(AADDR(kb + 1, 3072));
    if constexpr (has_st) st16(KO.rs, KO.sv, KO.dst + (se >> 3) * 16384 + ((se >> 2) & 1) * KO.s2 + ((se >> 1) & 1) * KO.s1 + (se & 1) * KO.s0, __builtin_bit_cast(bf16x8, ow));
    if constexpr (st >= 57) { TAIL(3); TAIL(4); }
    SBAR();
    AEXP(8); ACVT(p0, 2, pe[4], pe[5]); AEXP(9); ACVT(p0, 3, pe[6], pe[7]); SBAR(); AEXP(10); AEXP(11);
    if constexpr (st >= 26 && st < 30) {
#pragma unroll
      for (int e = 0; e < 4; ++e) { asm volatile("" : "+v"(gt[st - 26][2 * e]), "+v"(gt[st - 26][2 * e + 1])); ow[e] = gt[st - 26][2 * e] | (gt[st - 26][2 * e + 1] << 16); }
      PIN(ow); SBAR(); }
    SBAR();
    mfma_v(o, va[c], __builtin_bit_cast(bf16x8, p0));
    if constexpr (st < 62) va[c] = ldsr(AADDR(kb + 2, 2048));
    if constexpr (rq >= 0) st_go<2 * (rq & 3) + 1>(S);
    if constexpr (st < 4) TGO(2 * st + 1);
    if constexpr (st < 16) GLD(2 * (st & 3) + 1);
    if constexpr (st >= 26 && st < 30) st16(KO.rs, S.lane16, KO.wof + (st - 26) * 1024, __builtin_bit_cast(bf16x8, ow));
    if constexpr (st >= 57) TAIL(5);
    SBAR();
    if constexpr (st < 12 && (st & 3) == 3) GDESC((st >> 2) + 1);
    AEXP(12); AEXP(13); ACVT(p1, 0, pe[8], pe[9]); AEXP(14); AEXP(15); ACVT(p1, 1, pe[10], pe[11]);
    pt[0] = pe[12]; pt[1] = pe[13]; pt[2] = pe[14]; pt[3] = pe[15];
    if constexpr (rq == 3) st_prep(S, S.y);
    SBAR();
  });
#undef AADDR
#undef AEXP
#undef ACVT
#undef TGO
#undef BGO
#undef GDESC
#undef GLD
#undef TAIL
  asm volatile("" ::"v"(negm));
  p1[2] = cvtpk(pt[0], pt[1]); p1[3] = cvtpk(pt[2], pt[3]);
  mfma_vn(o, vb[1], __builtin_bit_cast(bf16x8, p1));
  MFMA_DRAIN_V(o);
  wait_bar<16, false>();
  asm volatile("" : "+v"(g), "+v"(q[0]), "+v"(q[1]), "+v"(q[2]), "+v"(q[3]), "+v"(q[4]), "+v"(q[5]), "+v"(q[6]), "+v"(q[7]));
  asm volatile("" : "+v"(q[8]), "+v"(q[9]), "+v"(q[10]), "+v"(q[11]), "+v"(q[12]), "+v"(q[13]), "+v"(q[14]), "+v"(q[15]));
  a_finish(ca, o, g, lane);
}

DI void skip_slot(unsigned& s, unsigned res) { s = s == res ? nslot(s) : s; PINS(s); }
template <bool MERGE>
DI void op_head(f32x16 (&o)[8], const bf16x8 (&ca)[2], u32x4 (&cb)[16], f32x16& c0, f32x16& z, bf16x8 (&fr)[6], const float fa, const float fb, Stream& S, unsigned& ra, unsigned& rb_, unsigned& res, const unsigned wid) {
  u32x4 pb[16];
#define CONV(c, e) do { const float x0 = ard(o[(c) >> 1][8 * ((c) & 1) + 2 * (e)]), x1 = ard(o[(c) >> 1][8 * ((c) & 1) + 2 * (e) + 1]); \
    unsigned v_ = MERGE ? cvtpk(fmaf(x0, fa, bflo(pb[c][e]) * fb), fmaf(x1, fa, bfhi(pb[c][e]) * fb)) : cvtpk(x0 * fa, x1 * fa); PIN(v_); cb[c][e] = v_; } while (0)
  if constexpr (MERGE) {
    sfor<2>([&](auto H) __attribute__((always_inline)) { constexpr int h = decltype(H)::v;
      open_item(S, rb_, wid * 8192 + S.lane16);
      sfor<8>([&](auto C) __attribute__((always_inline)) { constexpr int c = decltype(C)::v; st_pre<c>(S, S.y); pb[8 * h + c] = __builtin_bit_cast(u32x4, ldsr(rb_ + c * 1024)); st_go<c>(S); SBAR(); });
      st_prep(S, S.y);
      wait_bar<16, false>(); });
  }
  res = S.rslot; open_item(S, ra, S.lane16);
  fr[0] = ldsr(ra); fr[1] = ldsr(ra + 26 * 1024); fr[2] = ldsr(ra + 1024); fr[3] = ldsr(ra + 27 * 1024);
  CONV(0, 0); CONV(0, 1); CONV(0, 2); CONV(0, 3);
  wait_bar<8, false>(); open_item(S, rb_, S.lane16);
  fr[4] = ldsr(rb_); fr[5] = ldsr(rb_ + 1024);
  SBAR();
  sfor<36>([&](auto U) __attribute__((always_inline)) { constexpr int u = decltype(U)::v, v = u + 6; const f32x16 zero = {};
    if constexpr (u == 32) { wait_bar<8, false>(); skip_slot(S.rslot, res); open_item(S, rb_, S.lane16); }
    if constexpr (u == 0) c0 = MFMA(fr[0], ca[0], zero); else if constexpr (u == 1) z = MFMA(fr[1], ca[0], zero);
    else if constexpr (u == 2) c0 = MFMA(fr[2], ca[1], c0); else if constexpr (u == 3) z = MFMA(fr[3], ca[1], z);
    else if constexpr (u & 1) z = MFMA(fr[u % 6], __builtin_bit_cast(bf16x8, cb[(u - 4) >> 1]), z);
    else c0 = MFMA(fr[u % 6], __builtin_bit_cast(bf16x8, cb[(u - 4) >> 1]), c0);
    SBAR();
    fr[u % 6] = ldsr(v < 36 ? rb_ + (v - 4) * 1024 : v < 38 ? ra + (v - 34) * 1024 : rb_ + (v - 38) * 1024);
    if constexpr (u < 16 && (u & 1)) st_go<(u >> 1)>(S);
    SBAR();
    if constexpr (u == 0 || u == 8) st_pre<u / 2>(S, S.y);
    if constexpr (u == 18) { skip_slot(S.dslot, res + wid * 8192); st_prep(S, S.y); }
    if constexpr (u < 30) { CONV(1 + (u >> 1), 2 * (u & 1)); CONV(1 + (u >> 1), 2 * (u & 1) + 1); }
    SBAR(); });
#undef CONV
}
DI float op_body(const bf16x8 (&ca)[2], const u32x4 (&cb)[16], f32x16& c0, f32x16& z, bf16x8 (&fr)[6], Stream& S, unsigned ra, unsigned rb_, const unsigned res_,
                 const __amdgpu_buffer_rsrc_t rs, unsigned og, const unsigned wid, unsigned& f3v, const char* f3p TR_ARG) {
  f32x16 c1; u32x4 ow; const unsigned res = rfl(res_);
  S.rslot = rfl(S.rslot); S.dslot = rfl(S.dslot); S.dseq = rfl(S.dseq); S.y.so = rfl(S.y.so); S.y.m0 = rfl(S.y.m0);
#pragma unroll 1
  for (int k = 0; k < 6; ++k) {
    TRC();
    sfor<36>([&](auto U) __attribute__((always_inline)) { constexpr int u = decltype(U)::v, v = u + 6, w = u % 18; const f32x16 zero = {};
      f32x16& cur = u < 18 ? c1 : c0; f32x16& prv = u < 18 ? c0 : c1;
      if constexpr (u == 32) { wait_bar<11, false>(); skip_slot(S.rslot, res); open_item(S, rb_, S.lane16); }
      if constexpr (w == 0) cur = MFMA(fr[u % 6], ca[0], zero); else if constexpr (w == 1) cur = MFMA(fr[u % 6], ca[1], cur); else cur = MFMA(fr[u % 6], __builtin_bit_cast(bf16x8, cb[w - 2]), cur);
      SBAR();
      fr[u % 6] = ldsr(v < 18 ? rb_ + (v - 2) * 1024 : v < 20 ? ra + (v - 14) * 1024 : v < 36 ? rb_ + (v - 4) * 1024 : v < 38 ? ra + (v - 30) * 1024 : rb_ + (v - 38) * 1024);
      if constexpr (u % 3 == 1 && u < 32 && u != 10 && u != 28 && u != 31) st_go<(u < 10 ? u / 3 : u / 3 - 1)>(S);
      if constexpr (w == 10 || w == 14) st16(rs, S.lane16 + (u >= 18 ? 2048 : 0) + (w == 14 ? 1024 : 0), og, __builtin_bit_cast(bf16x8, ow));
      SBAR();
      if constexpr (u == 0 || u == 15) st_pre<u ? 4 : 0>(S, S.y);
      if constexpr (u == 26) { skip_slot(S.dslot, res + wid * 8192); st_prep(S, S.y); }
      if constexpr (w >= 6 && w < 14) { unsigned x = cvtpk(ard(prv[2 * (w - 6)]), ard(prv[2 * (w - 6) + 1])); PIN(x); ow[(w - 6) & 3] = x; }
      if constexpr (u == 35) { ra += 4096; PIN(ra); og += 4096; }
      SBAR(); });
  }
  asm volatile("global_load_dword %0, %1, off sc1" : "=v"(f3v) : "v"(f3p) : "memory");
  st16(rs, S.lane16 + 2048, og, pack8(z, 0)); st16(rs, S.lane16 + 3072, og, pack8(z, 1));
  st16(rs, S.lane16, og, pack8(c0, 0)); st16(rs, S.lane16 + 1024, og, pack8(c0, 1));
  return z[0];
}

DI void safe_redo(const __amdgpu_buffer_rsrc_t rs, const unsigned og, const unsigned head, const unsigned tb, const float g, const unsigned lane) {
  const unsigned lane16 = lane * 16;
  bf16x8 ca[2], cb[16];
  {
    const unsigned qa = (unsigned)WS_QA + (head * 64 + tb) * 2048; const bf16x8 q0 = ld16(rs, lane16, qa), q1 = ld16(rs, lane16, qa + 1024);
    float m = -1e30f; f32x16 o = splat16(opaque0());
    for (int kb = 0; kb < 64; ++kb) {
      const unsigned p = (unsigned)WS_KVA + (head * 64 + kb) * 4096;
      f32x16 t = {}; t = MFMA(ld16(rs, lane16, p), q0, t); t = MFMA(ld16(rs, lane16, p + 1024), q1, t);
      const float mn = fmaxf(m, max16(t)), al = __builtin_amdgcn_exp2f(m - mn); m = mn;
#pragma unroll
      for (int e = 0; e < 16; ++e) t[e] = __builtin_amdgcn_exp2f(t[e] - mn);
      o *= al; o = MFMA(ld16(rs, lane16, p + 2048), pack8(t, 0), o); o = MFMA(ld16(rs, lane16, p + 3072), pack8(t, 1), o);
    }
    a_finish(ca, o, g, lane);
  }
  {
    const unsigned qg = (unsigned)WS_QB + (head * 64 + tb) * 16384;
    const int qrow = (int)tb * 32 + (int)(lane & 31) - 4 * (int)(lane >> 5);
    float m = -1e30f, ls = 0.f; f32x16 o[8];
#pragma unroll
    for (int d = 0; d < 8; ++d) o[d] = splat16(opaque0());
    for (int kb = 0; kb <= (int)tb; ++kb) {
      const unsigned kp = (unsigned)WS_KB + (head * 64 + kb) * 16384, vp = (unsigned)WS_VB + (head * 64 + kb) * 16384;
      f32x16 s = {};
#pragma unroll
      for (int j = 0; j < 16; ++j) s = MFMA(ld16(rs, lane16, kp + j * 1024), ld16(rs, lane16, qg + j * 1024), s);
#pragma unroll
      for (int e = 0; e < 16; ++e) s[e] = maskv(s[e], qrow - kb * 32, e);
      const float mn = fmaxf(m, max16(s)), al = __builtin_amdgcn_exp2f(m - mn); m = mn; ls *= al;
#pragma unroll
      for (int e = 0; e < 16; ++e) { s[e] = __builtin_amdgcn_exp2f(s[e] - mn); ls += s[e]; }
      const bf16x8 p0 = pack8(s, 0), p1 = pack8(s, 1);
#pragma unroll
      for (int d = 0; d < 8; ++d) { o[d] *= al; o[d] = MFMA(ld16(rs, lane16, vp + d * 1024), p0, o[d]); o[d] = MFMA(ld16(rs, lane16, vp + 8192 + d * 1024), p1, o[d]); }
    }
    const float inv = (1.f - g) / hsum(ls);
#pragma unroll
    for (int c = 0; c < 16; ++c) cb[c] = pack8(o[c >> 1], c & 1, inv);
  }
  const unsigned wf = (unsigned)WS_WOF + head * 262144;
  DRAIN();
  for (int nb = 0; nb < 14; ++nb) {
    const int fa = 2 * nb;
    f32x16 a = {}; a = MFMA(ld16(rs, lane16, wf + fa * 1024), ca[0], a); a = MFMA(ld16(rs, lane16, wf + (fa + 1) * 1024), ca[1], a);
#pragma unroll
    for (int ks = 0; ks < 16; ++ks) a = MFMA(ld16(rs, lane16, wf + wf_b(nb, ks) * 1024), cb[ks], a);
    st16(rs, lane16, og + (2 * nb) * 1024, pack8(a, 0)); st16(rs, lane16, og + (2 * nb + 1) * 1024, pack8(a, 1));
  }
}

__global__ __launch_bounds__(256, 1) void fused_fwd(const char* __restrict__ x, const char* __restrict__ wqkv, const char* __restrict__ ckv, const char* __restrict__ wq,
                                                    const char* __restrict__ wuk, const char* __restrict__ wuv, const char* __restrict__ gate_w,
                                                    const char* __restrict__ wo_a, const char* __restrict__ wo_b, char* ws, char* __restrict__ out) {
  extern __shared__ __attribute__((aligned(16))) char lds[];
  const unsigned wid = rfl(threadIdx.x >> 6);
  STAMP_DECL STAMPRT(0); STAMPIT(1); TR_DECL TRC();
  Sync Y; asm volatile("s_getpc_b64 %0\n.Lgm_base:" : "=s"(Y.pc)); Y.ws = ws; Y.lane = threadIdx.x & 63;
  asm volatile("global_load_dword %0, %1, off sc1" : "=v"(Y.token) : "v"(ws + WS_W0) : "memory");
  const __amdgpu_buffer_rsrc_t rs = __builtin_amdgcn_make_buffer_rsrc(ws, 0, (int)WS_END, 0x20000);
  Y.rs = rs;
  { const unsigned b_ = blockIdx.x, p_ = b_ >> 5; Y.qao = (unsigned)WS_QA + ((2 * (b_ & 7) + ((b_ >> 3) & 1)) * 64 + (((b_ >> 4) & 1) ? p_ : 15 - p_) * 4 + wid) * 2048; }
  f32x4 acc4[32][2];
  proj_phase(acc4, x, wqkv, ckv, wq, wuk, wuv, gate_w, wo_a, wo_b, ws, (unsigned)WS_KVA + (2 * (blockIdx.x & 7) + ((blockIdx.x >> 3) & 1)) * 262144, Y, Y.lane, wid TR_PASS);
  STAMPIT(2); STAMPRT(11);
  LABEL(a);
  unsigned lane, bid = blockIdx.x;
  asm volatile("v_mbcnt_lo_u32_b32 %0, -1, 0\n\tv_mbcnt_hi_u32_b32 %0, -1, %0" : "=v"(lane)); PINS(bid);
  const unsigned lane16 = lane * 16, tid = wid * 64 + lane, head = 2 * (bid & 7) + ((bid >> 3) & 1), pi = bid >> 5, roleb = (bid >> 4) & 1;
  const unsigned token = Y.token;
  if (bid == 0 && wid == 0) flag_set(ws + WS_W0, token);
  const int qlong = 15 - (int)pi, nt1 = 32 - 2 * (int)pi - TA;
  const int qb = roleb ? (int)pi : qlong, nt2 = roleb ? 2 * (int)pi + 2 : TA;
  const bool produce = roleb && nt1 > 0, consume = !roleb && nt1 > 0;
  const unsigned pw = (head * 8 + pi) * 4 + wid;
  const unsigned po = (unsigned)WS_PO + pw * 16384, tb = qb * 4 + wid;
  Stream S; unsigned pj;
  {
    const unsigned n1 = 8 + (roleb ? 2 * nt1 : 0), n2 = n1 + 2 * nt2, n3 = n2 + (consume ? 2 : 0), j = lane;
    const unsigned jb = j < n1 ? j - 8 + 2 * TA : j - n1, kv = (unsigned)WS_KB + head * (unsigned)MB + (jb & 1) * (unsigned)(WS_VB - WS_KB) + (jb >> 1) * 32768;
    unsigned a = j >= 8 ? kv : (unsigned)WS_KVA + head * 262144 + j * 32768;
    a = j >= n3 ? (unsigned)WS_WOF + head * 262144 + (j - n3) * 32768 : a;
    a += wid * 8192;
    a = (j >= n2 && j < n3) ? po + (j - n2) * 8192 : a; pj = n2;
    S.tab = j < n3 + 8 ? a : 0x7ffffff0u; S.srd = mksrd(ws, (unsigned)WS_END); S.lane16 = lane16;
    S.dseq = 5; S.dslot = 65536 + wid * 8192; S.rslot = 65536;
  }
  st_prep(S, S.y);
  wait_bar<22, false>();
  asm volatile("" : "+v"(Y.qa[0]), "+v"(Y.qa[1]));
  float g;
  STAMPIT(3);
  bf16x8 ca[2], q[16];
  { const unsigned xcd = bid & 7, loc = bid >> 3, rb = loc & 7, sel = loc >> 3, head2 = 2 * xcd + (sel & 1), isV = sel >> 1, tb0 = rb * 8 + (wid >> 1) * 4;
    KvOut KO; KO.rs = rs; KO.dst = (isV ? (unsigned)WS_VB : (unsigned)WS_KB) + ((head2 * 64 + tb0) * 16 + (wid & 1) * (isV ? 4 : 8)) * 1024; KO.s2 = isV ? 2048 : 256; KO.s1 = isV ? 1024 : 4096; KO.s0 = isV ? 256 : 2048;
    KO.sv = (lane >> 5) * (isV ? 8192 : 1024) + ((lane >> 4) & 1) * 512 + (lane & 15) * 16; KO.wof = (unsigned)WS_WOF + (bid * 4 + wid) * 4096;
    { KO.gtab = wf_src(((bid * 4 + wid) & 63) * 4 + (lane & 3), bid >> 4);
      KO.gv0 = 4 * (lane >> 5) * 896 + 2 * (lane & 31); KO.woa = wo_a; KO.wob = wo_b; }
    const unsigned cq = (256 * head + (lane & 1) * 255) / 192, rq_ = ((lane & 2) ? qlong : qb) >> 1;
    const unsigned need = lane < 16 ? (head >> 1) + 8 * ((lane & 7) + 8 * ((head & 1) + 2 * (lane >> 3))) : lane < 20 ? (cq & 7) + 8 * (rq_ + 8 * (cq >> 3)) : 16 * head + ((lane - 20) & 15);
    a_phase(ca, q, (unsigned)WS_QB + (head * 64 + (produce ? qlong * 4 + wid : tb)) * 16384, acc4, Y, KO, S, g, ws + WS_G + (tb * 32 + (lane & 31)) * 4, lane,
            ws + WS_F2 + bid * 4, ws + WS_F2 + need * 4, token, Y.pc, tid, wid TR_PASS); }
  STAMPIT(4); STAMPRT(12);
  LABEL(b);
  char* fh = ws + WS_FH + pw * 64; char* ml = ws + WS_ML + pw * 512 + lane * 8;
  f32x16 o[8]; float mref, lsum, mb, lb; bool late = false;
  for (int pass = produce ? 0 : 1; pass < 2; ++pass) {
    b_segment(o, q, mref, lsum, S, pass ? qb : qlong, pass ? 0 : TA, pass ? nt2 : TA + nt1, lane, wid, fh, ml, token, consume, mb, lb, late, pj TR_PASS);
    if (pass == 0) {
#pragma unroll
      for (int j = 0; j < 16; ++j) q[j] = ld16(rs, lane16, (unsigned)WS_QB + ((head * 64 + tb) * 16 + j) * 1024);
      const float inv = lsum > 0.f ? 1.f / lsum : 0.f;
#pragma unroll
      for (int c = 0; c < 16; ++c) { if (!(c & 1)) PINA(o[c >> 1]); st16(rs, lane16, po + c * 1024, pack8(o[c >> 1], c & 1, inv)); SBAR(); }
      __hip_atomic_store((GAS unsigned long long*)ml, ((unsigned long long)__float_as_uint(lsum) << 32) | __float_as_uint(mref), __ATOMIC_RELAXED, __HIP_MEMORY_SCOPE_AGENT);
      DRAIN();
      flag_set(fh, token);
    }
  }
  STAMPIT(5); STAMPRT(13);
  LABEL(c);
  const unsigned og = (unsigned)WS_OP + (head * 64 + tb) * 28 * 1024;
  u32x4 cb[16]; f32x16 c0, z; bf16x8 fr[6]; unsigned ra, rb_, res;
  float chk; unsigned f3v; const char* f3p = ws + WS_F3 + (tb * 16 + (lane & 15)) * 4;
  if (consume) {
    if (late) {
      flag_wait(fh, token);
      const unsigned long long w = __hip_atomic_load((GAS unsigned long long*)ml, __ATOMIC_RELAXED, __HIP_MEMORY_SCOPE_AGENT);
      mb = __uint_as_float((unsigned)w); lb = __uint_as_float((unsigned)(w >> 32));
      Desc D; D.so = po; D.m0 = S.rslot + wid * 8192;
      sfor<16>([&](auto Q) __attribute__((always_inline)) { constexpr int qq = decltype(Q)::v;
        if constexpr (qq == 8) { D.so = po + 8192; D.m0 = nslot(S.rslot) + wid * 8192; }
        st_pre<(qq & 7)>(S, D); if constexpr (!(qq & 3)) NOP0(); st_go<(qq & 7)>(S); });
      DRAIN();
    }
    const float m = lb > 0.f ? fmaxf(mref, mb) : mref, ea = __builtin_amdgcn_exp2f(mref - m), wb = lb > 0.f ? lb * __builtin_amdgcn_exp2f(mb - m) : lb * 0.f;
    const float den = lsum * ea + wb, inv = (1.f - g) / den;
    chk = den; op_head<true>(o, ca, cb, c0, z, fr, ea * inv, wb * inv, S, ra, rb_, res, wid);
  } else { chk = lsum; op_head<false>(o, ca, cb, c0, z, fr, (1.f - g) / lsum, 0.f, S, ra, rb_, res, wid); }
  chk += op_body(ca, cb, c0, z, fr, S, ra, rb_, res, rs, og, wid, f3v, f3p TR_PASS);
  STAMPIT(6); STAMPRT(14);
  DRAIN(); asm volatile("" : "+v"(f3v));
  if (__builtin_expect(__any(!(chk * 0.f == 0.f)), 0)) { safe_redo(rs, og, head, tb, g, lane); DRAIN(); }
  flag_set(ws + WS_F3 + (tb * 16 + head) * 4, token);
  STAMPIT(7);
  if (!__all(f3v == token || (lane & 15) == head)) flag_wait(f3p, token);
  STAMPIT(8);
  {
    u32x4 v[2][16]; unsigned elo = 0x00003f80u, ehi = 0x3f800000u; PIN(elo); PIN(ehi);
    const unsigned c0 = head * 112 + lane, c1 = lane < 48 ? c0 + 64 : c0;
#pragma unroll
    for (int h = 0; h < 16; ++h) { v[0][h] = __builtin_bit_cast(u32x4, ld16(rs, c0 * 16, (unsigned)WS_OP + (h * 64 + tb) * 28 * 1024)); v[1][h] = __builtin_bit_cast(u32x4, ld16(rs, c1 * 16, (unsigned)WS_OP + (h * 64 + tb) * 28 * 1024)); }
    SBAR();
#pragma unroll
    for (int k = 0; k < 2; ++k) { float a[8] = {};
#pragma unroll
      for (int h = 0; h < 16; ++h)
#pragma unroll
        for (int e = 0; e < 4; ++e) { a[2 * e] = dot2(v[k][h][e], elo, a[2 * e]); a[2 * e + 1] = dot2(v[k][h][e], ehi, a[2 * e + 1]); }
      const unsigned c = k ? c1 : c0;
      char* op = out + (size_t)(tb * 32 + (c & 31)) * 896 + ((c >> 6) * 16 + 4 * ((c >> 5) & 1)) * 2;
      if (k == 0 || lane < 48) { *(u32x2*)op = (u32x2){cvtpk(a[0], a[1]), cvtpk(a[2], a[3])}; *(u32x2*)(op + 16) = (u32x2){cvtpk(a[4], a[5]), cvtpk(a[6], a[7])}; } }
  }
  LABEL(end);
  STAMPIT(9); STAMPRT(10); STAMP_OUT(1, 15); TRC(); TR_OUT();
}

extern "C" void kernel_launch(void* const* d_in, const int* in_sizes, int n_in, void* d_out, int out_size, void* d_ws, size_t ws_size, hipStream_t stream) {
  (void)in_sizes; (void)n_in; (void)out_size; (void)ws_size;
  (void)hipFuncSetAttribute((const void*)fused_fwd, hipFuncAttributeMaxDynamicSharedMemorySize, 163840);
  fused_fwd<<<256, 256, 163840, stream>>>((const char*)d_in[0], (const char*)d_in[1], (const char*)d_in[4], (const char*)d_in[5], (const char*)d_in[6], (const char*)d_in[7], (const char*)d_in[9],
                                          (const char*)d_in[2], (const char*)d_in[8], (char*)d_ws, (char*)d_out);
}
```

```cpp
#include <hip/hip_runtime.h>
#include <stdint.h>
#include <utility>

typedef short bf16x8 __attribute__((ext_vector_type(8)));
typedef short s16x4 __attribute__((ext_vector_type(4)));
typedef float f32x16 __attribute__((ext_vector_type(16)));
typedef float f32x4 __attribute__((ext_vector_type(4)));
typedef float f32x2 __attribute__((ext_vector_type(2)));
typedef __bf16 bf16x2_t __attribute__((ext_vector_type(2)));
typedef unsigned u32x4 __attribute__((ext_vector_type(4)));
typedef unsigned u32x2 __attribute__((ext_vector_type(2)));
#define LAS __attribute__((address_space(3)))
#define GAS __attribute__((address_space(1)))
#define DI __device__ __forceinline__
#define SBAR() __builtin_amdgcn_sched_barrier(0)
#define MFMA(a, b, c) __builtin_amdgcn_mfma_f32_32x32x16_bf16((a), (b), (c), 0, 0, 0)
#define PIN(x) asm volatile("" : "+v"(x))

constexpr size_t MB = 1u << 20;
constexpr size_t WS_QB = 0, WS_KB = 16 * MB, WS_VB = 32 * MB, WS_QA = 48 * MB, WS_KVA = 50 * MB, WS_G = 54 * MB, WS_W0 = 54 * MB + 65536, WS_F1 = WS_W0 + 4096, WS_F2 = WS_W0 + 65536, WS_F3 = WS_W0 + 131072, WS_FH = WS_W0 + 32768,
                 WS_ML = 55 * MB, WS_PO = 56 * MB, WS_OP = 64 * MB, WS_WOF = 92 * MB, WS_END = 96 * MB;
#define TA_ 18
constexpr int TA = TA_;
#define F2S 128
#define SLP 8
#define SLP3 2
#define F3S 128
constexpr float LOG2E = 1.4426950408889634f;
constexpr float QSCALE_B = 0.0625f * LOG2E;
constexpr float QSCALE_A = 0.18898223650461362f * LOG2E;

template <int I> struct IC { static constexpr int v = I; };
template <class F, int... Is> DI void sfor_(F&& f, std::integer_sequence<int, Is...>) { (f(IC<Is>{}), ...); }
template <int N, class F> DI void sfor(F&& f) { sfor_(f, std::make_integer_sequence<int, N>{}); }
DI void mfma_vc(f32x16& d, const bf16x8& a, const bf16x8& b, const f32x16& c) { asm volatile("v_mfma_f32_32x32x16_bf16 %0, %1, %2, %3" : "=&v"(d) : "v"(a), "v"(b), "v"(c)); }
DI void mfma_v(f32x16& d, const bf16x8& a, const bf16x8& b) { asm volatile("v_mfma_f32_32x32x16_bf16 %0, %1, %2, %0" : "+v"(d) : "v"(a), "v"(b)); }
DI void mfma_vn(f32x16& d, const bf16x8& a, const bf16x8& b) { asm volatile("s_nop 1\n\tv_mfma_f32_32x32x16_bf16 %0, %1, %2, %0" : "+v"(d) : "v"(a), "v"(b)); }
DI void mfma_a(f32x16& d, const bf16x8& a, const bf16x8& b) { asm volatile("v_mfma_f32_32x32x16_bf16 %0, %1, %2, %0" : "+a"(d) : "v"(a), "v"(b)); }
#define MFMA_DRAIN_V(x) asm volatile("s_nop 7\n\ts_nop 7" : "+v"(x))
#define MFMA_DRAIN_O(o) asm volatile("s_nop 7\n\ts_nop 7" : "+a"(o[0]), "+a"(o[1]), "+a"(o[2]), "+a"(o[3]), "+a"(o[4]), "+a"(o[5]), "+a"(o[6]), "+a"(o[7]))
DI float ard(float a) { float r; asm volatile("v_accvgpr_read_b32 %0, %1" : "=v"(r) : "a"(a)); return r; }
DI float opaque0() { float z; asm volatile("v_mov_b32 %0, 0" : "=v"(z)); return z; }
DI unsigned wf_src(unsigned f, unsigned hd) { const bool isa = f < 32;
  const unsigned nb = isa ? (f < 28 ? f >> 1 : 0) : f < 64 ? (f & 1) * 13 : (f - 48) >> 4, ks = isa ? f & 1 : f < 64 ? (f - 32) >> 1 : f & 15;
  return ((hd * (isa ? 28 : 256) + 16 * ks) * 896 + 64 * nb) | (isa ? 0x40000000u : 0u) | (isa && ks == 1 ? 0x80000000u : 0u); }
DI unsigned rfl(unsigned x) { return __builtin_amdgcn_readfirstlane(x); }
DI unsigned cvtpk(float lo, float hi) { f32x2 v = {lo, hi}; bf16x2_t b = __builtin_convertvector(v, bf16x2_t); return __builtin_bit_cast(unsigned, b); }
DI float dot2(unsigned a, unsigned b, float c) { return __builtin_amdgcn_fdot2_f32_bf16(__builtin_bit_cast(bf16x2_t, a), __builtin_bit_cast(bf16x2_t, b), c, false); }
DI float bflo(unsigned w) { return __uint_as_float(w << 16); }
DI float bfhi(unsigned w) { return __uint_as_float(w & 0xffff0000u); }
DI u32x4 mksrd(const void* p, unsigned bytes) { u32x4 s = {(unsigned)(uintptr_t)p, (unsigned)((uintptr_t)p >> 32) & 0xffffu, bytes, 0x20000u}; return s; }
DI bf16x8 ldsr(unsigned a) { return *(const LAS bf16x8*)(uintptr_t)a; }
DI bf16x8 ldstr(unsigned a, unsigned a2) {
  s16x4 x = __builtin_amdgcn_ds_read_tr16_b64_v4i16((LAS s16x4*)(uintptr_t)a);
  s16x4 y = __builtin_amdgcn_ds_read_tr16_b64_v4i16((LAS s16x4*)(uintptr_t)a2);
  return __builtin_shufflevector(x, y, 0, 1, 2, 3, 4, 5, 6, 7);
}
DI void set_m0(unsigned v) { asm volatile("s_mov_b32 m0, %0" ::"s"(v) : "memory"); }
template <int OFF, int POL = 1> DI void dma16(unsigned voff, u32x4 srd, unsigned soff) {
  if constexpr (POL == 1) asm volatile("buffer_load_dwordx4 %0, %1, %2 offen offset:%3 sc1 lds" ::"v"(voff), "s"(srd), "s"(soff), "i"(OFF) : "memory");
  else if constexpr (POL == 2) asm volatile("buffer_load_dwordx4 %0, %1, %2 offen offset:%3 nt lds" ::"v"(voff), "s"(srd), "s"(soff), "i"(OFF) : "memory");
  else asm volatile("buffer_load_dwordx4 %0, %1, %2 offen offset:%3 lds" ::"v"(voff), "s"(srd), "s"(soff), "i"(OFF) : "memory");
}
DI void touch(const char* p) { asm volatile("global_load_dword a255, %0, off" ::"v"(p) : "memory", "a255"); }
#define LABEL(n) asm volatile(".Lgm_" #n ":" ::: "memory")
#define LOFF(n) ({ unsigned o_; asm volatile("s_mov_b32 %0, .Lgm_" #n "-.Lgm_base" : "=s"(o_)); o_; })
DI const char* code_line(const char* pc, unsigned from, unsigned to, unsigned line) { const unsigned o = from + line * 64; return pc + (o < to ? o : from); }
#define PINS(x) asm volatile("" : "+s"(x))
#define PINA(x) asm volatile("" : "+a"(x))
#define NOP0() asm volatile("s_nop 0" ::: "memory")
template <int V, bool L0> DI void wait_bar() {
  asm volatile("" ::: "memory");
  __builtin_amdgcn_s_waitcnt((V & 15) | ((V >> 4) << 14) | (7 << 4) | (L0 ? 0 : (15 << 8)));
  __builtin_amdgcn_s_barrier();
  asm volatile("" ::: "memory");
  SBAR();
}
template <int N> DI void wait_lgkm() { __builtin_amdgcn_s_waitcnt(15 | (3 << 14) | (7 << 4) | (N << 8)); }
DI bf16x8 pack8(const f32x16& a, int s, float sc) {
  u32x4 w = {cvtpk(a[8 * s] * sc, a[8 * s + 1] * sc), cvtpk(a[8 * s + 2] * sc, a[8 * s + 3] * sc), cvtpk(a[8 * s + 4] * sc, a[8 * s + 5] * sc), cvtpk(a[8 * s + 6] * sc, a[8 * s + 7] * sc)};
  return __builtin_bit_cast(bf16x8, w);
}
DI bf16x8 pack8(const f32x16& a, int s) {
  u32x4 w = {cvtpk(a[8 * s], a[8 * s + 1]), cvtpk(a[8 * s + 2], a[8 * s + 3]), cvtpk(a[8 * s + 4], a[8 * s + 5]), cvtpk(a[8 * s + 6], a[8 * s + 7])};
  return __builtin_bit_cast(bf16x8, w);
}
DI f32x2 halves(float v) {
  auto r = __builtin_amdgcn_permlane32_swap(__float_as_uint(v), __float_as_uint(v), false, false);
  return (f32x2){__uint_as_float(r[0]), __uint_as_float(r[1])};
}
DI float hmax(float v) { const f32x2 h = halves(v); return fmaxf(h[0], h[1]); }
DI float hsum(float v) { const f32x2 h = halves(v); return h[0] + h[1]; }

#define STAMP_DECL
#define STAMPIT(i)
#define STAMPRT(i)
#define STAMP_OUT(k, n)
#define TR_DECL
#define TR_ARG
#define TR_PASS
#define TRC()
#define TR_OUT()
__host__ __device__ constexpr int wf_b(int nb, int ks) { return nb == 0 ? 32 + 2 * ks : nb == 13 ? 33 + 2 * ks : 48 + 16 * nb + ks; }
constexpr unsigned XRING = 65536;

#define SPOL 1
struct Prov { u32x4 srd; unsigned voff[2], soff, pstr, kstr; };
DI void prov_next(Prov& p) { p.soff += p.kstr; }
struct K1State { unsigned xs, ws, xd; };
struct K1Regs { unsigned xl, xr0, xr1, mw, mx; };

template <int Q, int POL = 1> DI void k1_dma(Prov& p, unsigned m0b) { set_m0(m0b + Q * 4096); unsigned so = p.soff + Q * p.pstr; PINS(so); NOP0(); dma16<0, POL>(p.voff[Q & 1], p.srd, so); }

#define ST_AUX 16
DI void st16(__amdgpu_buffer_rsrc_t rs, unsigned voff, unsigned soff, bf16x8 v) {
  __builtin_amdgcn_raw_buffer_store_b128(__builtin_bit_cast(u32x4, v), rs, voff, soff, ST_AUX);
  asm volatile("s_nop 1" ::"v"(v) : "memory");
}

struct Sync { char* ws; unsigned token, lane; u32x4 fv;
  __amdgpu_buffer_rsrc_t rs; bf16x8 sq[24]; unsigned sqe, sql, sv; bool stored; bf16x8 qa[2]; unsigned qao, tso, tm0;
  const char* pc; unsigned pfd[2], pfl;
  unsigned tab, need, gtab, ksv, cla, clb;
};
DI void flag_set(char* p, unsigned token) { __hip_atomic_store((GAS unsigned*)p, token, __ATOMIC_RELAXED, __HIP_MEMORY_SCOPE_AGENT); }
DI unsigned flag_get(const char* p) { return __hip_atomic_load((GAS unsigned*)p, __ATOMIC_RELAXED, __HIP_MEMORY_SCOPE_AGENT); }
constexpr unsigned SPIN_CAP = 1u << 20;
template <int SL = SLP> DI void flag_wait(const char* p, unsigned token) {
#pragma unroll 1
  for (unsigned i = 0; i < SPIN_CAP; ++i) { if (__all(flag_get(p) == token)) break; __builtin_amdgcn_s_sleep(SL); }
}
#define DRAIN() do { asm volatile("s_waitcnt vmcnt(0)" ::: "memory"); __builtin_amdgcn_s_waitcnt(0x0f70); } while (0)

struct Role { unsigned head, pi, roleb, pw, po, tb; int qlong, nt1, qb, nt2; bool produce, consume; };
DI Role mk_role(unsigned bid, unsigned wid) { Role r; r.head = 2 * (bid & 7) + ((bid >> 3) & 1); r.pi = bid >> 5; r.roleb = (bid >> 4) & 1;
  r.qlong = 15 - (int)r.pi; r.nt1 = 32 - 2 * (int)r.pi - TA;
  r.qb = r.roleb ? (int)r.pi : r.qlong; r.nt2 = r.roleb ? 2 * (int)r.pi + 2 : TA;
  r.produce = r.roleb && r.nt1 > 0; r.consume = !r.roleb && r.nt1 > 0;
  r.pw = (r.head * 8 + r.pi) * 4 + wid; r.po = (unsigned)WS_PO + r.pw * 16384; r.tb = r.qb * 4 + wid; return r; }
DI unsigned sel(bool c, unsigned a, unsigned b) { return b ^ ((a ^ b) & (0u - (unsigned)c)); }
DI void mk_tables(Sync& Y, const unsigned bid, const unsigned wid, const unsigned lane) {
  const Role r = mk_role(bid, wid); const unsigned head = r.head, j = lane;
  const unsigned n1 = 8 + (r.roleb ? 2 * r.nt1 : 0), n2 = n1 + 2 * r.nt2, n3 = n2 + (r.consume ? 2 : 0);
  const unsigned jb = sel(j < n1, j - 8 + 2 * TA, j - n1), kv = (unsigned)WS_KB + head * (unsigned)MB + (jb & 1) * (unsigned)(WS_VB - WS_KB) + (jb >> 1) * 32768;
  unsigned a = sel(j >= 8, kv, (unsigned)WS_KVA + head * 262144 + j * 32768);
  a = sel(j >= n3, (unsigned)WS_WOF + head * 262144 + (j - n3) * 32768, a) + wid * 8192;
  a = sel(j >= n2 && j < n3, r.po + (j - n2) * 8192, a);
  Y.tab = sel(j < n3 + 8, a, 0x7ffffff0u); PIN(Y.tab);
  const unsigned cq = (256 * head + (lane & 1) * 255) / 192, rq_ = sel(lane & 2, r.qlong, r.qb) >> 1;
  Y.need = (unsigned)WS_F2 + F2S * sel(lane < 16, (head >> 1) + 8 * ((lane & 7) + 8 * ((head & 1) + 2 * (lane >> 3))), sel(lane < 20, (cq & 7) + 8 * (rq_ + 8 * (cq >> 3)), 16 * head + ((lane - 20) & 15))); PIN(Y.need);
  Y.gtab = wf_src(((bid * 4 + wid) & 63) * 4 + (lane & 3), bid >> 4); PIN(Y.gtab);
  Y.ksv = (lane >> 5) * ((bid >> 7) ? 8192 : 1024) + ((lane >> 4) & 1) * 512 + (lane & 15) * 16; PIN(Y.ksv);
}
constexpr int g_go(int N2, int kh, int i) {
  if (N2 == 8) { const int m = i >> 3, n = i & 7; return n == 7 ? (kh ? ((m & 1) ? -1 : m >> 1) : (m < 4 ? m : 2 * m - 3)) : (n == 3 && !kh && m >= 4) ? 2 * m - 4 : -1; }
  return (i & 3) != 3 ? -1 : kh ? (((i & 7) == 7 && i < 32) ? i >> 3 : -1) : i >> 2; }
constexpr int g_tr(int N2, int i) {
  if (N2 == 8) return ((i >> 3) < 4 && (i & 7) >= 1 && (i & 7) <= 4) ? 4 * (i >> 3) + (i & 7) - 1 : -1;
  if ((i & 3) > 1 || i % 6 == 0) return -1;
  int c = 0; for (int j = 0; j < i; ++j) c += (j & 3) <= 1 && j % 6 != 0; return c < 12 ? c : -1; }
DI unsigned xnext(unsigned s) { return s == 65536u ? 0u : s + 32768u; }
template <bool SWAP, int NTL, bool FIRST, int QT>
DI void k1_ktile(f32x4 (&acc)[8 * NTL][2], bf16x8 (&xa)[4], s16x4 (&wf)[2][4 * NTL], K1Regs& R, unsigned (&wr)[2 * NTL], K1State& st, Prov& xp, Prov& wp,
                 const Prov& xn, const Prov& wn, const int t, const int NKT, const unsigned wid, Sync& Y) {
  constexpr int N2 = 2 * NTL; constexpr bool BOOT = FIRST && NTL == 3;
  sfor<2>([&](auto KH) __attribute__((always_inline)) { constexpr int kh = decltype(KH)::v;
    constexpr int VW = BOOT ? 14 : QT == 12 ? (kh ? 19 : 16) : QT > 12 ? 22 : 16;
    if constexpr (kh == 0 && QT == 14) {
      if (wid == 0) { asm volatile("s_waitcnt vmcnt(44)" : "+v"(Y.fv));
        if (!__all(Y.fv[0] == Y.token && Y.fv[1] == Y.token && Y.fv[2] == Y.token && Y.fv[3] == Y.token))
#pragma unroll 1
          for (int i = 0; i < 4; ++i) flag_wait(Y.ws + WS_F1 + Y.lane * 16 + i * 4, Y.token); } }
    if constexpr (QT == 0) { if (!Y.stored) asm volatile("s_waitcnt vmcnt(16)" ::: "memory"); wait_bar<40, false>(); }
    else if constexpr (!(BOOT && kh == 0)) wait_bar<VW, false>();
    const unsigned mw = R.mw + kh * 16384, mx = R.mx;
    const unsigned bw2 = R.mw ^ 32768u;
    unsigned so = 0, qso = 0;
    sfor<8 * N2>([&](auto I) __attribute__((always_inline)) { constexpr int i = decltype(I)::v, m = i / N2, n = i % N2, pr = SWAP ? NTL * m + (n >> 1) : N2 * (m >> 1) + n, hf = SWAP ? n & 1 : m & 1;
      const bf16x8 wv = __builtin_shufflevector(wf[kh][2 * n], wf[kh][2 * n + 1], 0, 1, 2, 3, 4, 5, 6, 7);
      constexpr int go = BOOT ? -1 : g_go(N2, kh, i);
#define K1_OPS "v"(SWAP ? wv : xa[m & 3]), "v"(SWAP ? xa[m & 3] : wv)
      if constexpr (go >= 0) {
        const Prov& pp = go < 4 ? wp : xp;
        if constexpr (FIRST && kh == 0) asm volatile("v_mfma_f32_16x16x32_bf16 %0, %1, %2, 0\n\tbuffer_load_dwordx4 %3, %4, %5 offen lds" : "=a"(acc[pr][hf]) : K1_OPS, "v"(pp.voff[go & 1]), "s"(pp.srd), "s"(so) : "memory");
        else if constexpr (QT >= 14) asm volatile("v_mfma_f32_16x16x32_bf16 %0, %1, %2, %0\n\tbuffer_load_dwordx4 %3, %4, %5 offen sc1 lds" : "+a"(acc[pr][hf]) : K1_OPS, "v"(pp.voff[go & 1]), "s"(pp.srd), "s"(so) : "memory");
        else asm volatile("v_mfma_f32_16x16x32_bf16 %0, %1, %2, %0\n\tbuffer_load_dwordx4 %3, %4, %5 offen lds" : "+a"(acc[pr][hf]) : K1_OPS, "v"(pp.voff[go & 1]), "s"(pp.srd), "s"(so) : "memory");
      } else if constexpr (FIRST && kh == 0) asm volatile("v_mfma_f32_16x16x32_bf16 %0, %1, %2, 0" : "=a"(acc[pr][hf]) : K1_OPS);
      else asm volatile("v_mfma_f32_16x16x32_bf16 %0, %1, %2, %0" : "+a"(acc[pr][hf]) : K1_OPS);
#undef K1_OPS
      SBAR();
      if constexpr (n == 0) xa[(m + 2) & 3] = ldsr(m < 6 ? (kh ? R.xr1 : R.xr0) + (m + 2) * 2048 : (kh ? R.xr0 : R.xr1) + (m - 6) * 2048);
      constexpr int tr = g_tr(N2, i);
      if constexpr (tr >= 0) wf[kh ^ 1][tr] = __builtin_amdgcn_ds_read_tr16_b64_v4i16((LAS s16x4*)(uintptr_t)(wr[tr >> 1] + (kh ? 0 : 16384) + (tr & 1) * 2048));
      if constexpr (BOOT) {
        constexpr int p = (i & 3) == 3 ? i >> 2 : -1;
        if constexpr (kh == 0 && p >= 0) { if constexpr (p < 4) k1_dma<p, 0>(wp, bw2 + 16384); else k1_dma<p - 4, 0>(xp, mx); if constexpr (p == 3) prov_next(wp); if constexpr (p == 11) prov_next(xp); }
        if constexpr (kh == 1 && p >= 0 && p < 8) { if constexpr (p < 4) k1_dma<p, 0>(wp, mw - 16384); else k1_dma<p - 4, 0>(wp, mw); if constexpr (p == 3) prov_next(wp); }
      } else {
        constexpr int pm = g_go(N2, kh, i + (N2 == 8 ? 2 : 1)), ps = g_go(N2, kh, i + 1);
        if constexpr (pm >= 0 && i + 1 < 8 * N2) set_m0(pm < 4 ? mw + pm * 4096 : mx + (pm - 4) * 4096);
        if constexpr (ps >= 0 && i + 1 < 8 * N2) { so = ps == 0 ? wp.soff : ps == 4 ? xp.soff : so + (ps < 4 ? wp.pstr : xp.pstr); PINS(so); }
      }
      constexpr int wx = kh ? -1 : N2 == 8 ? ((m >= 4 && (n & 3) == 0) ? 2 * (m - 4) + (n >> 2) : -1) : i == 29 ? 0 : i == 32 ? 1 : i == 33 ? 2 : i == 37 ? 3 : i == 40 ? 4 : i == 41 ? 5 : -1;
      if constexpr (wx >= 0) { wr[wx] ^= 32768u; PIN(wr[wx]); }
      if constexpr (!BOOT && n == 0 && m == 2) { prov_next(wp); PINS(wp.soff); }
      if constexpr (!BOOT && kh == 0 && n == 0 && m == (N2 == 8 ? 5 : 4)) { prov_next(xp); PINS(xp.soff); }
      if constexpr (kh == 1 && m == 4 && n == 1) { st.xs = xnext(st.xs); PINS(st.xs); }
      if constexpr (kh == 1 && m == 4 && n == 3) { R.xr0 = R.xl + st.xs; PIN(R.xr0); }
      if constexpr (kh == 1 && m == 6 && n == 1) { R.xr1 = R.xr0 ^ 64u; PIN(R.xr1); }
      if constexpr (kh == 1 && m == 5 && n == 3) { R.mx = R.mx >= XRING + 65536u ? R.mx - 65536u : R.mx + 32768u; PINS(R.mx); }
      if constexpr (kh == 1 && m == 7 && n == 1) { R.mw ^= 32768u; PINS(R.mw); }
      if constexpr (kh == 1 && m == 7 && n == 2) { if (t == NKT - 3) { wp = wn; xp = xn; } }
      if constexpr (NTL == 4 && QT == -1 && kh == 1 && m == 7 && n == 4) { if (t == 6 && Y.stored) DRAIN(); }
      if constexpr (NTL == 4 && QT == -1 && kh == 1 && m == 5 && n == 5) { if (t == 7 && wid == 0) flag_set(Y.ws + WS_F1 + blockIdx.x * 4, Y.token); }
      if constexpr (n == N2 - (N2 == 8 ? 4 : 2) && (N2 == 6 || m >= 4)) wait_lgkm<N2 == 8 ? 1 : 3>();
      if constexpr (QT >= 12) { constexpr int le = 6 * (QT - 12) + 3 * kh + (m >> 1) - 1;
        if constexpr (m >= 2 && !(m & 1) && n == 0) { qso = __builtin_amdgcn_readlane(Y.sql, le); PINS(qso); }
        if constexpr (m >= 2 && !(m & 1) && n == (m < 4 ? 5 : 4)) st16(Y.rs, Y.sv, qso, Y.sq[le]); }
      if constexpr (QT == 14 && kh == 1 && n == 3 && (m == 5 || m == 7))
        asm volatile("s_nop 4\n\tbuffer_load_dwordx4 %0, %1, %2, %3 offen offset:%4 sc1" : "=v"(Y.qa[m == 7]) : "v"(Y.lane * 16), "s"(xn.srd), "s"(Y.qao), "i"(m == 7 ? 1024 : 0) : "memory");
      SBAR();
    });
    if constexpr (BOOT && kh == 1) prov_next(wp);
  });
}

template <bool SWAP, int NTL>
DI void k1_gemm(f32x4 (&acc)[8 * NTL][2], K1State& st, Prov& xp, Prov& wp, const Prov& xn, const Prov& wn, const int NKT, const unsigned wid, const unsigned lane, Sync& Y) {
  const unsigned c = lane & 15, g = lane >> 4, q = (lane >> 2) & 3, wrow = wid >> 1, wc = wid & 1;
  K1Regs R; unsigned wr[2 * NTL];
  R.xl = XRING + (128 * wrow + c) * 128 + 16 * (g ^ (c >> 1)); R.xr0 = R.xl + st.xs; R.xr1 = R.xr0 ^ 64u; R.mw = st.ws + wid * 1024; R.mx = XRING + st.xd + wid * 1024; PIN(R.xl); PIN(R.xr0); PIN(R.xr1);
#pragma unroll
  for (int n = 0; n < 2 * NTL; ++n) { wr[n] = st.ws + (8 * g + q) * 512 + 32 * ((2 * NTL * wc + n) ^ (q | ((g & 1) << 2))) + 8 * (lane & 3); PIN(wr[n]); }
  bf16x8 xa[4]; s16x4 wf[2][4 * NTL];
  if constexpr (NTL == 3) wait_bar<14, false>();
  xa[0] = ldsr(R.xr0); xa[1] = ldsr(R.xr0 + 2048);
#pragma unroll
  for (int n = 0; n < 4 * NTL; ++n) wf[0][n] = __builtin_amdgcn_ds_read_tr16_b64_v4i16((LAS s16x4*)(uintptr_t)(wr[n >> 1] + (n & 1) * 2048));
  SBAR();
  k1_ktile<SWAP, NTL, true, NTL == 4 ? 0 : -1>(acc, xa, wf, R, wr, st, xp, wp, xn, wn, 0, NKT, wid, Y);
  if constexpr (NTL == 4) {
    for (int t = 1; t < 12; ++t) k1_ktile<SWAP, NTL, false, -1>(acc, xa, wf, R, wr, st, xp, wp, xn, wn, t, NKT, wid, Y);
    if constexpr (SWAP) LABEL(k0); else LABEL(k1);
    asm volatile("global_load_dword %0, %2, off\n\tglobal_load_dword %1, %3, off" : "=&v"(Y.pfd[0]), "=&v"(Y.pfd[1])
                 : "v"(code_line(Y.pc, LOFF(a), LOFF(b), wid * 64 + lane)), "v"(code_line(Y.pc, LOFF(a), LOFF(b), 256 + wid * 64 + lane)) : "memory");
    if (wid == 0) asm volatile("global_load_dwordx4 %0, %1, off sc1" : "=v"(Y.fv) : "v"(Y.ws + WS_F1 + Y.lane * 16) : "memory");
    sfor<4>([&](auto T) __attribute__((always_inline)) { constexpr int t = 12 + decltype(T)::v; k1_ktile<SWAP, NTL, false, t>(acc, xa, wf, R, wr, st, xp, wp, xn, wn, t, 16, wid, Y); });
    asm volatile("" ::"v"(Y.pfd[0]), "v"(Y.pfd[1]), "v"(Y.pfl));
  } else
    for (int t = 1; t < NKT; ++t) k1_ktile<SWAP, NTL, false, -1>(acc, xa, wf, R, wr, st, xp, wp, xn, wn, t, NKT, wid, Y);
  st.ws = R.mw - wid * 1024; st.xd = R.mx - XRING - wid * 1024;
  asm volatile("s_nop 7" ::: "memory"); SBAR();
}

DI Prov mk_xprov(const char* X, unsigned ld, unsigned bytes, unsigned rb, unsigned wid, unsigned lane, bool vperm) {
  Prov p; p.srd = mksrd(X, bytes);
  p.voff[0] = p.voff[1] = (lane >> 3) * ld + 16 * ((lane & 7) ^ (4 * (wid & 1) + (lane >> 4)));
  p.soff = (256 * rb + 8 * (vperm ? ((wid & 1) << 1) | (wid >> 1) : wid)) * ld; p.pstr = 32 * ld; p.kstr = 128; return p;
}
template <class F> DI Prov mk_wprov(const char* W, unsigned ld, unsigned bytes, unsigned wid, unsigned lane, F colf) {
  Prov p; p.srd = mksrd(W, bytes);
#pragma unroll
  for (int o = 0; o < 2; ++o) { const unsigned ch = (lane & 31) ^ ((2 * (wid & 1) + (lane >> 5) + 4 * o) << 1), col = colf(ch);
    p.voff[o] = col < 0x10000u ? (lane >> 5) * ld + 2 * col : 0x7ffffff0u; }
  p.soff = 2 * wid * ld; p.pstr = 8 * ld; p.kstr = 32 * ld; return p;
}
DI u32x4 pack44(const f32x4& a, const f32x4& b, float sc) { return (u32x4){cvtpk(a[0] * sc, a[1] * sc), cvtpk(a[2] * sc, a[3] * sc), cvtpk(b[0] * sc, b[1] * sc), cvtpk(b[2] * sc, b[3] * sc)}; }
DI void proj_phase(f32x4 (&acc4)[32][2], const char* x, const char* wqkv, const char* ckv, const char* wq, const char* wuk, const char* wuv, const char* gate_w,
                   const char* wo_a, const char* wo_b, char* ws, const unsigned kva_off, Sync& Y, const unsigned lane, const unsigned wid TR_ARG) {
  const unsigned wrow = wid >> 1, wc = wid & 1, lane16 = lane * 16;
  const unsigned bid = blockIdx.x, xcd = bid & 7, loc = bid >> 3, rb = loc & 7, sel = loc >> 3;
  const unsigned cb = xcd + 8 * sel;
  const unsigned head2 = 2 * xcd + (sel & 1), isV = sel >> 1;
  const Prov xkv = mk_xprov(ckv, 2048, 2048 * 2048, rb, wid, lane, isV);
  const Prov wkv = mk_wprov(isV ? wuv : wuk, 8192, 1024 * 8192, wid, lane, [&](unsigned ch) { return head2 * 256 + 8 * ch; });
  Prov xp = cb < 31 ? mk_xprov(x, 896, 2048 * 896, rb, wid, lane, cb >= 28) : xkv;
  Prov wp = cb < 22 ? mk_wprov(wq, 8192, 448 * 8192, wid, lane, [&](unsigned ch) { return ch < 24 ? cb * 192 + 8 * ch : 0x10000u; })
          : cb < 31 ? mk_wprov(wqkv, 2688, 448 * 2688, wid, lane, [&](unsigned ch) { const unsigned vc = (cb < 28 ? cb - 22 : cb - 28) * 24 + ch, slot = vc >> 2;
                         return (ch < 24 && slot < (cb < 28 ? 32u : 16u)) ? (cb < 28 ? 0u : 896u) + 28 * slot + 8 * (vc & 3) : 0x10000u; })
          : wkv;
  K1State st = cb < 31 ? K1State{32768u, 32768u, 0u} : K1State{65536u, 0u, 32768u};
  const unsigned x0 = XRING + st.xs + wid * 1024, w0 = st.ws + wid * 1024;
  sfor<8>([&](auto I) __attribute__((always_inline)) { k1_dma<decltype(I)::v, 0>(xp, x0); }); prov_next(xp);
  sfor<4>([&](auto I) __attribute__((always_inline)) { k1_dma<decltype(I)::v, 0>(wp, w0); }); prov_next(wp);
  sfor<4>([&](auto I) __attribute__((always_inline)) { k1_dma<decltype(I)::v, 0>(wp, w0 + 16384); }); prov_next(wp);
  sfor<4>([&](auto I) __attribute__((always_inline)) { k1_dma<decltype(I)::v, 0>(wp, w0 ^ 32768u); }); prov_next(wp);
  sfor<8>([&](auto I) __attribute__((always_inline)) { k1_dma<decltype(I)::v, 0>(xp, XRING + xnext(st.xs) + wid * 1024); }); prov_next(xp);
  touch(code_line(Y.pc, 0, LOFF(end), (rb * 4 + wid) * 64 + lane));
  { const unsigned e = wf_src(((bid * 4 + wid) & 63) * 4 + (lane >> 4), bid >> 4), row = ((int)e < 0 && (lane & 15) >= 12) ? 0 : lane & 15;
    touch(((e & 0x40000000u) ? wo_a : wo_b) + (e & 0x3fffffffu) + row * 896); }
  mk_tables(Y, bid, wid, lane);
  Y.stored = cb >= 22; Y.sv = (lane >> 5) * 1024 + ((lane >> 4) & 1) * 512 + (lane & 15) * 16;
  const unsigned tb0 = rb * 8 + wrow * 4;
  {
    const bool vt = cb >= 28; const unsigned hi = vt ? lane / 6 : lane / 3, lo = lane - hi * (vt ? 6 : 3), tb = tb0 + (vt ? hi : hi >> 1), odd = (vt ? lo : hi) & 1;
    const unsigned c32 = (cb < 22 ? cb : cb < 28 ? cb - 22 : cb - 28) * 6 + wc * 3 + (vt ? lo >> 1 : lo);
    unsigned d = cb < 22 ? (unsigned)WS_QB + (((c32 >> 3) * 64 + tb) * 16 + (c32 & 7) * 2) * 1024
               : vt ? (unsigned)WS_KVA + (c32 * 64 + tb) * 4096 + 2048
               : c32 < 16 ? (unsigned)WS_QA + (c32 * 64 + tb) * 2048 : (unsigned)WS_KVA + ((c32 - 16) * 64 + tb) * 4096;
    d += odd * 256;
    if (cb == 31 || c32 >= (cb < 22 ? 128u : cb < 28 ? 32u : 16u)) d = 0x7ffffff0u;
    Y.sqe = cb >= 22 ? d : 0x7ffffff0u; Y.sql = cb < 22 ? d : 0x7ffffff0u;
  }
  if (cb < 31) {
    f32x4 acc[24][2];
    if (cb < 28) {
      k1_gemm<true, 3>(acc, st, xp, wp, xkv, wkv, 7, wid, lane, Y);
      TRC();
      const bool pad = cb >= 22 && lane >= 48;
#pragma unroll
      for (int e = 0; e < 24; ++e) { const float sc = cb < 22 ? QSCALE_B : (cb - 22) * 6 + wc * 3 + e % 3 < 16 ? QSCALE_A : 1.f;
        PINA(acc[e][0]); PINA(acc[e][1]); u32x4 w = pack44(acc[e][0], acc[e][1], sc); if (pad) { w[2] = 0u; w[3] = 0u; }
        Y.sq[e] = __builtin_bit_cast(bf16x8, w); PIN(Y.sq[e]); if (e & 1) SBAR(); }
    } else {
      k1_gemm<false, 3>(acc, st, xp, wp, xkv, wkv, 7, wid, lane, Y);
      TRC();
      const unsigned fill = (lane & 15) == 12 ? 0x3f803f80u : 0u;
#pragma unroll
      for (int e = 0; e < 24; ++e) { PINA(acc[e][0]); PINA(acc[e][1]); u32x4 w = pack44(acc[e][0], acc[e][1], 1.f); if ((e & 1) && (lane & 15) >= 12) w = (u32x4){fill, fill, fill, fill};
        Y.sq[e] = __builtin_bit_cast(bf16x8, w); PIN(Y.sq[e]); if (e & 1) SBAR(); }
    }
  } else {
    sfor<4>([&](auto I) __attribute__((always_inline)) { k1_dma<decltype(I)::v, 0>(wp, (w0 ^ 32768u) + 16384); }); prov_next(wp);
    u32x4 gw_[7];
#pragma unroll
    for (int i = 0; i < 7; ++i) gw_[i] = *(const u32x4*)(gate_w + (8 * i + (lane & 7)) * 16);
#pragma unroll 2
    for (int p = 0; p < 8; ++p) { const unsigned rw = rb * 256 + wid * 64 + p * 8 + (lane >> 3); float s = 0.f;
#pragma unroll
      for (int i = 0; i < 7; ++i) { const u32x4 a = *(const u32x4*)(x + rw * 896 + (8 * i + (lane & 7)) * 16);
#pragma unroll
        for (int e = 0; e < 4; ++e) s = dot2(a[e], gw_[i][e], s); }
      s += __shfl_xor(s, 1); s += __shfl_xor(s, 2); s += __shfl_xor(s, 4);
      flag_set(ws + WS_G + rw * 4, __float_as_uint(1.f / (1.f + __expf(-s)))); }
#pragma unroll
    for (int e = 0; e < 24; ++e) { Y.sq[e] = bf16x8{}; PIN(Y.sq[e]); }
    wait_bar<0, false>();
    TRC();
  }
  if (Y.stored) sfor<24>([&](auto E) __attribute__((always_inline)) { constexpr int e = decltype(E)::v;
    unsigned so = __builtin_amdgcn_readlane(Y.sqe, e); asm volatile("s_nop 4" : "+s"(so)); st16(Y.rs, Y.sv, so, Y.sq[e]); });
  asm volatile("" : "+v"(Y.token)); Y.token = rfl(Y.token) + 1u;
  TRC();
  Prov xa_, wa_; xa_.srd = mksrd(ws, (unsigned)WS_END); xa_.voff[0] = xa_.voff[1] = lane16; xa_.soff = kva_off + wid * 1024; xa_.pstr = 4096; xa_.kstr = 32768;
  wa_ = xa_; wa_.soff += 3 * 32768; wa_.kstr = 16384;
  Y.pfl = 0u; Y.cla = LOFF(k0) + (wid * 64 + lane) * 64 % 12288; Y.clb = LOFF(k1) + (wid * 64 + lane) * 64 % 12288;
  if (!isV) k1_gemm<true, 4>(acc4, st, xp, wp, xa_, wa_, 16, wid, lane, Y);
  else k1_gemm<false, 4>(acc4, st, xp, wp, xa_, wa_, 16, wid, lane, Y);
  Y.tso = xp.soff; Y.tm0 = XRING + st.xd + wid * 1024;
}

struct Desc { unsigned so, m0; };
struct Stream {
  u32x4 srd; unsigned tab, lane16;
  unsigned dseq, dslot, rslot, so;
  Desc y;
};
DI unsigned nslot(unsigned s) { return s >= 131072u ? s - 131072u : s + 32768u; }
DI void st_prep(Stream& S, Desc& D) { D.so = __builtin_amdgcn_readlane(S.tab, S.dseq); PINS(D.so); D.m0 = S.dslot; S.dslot = nslot(S.dslot); S.dseq += 1; PINS(S.dslot); }
template <int Q> DI void st_pre(Stream& S, const Desc& D) {
  if constexpr (Q == 0) { set_m0(D.m0); S.so = D.so; } else if constexpr (Q == 4) { set_m0(D.m0 + 4096u); S.so = D.so + 4096u; PINS(S.so); }
}
template <int Q> DI void st_go(Stream& S) { dma16<(Q & 3) * 1024, SPOL>(S.lane16, S.srd, S.so); }
DI void st_item(Stream& S) {
  sfor<8>([&](auto Q) __attribute__((always_inline)) { st_pre<decltype(Q)::v>(S, S.y); if constexpr (!(decltype(Q)::v & 3)) NOP0(); st_go<decltype(Q)::v>(S); });
  st_prep(S, S.y); asm volatile("s_nop 4" ::: "memory");
}
DI float maskv(float v, int dq, int rr) { return dq >= ((rr & 3) + 8 * (rr >> 2)) ? v : -__builtin_inff(); }
DI float max16(const f32x16& s) {
  float a = fmaxf(fmaxf(s[0], s[1]), s[2]), b = fmaxf(fmaxf(s[3], s[4]), s[5]);
  a = fmaxf(fmaxf(a, s[6]), s[7]); b = fmaxf(fmaxf(b, s[8]), s[9]); a = fmaxf(fmaxf(a, s[10]), s[11]); b = fmaxf(fmaxf(b, s[12]), s[13]);
  a = fmaxf(fmaxf(a, s[14]), s[15]); a = fmaxf(a, b);
  return hmax(a);
}
DI float max3f(float a, float b, float c) { float r; asm("v_max3_f32 %0, %1, %2, %3" : "=v"(r) : "v"(a), "v"(b), "v"(c)); return r; }
DI f32x16 splat16(float v) { f32x16 r; for (int i = 0; i < 16; ++i) r[i] = v; return r; }
DI void open_item(Stream& S, unsigned& rb_, unsigned off) { rb_ = S.rslot + off; PIN(rb_); S.rslot = nslot(S.rslot); PINS(S.rslot); }
DI void unopen_item(Stream& S) { S.rslot = S.rslot == 0u ? 131072u : S.rslot - 32768u; }
DI void set_c(f32x16& c0, f32x16& c1, float v, int dq) {
#pragma unroll
  for (int e = 0; e < 16; ++e) { c0[e] = maskv(v, dq, e); c1[e] = maskv(v, dq - 32, e); }
  PIN(c0); PIN(c1); asm volatile("s_nop 1" ::: "memory");
}

template <bool FIRST>
DI void b_tile(f32x16 (&o)[8], const bf16x8 (&q)[16], bf16x8 (&fr)[8], f32x16& c0, f32x16& c1, float& mref, float& rs0, float& rs1, Stream& S, unsigned& rb_) {
  f32x16 s0, s1; u32x4 p[4]; float pe[32], ma, mb_; Desc X;
  constexpr int E0 = FIRST ? 22 : 18;
  sfor<64>([&](auto J) __attribute__((always_inline)) { constexpr int j = decltype(J)::v;
    if constexpr (j == 24) { wait_bar<16, false>(); open_item(S, rb_, S.lane16); }
    if constexpr (j == 56) { wait_bar<11, false>(); open_item(S, rb_, S.lane16); }
    if constexpr ((j & 3) == 0) wait_lgkm<4>();
    if constexpr (j == 0) mfma_vc(s0, fr[0], q[0], c0);
    else if constexpr (j < 16) mfma_v(s0, fr[j & 7], q[j]);
    else if constexpr (j == 16) mfma_vc(s1, fr[0], q[0], c1);
    else if constexpr (j < 32) mfma_v(s1, fr[j & 7], q[j - 16]);
    else if constexpr (FIRST && j < 40) asm volatile("v_mfma_f32_32x32x16_bf16 %0, %1, %2, 0" : "=&a"(o[j & 7]) : "v"(fr[j & 7]), "v"(p[0]));
    else mfma_a(o[j & 7], fr[j & 7], __builtin_bit_cast(bf16x8, p[(j - 32) >> 3]));
    fr[j & 7] = ldsr(rb_ + ((j + 8) & 31) * 1024);
    if constexpr (j < 16 && (j & 1)) st_go<(j >> 1)>(S);
    if constexpr (j == 51 || j == 53 || j == 55 || j == 57 || j == 59 || j == 61) st_go<((j - 51) >> 1)>(S);
    if constexpr (j == 62) st_go<6>(S);
    if constexpr (j == 63) st_go<7>(S);
    SBAR();
    if constexpr (FIRST) {
      if constexpr (j == 18) { ma = max3f(s0[0], s0[1], s0[2]); mb_ = max3f(s0[3], s0[4], s0[5]); ma = max3f(ma, s0[6], s0[7]); mb_ = max3f(mb_, s0[8], s0[9]); PIN(ma); PIN(mb_); }
      if constexpr (j == 19) { ma = max3f(ma, s0[10], s0[11]); mb_ = max3f(mb_, s0[12], s0[13]); ma = max3f(ma, s0[14], s0[15]); ma = fmaxf(ma, mb_); PIN(ma); }
      if constexpr (j == 20) { ma = hmax(ma); mref = ma == -__builtin_inff() ? 0.f : ma; PIN(mref); }
      if constexpr (j >= 56) { constexpr int e = 2 * (j - 56); float v = -mref; c0[e] = v; c0[e + 1] = v; c1[e] = v; c1[e + 1] = v; if constexpr (j == 63) { PIN(c0); PIN(c1); } }
    }
    if constexpr (j >= E0 && j < E0 + 32) { constexpr int e = j - E0;
      float v = e < 16 ? s0[e & 15] : s1[e & 15]; if constexpr (FIRST) v -= mref; v = __builtin_amdgcn_exp2f(v); PIN(v); pe[e] = v; }
    if constexpr (j >= E0 + 1 && j < E0 + 33) { constexpr int e = j - E0 - 1;
      if constexpr (e & 1) { rs1 += pe[e]; PIN(rs1); unsigned w = cvtpk(pe[e - 1], pe[e]); PIN(w); p[e >> 3][(e >> 1) & 3] = w; }
      else { rs0 += pe[e]; PIN(rs0); } }
    if constexpr (j == 0) st_pre<0>(S, S.y);
    if constexpr (j == 8) st_pre<4>(S, S.y);
    if constexpr (j == 16) st_prep(S, X);
    if constexpr (j == 17) st_prep(S, S.y);
    if constexpr (j == 50) st_pre<0>(S, X);
    if constexpr (j == 58) st_pre<4>(S, X);
    SBAR();
  });
}

DI void hand_step(const char* fh, const char* ml, const unsigned token, const int left, bool& late) {
  if (left == 2) {
    unsigned fw; asm volatile("v_accvgpr_read_b32 %0, a254" : "=v"(fw));
    late = rfl(fw) != token;
    asm volatile("global_load_dwordx2 a[252:253], %0, off sc1" ::"v"(ml) : "memory", "a252", "a253");
  }
  if (left == 3) asm volatile("global_load_dword a254, %0, off sc1" ::"v"(fh) : "memory", "a254");
}

DI void b_segment(f32x16 (&o)[8], const bf16x8 (&q)[16], float& mref, float& lsum, Stream& S, const int qb, const int t0, const int t1,
                  const unsigned lane, const unsigned wid, const char* fh, const char* ml, const unsigned token, const bool consume, float& mb, float& lb, bool& late, const unsigned pj, const char* opc TR_ARG) {
  TRC();
  const int qrow = qb * 128 + (int)wid * 32 + (int)(lane & 31) - 4 * (int)(lane >> 5);
  unsigned rb_; open_item(S, rb_, S.lane16);
  bf16x8 fr[8];
#pragma unroll
  for (int j = 0; j < 8; ++j) fr[j] = ldsr(rb_ + j * 1024);
  f32x16 c0, c1; float rs0 = 0.f, rs1 = 0.f;
  set_c(c0, c1, 0.f, qrow - t0 * 64);
  SBAR();
  b_tile<true>(o, q, fr, c0, c1, mref, rs0, rs1, S, rb_);
  const int tc = consume ? t1 : -8;
  for (int t = t0 + 1; t < t1; ++t) {
    TRC();
    hand_step(fh, ml, token, tc - t, late);
    if (tc - t == 2 && late) S.tab = lane - pj < 2u ? 0x7ffffff0u : S.tab;
    if (t == t1 - 2 || t1 - t0 == 2) touch(opc);
    if (t >= 2 * qb) set_c(c0, c1, -mref, qrow - t * 64);
    b_tile<false>(o, q, fr, c0, c1, mref, rs0, rs1, S, rb_);
  }
  SBAR();
  asm volatile("v_accvgpr_read_b32 %0, a252\n\tv_accvgpr_read_b32 %1, a253" : "=v"(mb), "=v"(lb));
  MFMA_DRAIN_O(o); SBAR(); TRC();
  unopen_item(S);
  lsum = hsum(rs0 + rs1);
}

DI void a_finish(bf16x8 (&ca)[2], f32x16 o, const float g, const unsigned lane) {
  const bool up = lane >= 32;
  const float l = halves(o[12])[1];
  const float sc = g / l + l * 0.f;
#pragma unroll
  for (int e = 12; e < 16; ++e) o[e] = up ? 0.f : o[e];
  ca[0] = pack8(o, 0, sc); ca[1] = pack8(o, 1, sc);
}
DI bf16x8 ld16(__amdgpu_buffer_rsrc_t rs, unsigned voff, unsigned soff) { return __builtin_bit_cast(bf16x8, __builtin_amdgcn_raw_buffer_load_b128(rs, voff, soff, 16)); }
struct KvOut { __amdgpu_buffer_rsrc_t rs; unsigned dst, s2, s1, s0, sv, wof, gtab, gv0; const char* woa; const char* wob; };
#define AC 52
constexpr int a_ops(int st) {
  return (st < 32) + (st < 4 ? 2 : 0) + (st < 16 ? 2 : 0) + (st >= 26 && st < 30) + (((st >= 16 && st < 20) || (st >= 24 && st < 28) || (st >= 38 && st < 42)) ? 2 : 0) + (st == AC - 4) + (st == AC - 3) + (st == AC - 1) + (st >= AC && st < AC + 8 ? 4 : 0) + (st >= AC + 8 && st < AC + 12 ? 2 : 0); }
constexpr int a_sum(int a, int b) { int n = 0; for (int i = a; i < b; ++i) n += a_ops(i); return n; }
DI void a_phase(bf16x8 (&ca)[2], bf16x8 (&q)[16], const unsigned qoff, const f32x4 (&acc)[32][2], const Sync& Y, const KvOut& KO, Stream& S, float& g, const unsigned gp, const unsigned lane,
                char* f2_mine, const unsigned f2_need, const unsigned token, const char* pc, const unsigned line, const unsigned wid TR_ARG) {
  TRC();
  const bf16x8 q0 = Y.qa[0], q1 = Y.qa[1];
  unsigned rb_, rn_; open_item(S, rb_, S.lane16); rn_ = rb_;
  bf16x8 kf[2][2], va[2], vb[2];
  kf[0][0] = ldsr(rb_); kf[0][1] = ldsr(rb_ + 1024); kf[1][0] = ldsr(rb_ + 4096); kf[1][1] = ldsr(rb_ + 5120);
  va[0] = ldsr(rb_ + 2048); vb[0] = ldsr(rb_ + 3072); va[1] = ldsr(rb_ + 4096 + 2048); vb[1] = vb[0];
  f32x16 s[2], o = {}, negm;
  { f32x16 z; asm volatile("v_mfma_f32_32x32x16_bf16 %0, %1, %2, 0" : "=&v"(z) : "v"(kf[0][0]), "v"(q0)); mfma_v(z, kf[0][1], q1); MFMA_DRAIN_V(z);
    negm = splat16(-max16(z)); PIN(negm); }
  u32x4 p0, p1 = {0u, 0u, 0u, 0u}, ow, gsrd; float pt[4] = {0.f, 0.f, 0.f, 0.f}; unsigned fw, gt[4][8], gso, gv1;
  PIN(o); asm volatile("s_nop 1" ::: "memory");
  mfma_vc(s[0], kf[0][0], q0, negm); mfma_v(s[0], kf[0][1], q1);
  kf[0][0] = ldsr(rb_ + 8192); kf[0][1] = ldsr(rb_ + 9216);
  MFMA_DRAIN_V(s[0]);
  SBAR();
#define AADDR(blk, off) ((blk) < 8 ? rb_ + (blk) * 4096 + (off) : rn_ + ((blk) - 8) * 4096 + (off))
#define AEXP(e) do { pe[e] = __builtin_amdgcn_exp2f(s[c][e]); PIN(pe[e]); } while (0)
#define ACVT(P, i, a, b) do { unsigned w_ = cvtpk(a, b); PIN(w_); P[i] = w_; } while (0)
#define TGO(p) do { set_m0(Y.tm0 + (p) * 4096); unsigned so_ = Y.tso + (p) * 4096; PINS(so_); NOP0(); dma16<0, 1>(S.lane16, S.srd, so_); } while (0)
#define BGO(p) do { if constexpr (((p) & 3) == 0) { st_pre<(p) & 4>(S, S.y); NOP0(); } st_go<(p) & 7>(S); if constexpr (((p) & 7) == 7) { st_prep(S, S.y); asm volatile("s_nop 3" ::: "memory"); } } while (0)
#define GDESC(f) do { const unsigned e_ = __builtin_amdgcn_readlane(KO.gtab, f); gsrd = (e_ & 0x40000000u) ? mksrd(KO.woa, 448 * 896) : mksrd(KO.wob, 4096 * 896); gso = e_ & 0x3fffffffu; \
    gv1 = ((int)e_ < 0 && lane >= 32) ? 0x7ffffff0u : KO.gv0 + 8 * 896; PINS(gsrd); PINS(gso); PIN(gv1); } while (0)
#define GLD(j) asm volatile("buffer_load_ushort %0, %1, %2, %3 offen offset:%4" : "=v"(gt[st >> 2][j]) : "v"((j) < 4 ? KO.gv0 : gv1), "s"(gsrd), "s"(gso), "i"(((j) & 3) * 896) : "memory")
#define QLD(f) asm volatile("buffer_load_dwordx4 %0, %1, %2, %3 offen sc1" : "=v"(q[f]) : "v"(S.lane16), "s"(S.srd), "s"(qoff + (f) * 1024) : "memory")
  GDESC(0); asm volatile("s_nop 4" ::: "memory");
  sfor<64>([&](auto ST) __attribute__((always_inline)) { constexpr int st = decltype(ST)::v, it = st >> 3, kb = st & 7, c = kb & 1, n = c ^ 1;
    constexpr bool has_st = st < 32; constexpr int se = st & 31;
    constexpr int rq = (st >= 16 && st < 20) ? st - 16 : (st >= 24 && st < 28) ? st - 24 : (st >= 38 && st < 42) ? st - 38 : -1;
    if constexpr (kb == 0) { TRC(); rb_ = rn_; }
    if constexpr (st == 36) DRAIN();
    if constexpr (kb == 5 && it < 7) { wait_bar<it == 0 ? a_sum(0, 5) : it == 1 ? a_sum(4, 13) : it == 6 ? a_sum(42, 53) : 63, false>(); open_item(S, rn_, S.lane16); }
    if constexpr (st == 37) { if (wid == 0) flag_set(f2_mine, token); }
    if constexpr (st == 26) asm volatile("s_waitcnt vmcnt(%0)" ::"i"(a_sum(16, 26)) : "memory");
        if constexpr (st == AC) { asm volatile("s_waitcnt vmcnt(2)" : "+v"(fw)); if (!__all(fw == token)) flag_wait(Y.ws + f2_need, token); }
    float pe[16];
    if constexpr (st < 62) wait_lgkm<4>();
    if constexpr (st < 63) mfma_vc(s[n], kf[n][0], q0, negm);
    if constexpr (st < 61) kf[n][0] = ldsr(AADDR(kb + 3, 0));
    if constexpr (st < 16) GLD(2 * (st & 3));
    if constexpr (st >= AC && st < AC + 8) QLD(2 * (st - AC));
    SBAR();
    AEXP(0); AEXP(1); ACVT(p1, 2, pt[0], pt[1]); AEXP(2); AEXP(3); ACVT(p1, 3, pt[2], pt[3]);
    if constexpr (has_st) { ACVT(ow, 0, ard(acc[se][0][0]), ard(acc[se][0][1])); ACVT(ow, 1, ard(acc[se][0][2]), ard(acc[se][0][3])); }
    if constexpr (rq >= 0 && (rq & 1) == 0) st_pre<(rq & 2) * 2>(S, S.y);
    SBAR();
    if constexpr (st < 63) mfma_v(s[n], kf[n][1], q1);
    if constexpr (st < 61) kf[n][1] = ldsr(AADDR(kb + 3, 1024));
    if constexpr (rq >= 0) st_go<2 * (rq & 3)>(S);
    if constexpr (st < 4) TGO(2 * st);
    if constexpr (st == AC - 3) touch(code_line(pc, LOFF(b), LOFF(c), line));
    if constexpr (st == AC - 4) asm volatile("buffer_load_dword %0, %1, %2, 0 offen sc1" : "=v"(fw) : "v"(f2_need), "s"(S.srd) : "memory");
    if constexpr (st == AC - 1) asm volatile("buffer_load_dword %0, %1, %2, %3 offen sc1" : "=v"(g) : "v"((lane & 31) * 4), "s"(S.srd), "s"(gp) : "memory");
    if constexpr (st >= AC && st < AC + 12) BGO(2 * (st - AC));
    SBAR();
    AEXP(4); AEXP(5); ACVT(p0, 0, pe[0], pe[1]); AEXP(6); AEXP(7); ACVT(p0, 1, pe[2], pe[3]);
    if constexpr (has_st) { ACVT(ow, 2, ard(acc[se][1][0]), ard(acc[se][1][1])); ACVT(ow, 3, ard(acc[se][1][2]), ard(acc[se][1][3])); }
    SBAR();
    mfma_v(o, vb[n], __builtin_bit_cast(bf16x8, p1));
    if constexpr (st < 63) vb[n] = ldsr(AADDR(kb + 1, 3072));
    if constexpr (has_st) st16(KO.rs, KO.sv, KO.dst + (se >> 3) * 16384 + ((se >> 2) & 1) * KO.s2 + ((se >> 1) & 1) * KO.s1 + (se & 1) * KO.s0, __builtin_bit_cast(bf16x8, ow));
    if constexpr (st >= AC && st < AC + 8) QLD(2 * (st - AC) + 1);
    SBAR();
    AEXP(8); ACVT(p0, 2, pe[4], pe[5]); AEXP(9); ACVT(p0, 3, pe[6], pe[7]); SBAR(); AEXP(10); AEXP(11);
    if constexpr (st >= 26 && st < 30) {
#pragma unroll
      for (int e = 0; e < 4; ++e) { asm volatile("" : "+v"(gt[st - 26][2 * e]), "+v"(gt[st - 26][2 * e + 1])); ow[e] = gt[st - 26][2 * e] | (gt[st - 26][2 * e + 1] << 16); }
      PIN(ow); SBAR(); }
    SBAR();
    mfma_v(o, va[c], __builtin_bit_cast(bf16x8, p0));
    if constexpr (st < 62) va[c] = ldsr(AADDR(kb + 2, 2048));
    if constexpr (rq >= 0) st_go<2 * (rq & 3) + 1>(S);
    if constexpr (st < 4) TGO(2 * st + 1);
    if constexpr (st < 16) GLD(2 * (st & 3) + 1);
    if constexpr (st >= 26 && st < 30) st16(KO.rs, S.lane16, KO.wof + (st - 26) * 1024, __builtin_bit_cast(bf16x8, ow));
    if constexpr (st >= AC && st < AC + 12) BGO(2 * (st - AC) + 1);
    SBAR();
    if constexpr (st < 12 && (st & 3) == 3) GDESC((st >> 2) + 1);
    AEXP(12); AEXP(13); ACVT(p1, 0, pe[8], pe[9]); AEXP(14); AEXP(15); ACVT(p1, 1, pe[10], pe[11]);
    pt[0] = pe[12]; pt[1] = pe[13]; pt[2] = pe[14]; pt[3] = pe[15];
    if constexpr (rq == 3) st_prep(S, S.y);
    SBAR();
  });
#undef AADDR
#undef AEXP
#undef ACVT
#undef TGO
#undef BGO
#undef GDESC
#undef GLD
#undef QLD
  asm volatile("" ::"v"(negm));
  p1[2] = cvtpk(pt[0], pt[1]); p1[3] = cvtpk(pt[2], pt[3]);
  mfma_vn(o, vb[1], __builtin_bit_cast(bf16x8, p1));
  MFMA_DRAIN_V(o);
  wait_bar<8, false>();
  asm volatile("" : "+v"(g), "+v"(q[0]), "+v"(q[1]), "+v"(q[2]), "+v"(q[3]), "+v"(q[4]), "+v"(q[5]), "+v"(q[6]), "+v"(q[7]));
  asm volatile("" : "+v"(q[8]), "+v"(q[9]), "+v"(q[10]), "+v"(q[11]), "+v"(q[12]), "+v"(q[13]), "+v"(q[14]), "+v"(q[15]));
  a_finish(ca, o, g, lane);
}

DI void skip_slot(unsigned& s, unsigned res) { s = s == res ? nslot(s) : s; PINS(s); }
template <bool MERGE>
DI void op_head(f32x16 (&o)[8], const bf16x8 (&ca)[2], u32x4 (&cb)[16], f32x16& c0, f32x16& z, bf16x8 (&fr)[6], const float fa, const float fb, Stream& S, unsigned& ra, unsigned& rb_, unsigned& res, const unsigned wid) {
  u32x4 pb[16];
#define CONV(c, e) do { const float x0 = ard(o[(c) >> 1][8 * ((c) & 1) + 2 * (e)]), x1 = ard(o[(c) >> 1][8 * ((c) & 1) + 2 * (e) + 1]); \
    unsigned v_ = MERGE ? cvtpk(fmaf(x0, fa, bflo(pb[c][e]) * fb), fmaf(x1, fa, bfhi(pb[c][e]) * fb)) : cvtpk(x0 * fa, x1 * fa); PIN(v_); cb[c][e] = v_; } while (0)
  if constexpr (MERGE) {
    sfor<2>([&](auto H) __attribute__((always_inline)) { constexpr int h = decltype(H)::v;
      open_item(S, rb_, wid * 8192 + S.lane16);
      sfor<8>([&](auto C) __attribute__((always_inline)) { constexpr int c = decltype(C)::v; st_pre<c>(S, S.y); pb[8 * h + c] = __builtin_bit_cast(u32x4, ldsr(rb_ + c * 1024)); st_go<c>(S); SBAR(); });
      st_prep(S, S.y);
      wait_bar<16, false>(); });
  }
  res = S.rslot; open_item(S, ra, S.lane16);
  fr[0] = ldsr(ra); fr[1] = ldsr(ra + 26 * 1024); fr[2] = ldsr(ra + 1024); fr[3] = ldsr(ra + 27 * 1024);
  CONV(0, 0); CONV(0, 1); CONV(0, 2); CONV(0, 3);
  wait_bar<8, false>(); open_item(S, rb_, S.lane16);
  fr[4] = ldsr(rb_); fr[5] = ldsr(rb_ + 1024);
  SBAR();
  sfor<36>([&](auto U) __attribute__((always_inline)) { constexpr int u = decltype(U)::v, v = u + 6; const f32x16 zero = {};
    if constexpr (u == 32) { wait_bar<8, false>(); skip_slot(S.rslot, res); open_item(S, rb_, S.lane16); }
    if constexpr (u == 0) c0 = MFMA(fr[0], ca[0], zero); else if constexpr (u == 1) z = MFMA(fr[1], ca[0], zero);
    else if constexpr (u == 2) c0 = MFMA(fr[2], ca[1], c0); else if constexpr (u == 3) z = MFMA(fr[3], ca[1], z);
    else if constexpr (u & 1) z = MFMA(fr[u % 6], __builtin_bit_cast(bf16x8, cb[(u - 4) >> 1]), z);
    else c0 = MFMA(fr[u % 6], __builtin_bit_cast(bf16x8, cb[(u - 4) >> 1]), c0);
    SBAR();
    fr[u % 6] = ldsr(v < 36 ? rb_ + (v - 4) * 1024 : v < 38 ? ra + (v - 34) * 1024 : rb_ + (v - 38) * 1024);
    if constexpr (u < 16 && (u & 1)) st_go<(u >> 1)>(S);
    SBAR();
    if constexpr (u == 0 || u == 8) st_pre<u / 2>(S, S.y);
    if constexpr (u == 18) { skip_slot(S.dslot, res + wid * 8192); st_prep(S, S.y); }
    if constexpr (u < 30) { CONV(1 + (u >> 1), 2 * (u & 1)); CONV(1 + (u >> 1), 2 * (u & 1) + 1); }
    SBAR(); });
#undef CONV
}
DI float op_body(const bf16x8 (&ca)[2], const u32x4 (&cb)[16], f32x16& c0, f32x16& z, bf16x8 (&fr)[6], Stream& S, unsigned ra, unsigned rb_, const unsigned res_,
                 const __amdgpu_buffer_rsrc_t rs, unsigned og, const unsigned wid, unsigned& f3v, const char* f3p TR_ARG) {
  f32x16 c1; u32x4 ow; const unsigned res = rfl(res_);
  S.rslot = rfl(S.rslot); S.dslot = rfl(S.dslot); S.dseq = rfl(S.dseq); S.y.so = rfl(S.y.so); S.y.m0 = rfl(S.y.m0);
#pragma unroll 1
  for (int k = 0; k < 6; ++k) {
    TRC();
    sfor<36>([&](auto U) __attribute__((always_inline)) { constexpr int u = decltype(U)::v, v = u + 6, w = u % 18; const f32x16 zero = {};
      f32x16& cur = u < 18 ? c1 : c0; f32x16& prv = u < 18 ? c0 : c1;
      if constexpr (u == 32) { wait_bar<11, false>(); skip_slot(S.rslot, res); open_item(S, rb_, S.lane16); }
      if constexpr (w == 0) cur = MFMA(fr[u % 6], ca[0], zero); else if constexpr (w == 1) cur = MFMA(fr[u % 6], ca[1], cur); else cur = MFMA(fr[u % 6], __builtin_bit_cast(bf16x8, cb[w - 2]), cur);
      SBAR();
      fr[u % 6] = ldsr(v < 18 ? rb_ + (v - 2) * 1024 : v < 20 ? ra + (v - 14) * 1024 : v < 36 ? rb_ + (v - 4) * 1024 : v < 38 ? ra + (v - 30) * 1024 : rb_ + (v - 38) * 1024);
      if constexpr (u % 3 == 1 && u < 32 && u != 10 && u != 28 && u != 31) st_go<(u < 10 ? u / 3 : u / 3 - 1)>(S);
      if constexpr (w == 10 || w == 14) st16(rs, S.lane16 + (u >= 18 ? 2048 : 0) + (w == 14 ? 1024 : 0), og, __builtin_bit_cast(bf16x8, ow));
      SBAR();
      if constexpr (u == 0 || u == 15) st_pre<u ? 4 : 0>(S, S.y);
      if constexpr (u == 26) { skip_slot(S.dslot, res + wid * 8192); st_prep(S, S.y); }
      if constexpr (w >= 6 && w < 14) { unsigned x = cvtpk(ard(prv[2 * (w - 6)]), ard(prv[2 * (w - 6) + 1])); PIN(x); ow[(w - 6) & 3] = x; }
      if constexpr (u == 35) { ra += 4096; PIN(ra); og += 4096; }
      SBAR(); });
  }
  asm volatile("global_load_dword %0, %1, off sc1" : "=v"(f3v) : "v"(f3p) : "memory");
  st16(rs, S.lane16 + 2048, og, pack8(z, 0)); st16(rs, S.lane16 + 3072, og, pack8(z, 1));
  st16(rs, S.lane16, og, pack8(c0, 0)); st16(rs, S.lane16 + 1024, og, pack8(c0, 1));
  return z[0];
}

DI void safe_redo(const __amdgpu_buffer_rsrc_t rs, const unsigned og, const unsigned head, const unsigned tb, const float g, const unsigned lane) {
  const unsigned lane16 = lane * 16;
  bf16x8 ca[2], cb[16];
  {
    const unsigned qa = (unsigned)WS_QA + (head * 64 + tb) * 2048; const bf16x8 q0 = ld16(rs, lane16, qa), q1 = ld16(rs, lane16, qa + 1024);
    float m = -1e30f; f32x16 o = splat16(opaque0());
    for (int kb = 0; kb < 64; ++kb) {
      const unsigned p = (unsigned)WS_KVA + (head * 64 + kb) * 4096;
      f32x16 t = {}; t = MFMA(ld16(rs, lane16, p), q0, t); t = MFMA(ld16(rs, lane16, p + 1024), q1, t);
      const float mn = fmaxf(m, max16(t)), al = __builtin_amdgcn_exp2f(m - mn); m = mn;
#pragma unroll
      for (int e = 0; e < 16; ++e) t[e] = __builtin_amdgcn_exp2f(t[e] - mn);
      o *= al; o = MFMA(ld16(rs, lane16, p + 2048), pack8(t, 0), o); o = MFMA(ld16(rs, lane16, p + 3072), pack8(t, 1), o);
    }
    a_finish(ca, o, g, lane);
  }
  {
    const unsigned qg = (unsigned)WS_QB + (head * 64 + tb) * 16384;
    const int qrow = (int)tb * 32 + (int)(lane & 31) - 4 * (int)(lane >> 5);
    float m = -1e30f, ls = 0.f; f32x16 o[8];
#pragma unroll
    for (int d = 0; d < 8; ++d) o[d] = splat16(opaque0());
    for (int kb = 0; kb <= (int)tb; ++kb) {
      const unsigned kp = (unsigned)WS_KB + (head * 64 + kb) * 16384, vp = (unsigned)WS_VB + (head * 64 + kb) * 16384;
      f32x16 s = {};
#pragma unroll
      for (int j = 0; j < 16; ++j) s = MFMA(ld16(rs, lane16, kp + j * 1024), ld16(rs, lane16, qg + j * 1024), s);
#pragma unroll
      for (int e = 0; e < 16; ++e) s[e] = maskv(s[e], qrow - kb * 32, e);
      const float mn = fmaxf(m, max16(s)), al = __builtin_amdgcn_exp2f(m - mn); m = mn; ls *= al;
#pragma unroll
      for (int e = 0; e < 16; ++e) { s[e] = __builtin_amdgcn_exp2f(s[e] - mn); ls += s[e]; }
      const bf16x8 p0 = pack8(s, 0), p1 = pack8(s, 1);
#pragma unroll
      for (int d = 0; d < 8; ++d) { o[d] *= al; o[d] = MFMA(ld16(rs, lane16, vp + d * 1024), p0, o[d]); o[d] = MFMA(ld16(rs, lane16, vp + 8192 + d * 1024), p1, o[d]); }
    }
    const float inv = (1.f - g) / hsum(ls);
#pragma unroll
    for (int c = 0; c < 16; ++c) cb[c] = pack8(o[c >> 1], c & 1, inv);
  }
  const unsigned wf = (unsigned)WS_WOF + head * 262144;
  DRAIN();
  for (int nb = 0; nb < 14; ++nb) {
    const int fa = 2 * nb;
    f32x16 a = {}; a = MFMA(ld16(rs, lane16, wf + fa * 1024), ca[0], a); a = MFMA(ld16(rs, lane16, wf + (fa + 1) * 1024), ca[1], a);
#pragma unroll
    for (int ks = 0; ks < 16; ++ks) a = MFMA(ld16(rs, lane16, wf + wf_b(nb, ks) * 1024), cb[ks], a);
    st16(rs, lane16, og + (2 * nb) * 1024, pack8(a, 0)); st16(rs, lane16, og + (2 * nb + 1) * 1024, pack8(a, 1));
  }
}

__global__ __launch_bounds__(256, 1) void fused_fwd(const char* __restrict__ x, const char* __restrict__ wqkv, const char* __restrict__ ckv, const char* __restrict__ wq,
                                                    const char* __restrict__ wuk, const char* __restrict__ wuv, const char* __restrict__ gate_w,
                                                    const char* __restrict__ wo_a, const char* __restrict__ wo_b, char* ws, char* __restrict__ out) {
  extern __shared__ __attribute__((aligned(16))) char lds[];
  const unsigned wid = rfl(threadIdx.x >> 6);
  STAMP_DECL STAMPRT(0); STAMPIT(1); TR_DECL TRC();
  Sync Y; asm volatile("s_getpc_b64 %0\n.Lgm_base:" : "=s"(Y.pc)); Y.ws = ws; Y.lane = threadIdx.x & 63;
  touch(Y.pc + threadIdx.x * 64);
  asm volatile("global_load_dword %0, %1, off sc1" : "=v"(Y.token) : "v"(ws + WS_W0) : "memory");
  const __amdgpu_buffer_rsrc_t rs = __builtin_amdgcn_make_buffer_rsrc(ws, 0, (int)WS_END, 0x20000);
  Y.rs = rs;
  { const unsigned b_ = blockIdx.x, p_ = b_ >> 5; Y.qao = (unsigned)WS_QA + ((2 * (b_ & 7) + ((b_ >> 3) & 1)) * 64 + (((b_ >> 4) & 1) ? p_ : 15 - p_) * 4 + wid) * 2048; }
  f32x4 acc4[32][2];
  proj_phase(acc4, x, wqkv, ckv, wq, wuk, wuv, gate_w, wo_a, wo_b, ws, (unsigned)WS_KVA + (2 * (blockIdx.x & 7) + ((blockIdx.x >> 3) & 1)) * 262144, Y, Y.lane, wid TR_PASS);
  STAMPIT(2); STAMPRT(11);
  LABEL(a);
  unsigned lane, bid = blockIdx.x;
  asm volatile("v_mbcnt_lo_u32_b32 %0, -1, 0\n\tv_mbcnt_hi_u32_b32 %0, -1, %0" : "=v"(lane)); PINS(bid);
  const unsigned lane16 = lane * 16, tid = wid * 64 + lane;
  const unsigned token = Y.token;
  if (bid == 0 && wid == 0) flag_set(ws + WS_W0, token);
  const Role R_ = mk_role(bid, wid);
  const unsigned head = R_.head, pw = R_.pw, po = R_.po, tb = R_.tb, pj = 8 + 2 * TA; const int qlong = R_.qlong, nt1 = R_.nt1, qb = R_.qb, nt2 = R_.nt2; const bool produce = R_.produce, consume = R_.consume;
  Stream S; S.tab = Y.tab; S.srd = mksrd(ws, (unsigned)WS_END); S.lane16 = lane16; S.dseq = 5; S.dslot = 65536 + wid * 8192; S.rslot = 65536;
  st_prep(S, S.y);
  wait_bar<22, false>();
  asm volatile("" : "+v"(Y.qa[0]), "+v"(Y.qa[1]));
  float g;
  STAMPIT(3);
  bf16x8 ca[2], q[16];
  { const unsigned xcd = bid & 7, loc = bid >> 3, rb = loc & 7, sel = loc >> 3, head2 = 2 * xcd + (sel & 1), isV = sel >> 1, tb0 = rb * 8 + (wid >> 1) * 4;
    KvOut KO; KO.rs = rs; KO.dst = (isV ? (unsigned)WS_VB : (unsigned)WS_KB) + ((head2 * 64 + tb0) * 16 + (wid & 1) * (isV ? 4 : 8)) * 1024; KO.s2 = isV ? 2048 : 256; KO.s1 = isV ? 1024 : 4096; KO.s0 = isV ? 256 : 2048;
    KO.sv = Y.ksv; KO.wof = (unsigned)WS_WOF + (bid * 4 + wid) * 4096; KO.gtab = Y.gtab; KO.gv0 = 4 * (lane >> 5) * 896 + 2 * (lane & 31); KO.woa = wo_a; KO.wob = wo_b;
    a_phase(ca, q, (unsigned)WS_QB + (head * 64 + (produce ? qlong * 4 + wid : tb)) * 16384, acc4, Y, KO, S, g, (unsigned)WS_G + tb * 128, lane,
            ws + WS_F2 + bid * F2S, Y.need, token, Y.pc, tid, wid TR_PASS); }
  STAMPIT(4); STAMPRT(12);
  LABEL(b);
  char* fh = ws + WS_FH + pw * 64; char* ml = ws + WS_ML + pw * 512 + lane * 8;
  f32x16 o[8]; float mref, lsum, mb, lb; bool late = false;
  for (int pass = produce ? 0 : 1; pass < 2; ++pass) {
    b_segment(o, q, mref, lsum, S, pass ? qb : qlong, pass ? 0 : TA, pass ? nt2 : TA + nt1, lane, wid, fh, ml, token, consume, mb, lb, late, pj, code_line(Y.pc, LOFF(c), LOFF(end), tid) TR_PASS);
    if (pass == 0) {
#pragma unroll
      for (int j = 0; j < 16; ++j) q[j] = ld16(rs, lane16, (unsigned)WS_QB + ((head * 64 + tb) * 16 + j) * 1024);
      const float inv = lsum > 0.f ? 1.f / lsum : 0.f;
#pragma unroll
      for (int c = 0; c < 16; ++c) { if (!(c & 1)) PINA(o[c >> 1]); st16(rs, lane16, po + c * 1024, pack8(o[c >> 1], c & 1, inv)); SBAR(); }
      __hip_atomic_store((GAS unsigned long long*)ml, ((unsigned long long)__float_as_uint(lsum) << 32) | __float_as_uint(mref), __ATOMIC_RELAXED, __HIP_MEMORY_SCOPE_AGENT);
      DRAIN();
      flag_set(fh, token);
    }
  }
  STAMPIT(5); STAMPRT(13);
  LABEL(c);
  const unsigned og = (unsigned)WS_OP + (head * 64 + tb) * 28 * 1024;
  u32x4 cb[16]; f32x16 c0, z; bf16x8 fr[6]; unsigned ra, rb_, res;
  float chk; unsigned f3v; const char* f3p = ws + WS_F3 + (tb * 16 + (lane & 15)) * F3S;
  if (consume) {
    if (late) {
      flag_wait(fh, token);
      const unsigned long long w = __hip_atomic_load((GAS unsigned long long*)ml, __ATOMIC_RELAXED, __HIP_MEMORY_SCOPE_AGENT);
      mb = __uint_as_float((unsigned)w); lb = __uint_as_float((unsigned)(w >> 32));
      Desc D; D.so = po; D.m0 = S.rslot + wid * 8192;
      sfor<16>([&](auto Q) __attribute__((always_inline)) { constexpr int qq = decltype(Q)::v;
        if constexpr (qq == 8) { D.so = po + 8192; D.m0 = nslot(S.rslot) + wid * 8192; }
        st_pre<(qq & 7)>(S, D); if constexpr (!(qq & 3)) NOP0(); st_go<(qq & 7)>(S); });
      DRAIN();
    }
    const float m = lb > 0.f ? fmaxf(mref, mb) : mref, ea = __builtin_amdgcn_exp2f(mref - m), wb = lb > 0.f ? lb * __builtin_amdgcn_exp2f(mb - m) : lb * 0.f;
    const float den = lsum * ea + wb, inv = (1.f - g) / den;
    chk = den; op_head<true>(o, ca, cb, c0, z, fr, ea * inv, wb * inv, S, ra, rb_, res, wid);
  } else { chk = lsum; op_head<false>(o, ca, cb, c0, z, fr, (1.f - g) / lsum, 0.f, S, ra, rb_, res, wid); }
  chk += op_body(ca, cb, c0, z, fr, S, ra, rb_, res, rs, og, wid, f3v, f3p TR_PASS);
  STAMPIT(6); STAMPRT(14);
  DRAIN(); asm volatile("" : "+v"(f3v));
  if (__builtin_expect(__any(!(chk * 0.f == 0.f)), 0)) { safe_redo(rs, og, head, tb, g, lane); DRAIN(); }
  flag_set(ws + WS_F3 + (tb * 16 + head) * F3S, token);
  STAMPIT(7);
  if (!__all(f3v == token || (lane & 15) == head)) flag_wait<SLP3>(f3p, token);
  STAMPIT(8);
  {
    u32x4 v[2][16]; unsigned elo = 0x00003f80u, ehi = 0x3f800000u; PIN(elo); PIN(ehi);
    const unsigned c0 = head * 112 + lane, c1 = lane < 48 ? c0 + 64 : c0;
#pragma unroll
    for (int h = 0; h < 16; ++h) { v[0][h] = __builtin_bit_cast(u32x4, ld16(rs, c0 * 16, (unsigned)WS_OP + (h * 64 + tb) * 28 * 1024)); v[1][h] = __builtin_bit_cast(u32x4, ld16(rs, c1 * 16, (unsigned)WS_OP + (h * 64 + tb) * 28 * 1024)); }
    SBAR();
#pragma unroll
    for (int k = 0; k < 2; ++k) { float a[8] = {};
#pragma unroll
      for (int h = 0; h < 16; ++h)
#pragma unroll
        for (int e = 0; e < 4; ++e) { a[2 * e] = dot2(v[k][h][e], elo, a[2 * e]); a[2 * e + 1] = dot2(v[k][h][e], ehi, a[2 * e + 1]); }
      const unsigned c = k ? c1 : c0;
      char* op = out + (size_t)(tb * 32 + (c & 31)) * 896 + ((c >> 6) * 16 + 4 * ((c >> 5) & 1)) * 2;
      if (k == 0 || lane < 48) { *(u32x2*)op = (u32x2){cvtpk(a[0], a[1]), cvtpk(a[2], a[3])}; *(u32x2*)(op + 16) = (u32x2){cvtpk(a[4], a[5]), cvtpk(a[6], a[7])}; } }
  }
  LABEL(end);
  STAMPIT(9); STAMPRT(10); STAMP_OUT(1, 15); TRC(); TR_OUT();
}

extern "C" void kernel_launch(void* const* d_in, const int* in_sizes, int n_in, void* d_out, int out_size, void* d_ws, size_t ws_size, hipStream_t stream) {
  (void)in_sizes; (void)n_in; (void)out_size; (void)ws_size;
  (void)hipFuncSetAttribute((const void*)fused_fwd, hipFuncAttributeMaxDynamicSharedMemorySize, 163840);
  fused_fwd<<<256, 256, 163840, stream>>>((const char*)d_in[0], (const char*)d_in[1], (const char*)d_in[4], (const char*)d_in[5], (const char*)d_in[6], (const char*)d_in[7], (const char*)d_in[9],
                                          (const char*)d_in[2], (const char*)d_in[8], (char*)d_ws, (char*)d_out);
}
```

```cpp
#include <hip/hip_runtime.h>
#include <stdint.h>
#include <utility>

typedef short bf16x8 __attribute__((ext_vector_type(8)));
typedef short s16x4 __attribute__((ext_vector_type(4)));
typedef float f32x16 __attribute__((ext_vector_type(16)));
typedef float f32x4 __attribute__((ext_vector_type(4)));
typedef float f32x2 __attribute__((ext_vector_type(2)));
typedef __bf16 bf16x2_t __attribute__((ext_vector_type(2)));
typedef unsigned u32x4 __attribute__((ext_vector_type(4)));
typedef unsigned u32x2 __attribute__((ext_vector_type(2)));
#define LAS __attribute__((address_space(3)))
#define GAS __attribute__((address_space(1)))
#define DI __device__ __forceinline__
#define SBAR() __builtin_amdgcn_sched_barrier(0)
#define MFMA(a, b, c) __builtin_amdgcn_mfma_f32_32x32x16_bf16((a), (b), (c), 0, 0, 0)
#define MFMA16(a, b, c) __builtin_amdgcn_mfma_f32_16x16x32_bf16((a), (b), (c), 0, 0, 0)
#define PIN(x) asm volatile("" : "+v"(x))

constexpr size_t MB = 1u << 20;
constexpr size_t WS_QB = 0, WS_KB = 16 * MB, WS_VB = 32 * MB, WS_QA = 48 * MB, WS_KVA = 50 * MB, WS_G = 54 * MB, WS_W0 = 54 * MB + 65536, WS_F1 = WS_W0 + 4096, WS_F2 = WS_W0 + 65536, WS_F3 = WS_W0 + 131072, WS_FH = WS_W0 + 32768,
                 WS_ML = 55 * MB, WS_PO = 56 * MB, WS_OP = 64 * MB, WS_WOF = 92 * MB, WS_END = 96 * MB;
#define TA_ 18
constexpr int TA = TA_;
#define F2S 128
#define SLP 8
#define SLP3 2
#define F3S 4
constexpr float MBIAS = 64.f;
constexpr float LOG2E = 1.4426950408889634f;
constexpr float QSCALE_B = 0.0625f * LOG2E;
constexpr float QSCALE_A = 0.18898223650461362f * LOG2E;

template <int I> struct IC { static constexpr int v = I; };
template <class F, int... Is> DI void sfor_(F&& f, std::integer_sequence<int, Is...>) { (f(IC<Is>{}), ...); }
template <int N, class F> DI void sfor(F&& f) { sfor_(f, std::make_integer_sequence<int, N>{}); }
DI void mfma_vc(f32x16& d, const bf16x8& a, const bf16x8& b, const f32x16& c) { asm volatile("v_mfma_f32_32x32x16_bf16 %0, %1, %2, %3" : "=&v"(d) : "v"(a), "v"(b), "v"(c)); }
DI void mfma_v(f32x16& d, const bf16x8& a, const bf16x8& b) { asm volatile("v_mfma_f32_32x32x16_bf16 %0, %1, %2, %0" : "+v"(d) : "v"(a), "v"(b)); }
DI void mfma_vn(f32x16& d, const bf16x8& a, const bf16x8& b) { asm volatile("s_nop 1\n\tv_mfma_f32_32x32x16_bf16 %0, %1, %2, %0" : "+v"(d) : "v"(a), "v"(b)); }
#define MFMA_DRAIN_V(x) asm volatile("s_nop 7\n\ts_nop 7" : "+v"(x))
DI float ard(float a) { float r; asm volatile("v_accvgpr_read_b32 %0, %1" : "=v"(r) : "a"(a)); return r; }
DI float opaque0() { float z; asm volatile("v_mov_b32 %0, 0" : "=v"(z)); return z; }
DI unsigned wf_src(unsigned f, unsigned hd) { const bool isa = f < 32;
  const unsigned nt = isa ? (f < 28 ? f : 0) : f < 64 ? (f & 1) + ((f >> 1) & 1) * 26 : 2 * ((f - 48) >> 4) + (f & 1), F = isa ? 0 : f < 64 ? (f - 32) >> 2 : (f >> 1) & 7;
  return ((hd * (isa ? 28 : 256) + 32 * F) * 896 + 32 * nt) | (isa ? 0x40000000u : 0u); }
DI unsigned rfl(unsigned x) { return __builtin_amdgcn_readfirstlane(x); }
DI unsigned cvtpk(float lo, float hi) { f32x2 v = {lo, hi}; bf16x2_t b = __builtin_convertvector(v, bf16x2_t); return __builtin_bit_cast(unsigned, b); }
DI float dot2(unsigned a, unsigned b, float c) { return __builtin_amdgcn_fdot2_f32_bf16(__builtin_bit_cast(bf16x2_t, a), __builtin_bit_cast(bf16x2_t, b), c, false); }
DI float bflo(unsigned w) { return __uint_as_float(w << 16); }
DI float bfhi(unsigned w) { return __uint_as_float(w & 0xffff0000u); }
DI u32x4 mksrd(const void* p, unsigned bytes) { u32x4 s = {(unsigned)(uintptr_t)p, (unsigned)((uintptr_t)p >> 32) & 0xffffu, bytes, 0x20000u}; return s; }
DI bf16x8 ldsr(unsigned a) { return *(const LAS bf16x8*)(uintptr_t)a; }
DI bf16x8 ldstr(unsigned a, unsigned a2) {
  s16x4 x = __builtin_amdgcn_ds_read_tr16_b64_v4i16((LAS s16x4*)(uintptr_t)a);
  s16x4 y = __builtin_amdgcn_ds_read_tr16_b64_v4i16((LAS s16x4*)(uintptr_t)a2);
  return __builtin_shufflevector(x, y, 0, 1, 2, 3, 4, 5, 6, 7);
}
DI void set_m0(unsigned v) { asm volatile("s_mov_b32 m0, %0" ::"s"(v) : "memory"); }
template <int OFF, int POL = 1> DI void dma16(unsigned voff, u32x4 srd, unsigned soff) {
  if constexpr (POL == 1) asm volatile("buffer_load_dwordx4 %0, %1, %2 offen offset:%3 sc1 lds" ::"v"(voff), "s"(srd), "s"(soff), "i"(OFF) : "memory");
  else if constexpr (POL == 2) asm volatile("buffer_load_dwordx4 %0, %1, %2 offen offset:%3 nt lds" ::"v"(voff), "s"(srd), "s"(soff), "i"(OFF) : "memory");
  else asm volatile("buffer_load_dwordx4 %0, %1, %2 offen offset:%3 lds" ::"v"(voff), "s"(srd), "s"(soff), "i"(OFF) : "memory");
}
DI void touch(const char* p) { asm volatile("global_load_dword a255, %0, off" ::"v"(p) : "memory", "a255"); }
#define LABEL(n) asm volatile(".Lgm_" #n ":" ::: "memory")
#define LOFF(n) ({ unsigned o_; asm volatile("s_mov_b32 %0, .Lgm_" #n "-.Lgm_base" : "=s"(o_)); o_; })
DI const char* code_line(const char* pc, unsigned from, unsigned to, unsigned line) { const unsigned o = from + line * 64; return pc + (o < to ? o : from); }
#define PINS(x) asm volatile("" : "+s"(x))
#define PINA(x) asm volatile("" : "+a"(x))
#define NOP0() asm volatile("s_nop 0" ::: "memory")
template <int V, bool L0> DI void wait_bar() {
  asm volatile("" ::: "memory");
  __builtin_amdgcn_s_waitcnt((V & 15) | ((V >> 4) << 14) | (7 << 4) | (L0 ? 0 : (15 << 8)));
  __builtin_amdgcn_s_barrier();
  asm volatile("" ::: "memory");
  SBAR();
}
template <int N> DI void wait_lgkm() { __builtin_amdgcn_s_waitcnt(15 | (3 << 14) | (7 << 4) | (N << 8)); }
DI bf16x8 pack8(const f32x16& a, int s, float sc) {
  u32x4 w = {cvtpk(a[8 * s] * sc, a[8 * s + 1] * sc), cvtpk(a[8 * s + 2] * sc, a[8 * s + 3] * sc), cvtpk(a[8 * s + 4] * sc, a[8 * s + 5] * sc), cvtpk(a[8 * s + 6] * sc, a[8 * s + 7] * sc)};
  return __builtin_bit_cast(bf16x8, w);
}
DI bf16x8 pack8(const f32x16& a, int s) {
  u32x4 w = {cvtpk(a[8 * s], a[8 * s + 1]), cvtpk(a[8 * s + 2], a[8 * s + 3]), cvtpk(a[8 * s + 4], a[8 * s + 5]), cvtpk(a[8 * s + 6], a[8 * s + 7])};
  return __builtin_bit_cast(bf16x8, w);
}
DI u32x4 pack44(const f32x4& a, const f32x4& b, float sc) { return (u32x4){cvtpk(a[0] * sc, a[1] * sc), cvtpk(a[2] * sc, a[3] * sc), cvtpk(b[0] * sc, b[1] * sc), cvtpk(b[2] * sc, b[3] * sc)}; }
DI f32x2 halves(float v) {
  auto r = __builtin_amdgcn_permlane32_swap(__float_as_uint(v), __float_as_uint(v), false, false);
  return (f32x2){__uint_as_float(r[0]), __uint_as_float(r[1])};
}
DI f32x2 rows16(float v) {
  auto r = __builtin_amdgcn_permlane16_swap(__float_as_uint(v), __float_as_uint(v), false, false);
  return (f32x2){__uint_as_float(r[0]), __uint_as_float(r[1])};
}
DI float hmax(float v) { const f32x2 h = halves(v); return fmaxf(h[0], h[1]); }
DI float hsum(float v) { const f32x2 h = halves(v); return h[0] + h[1]; }
DI float gmax(float v) { const f32x2 h = rows16(v); return hmax(fmaxf(h[0], h[1])); }
DI float gsum(float v) { const f32x2 h = rows16(v); return hsum(h[0] + h[1]); }

__host__ __device__ constexpr int wf_b(int nt, int F) { return nt < 2 ? 32 + 4 * F + nt : nt >= 26 ? 8 + 4 * F + nt : 48 + 16 * (nt >> 1) + 2 * F + (nt & 1); }
constexpr unsigned XRING = 65536;

#define SPOL 1
struct Prov { u32x4 srd; unsigned voff[2], soff, pstr, kstr; };
DI void prov_next(Prov& p) { p.soff += p.kstr; }
struct K1State { unsigned xs, ws, xd; };
struct K1Regs { unsigned xl, xr0, xr1, mw, mx; };

template <int Q, int POL = 1> DI void k1_dma(Prov& p, unsigned m0b) { set_m0(m0b + Q * 4096); unsigned so = p.soff + Q * p.pstr; PINS(so); NOP0(); dma16<0, POL>(p.voff[Q & 1], p.srd, so); }

#define ST_AUX 16
DI void st16(__amdgpu_buffer_rsrc_t rs, unsigned voff, unsigned soff, bf16x8 v) {
  __builtin_amdgcn_raw_buffer_store_b128(__builtin_bit_cast(u32x4, v), rs, voff, soff, ST_AUX);
  asm volatile("s_nop 1" ::"v"(v) : "memory");
}

struct Sync { char* ws; unsigned token, lane; u32x4 fv;
  __amdgpu_buffer_rsrc_t rs; bf16x8 sq[24]; unsigned sqe, sql, sv; bool stored; bf16x8 qa[2]; unsigned qao, tso, tm0;
  const char* pc; unsigned pfd[3];
  unsigned tab, need, gtab;
};
DI void flag_set(char* p, unsigned token) { __hip_atomic_store((GAS unsigned*)p, token, __ATOMIC_RELAXED, __HIP_MEMORY_SCOPE_AGENT); }
DI unsigned flag_get(const char* p) { return __hip_atomic_load((GAS unsigned*)p, __ATOMIC_RELAXED, __HIP_MEMORY_SCOPE_AGENT); }
constexpr unsigned SPIN_CAP = 1u << 20;
template <int SL = SLP> DI void flag_wait(const char* p, unsigned token) {
#pragma unroll 1
  for (unsigned i = 0; i < SPIN_CAP; ++i) { if (__all(flag_get(p) == token)) break; __builtin_amdgcn_s_sleep(SL); }
}
#define DRAIN() do { asm volatile("s_waitcnt vmcnt(0)" ::: "memory"); __builtin_amdgcn_s_waitcnt(0x0f70); } while (0)

struct Role { unsigned head, pi, roleb, pw, po, tb; int qlong, nt1, qb, nt2; bool produce, consume; };
DI Role mk_role(unsigned bid, unsigned wid) { Role r; r.head = 2 * (bid & 7) + ((bid >> 3) & 1); r.pi = bid >> 5; r.roleb = (bid >> 4) & 1;
  r.qlong = 15 - (int)r.pi; r.nt1 = 32 - 2 * (int)r.pi - TA;
  r.qb = r.roleb ? (int)r.pi : r.qlong; r.nt2 = r.roleb ? 2 * (int)r.pi + 2 : TA;
  r.produce = r.roleb && r.nt1 > 0; r.consume = !r.roleb && r.nt1 > 0;
  r.pw = (r.head * 8 + r.pi) * 4 + wid; r.po = (unsigned)WS_PO + r.pw * 16384; r.tb = r.qb * 4 + wid; return r; }
DI unsigned sel(bool c, unsigned a, unsigned b) { return b ^ ((a ^ b) & (0u - (unsigned)c)); }
DI void mk_tables(Sync& Y, const unsigned bid, const unsigned wid, const unsigned lane) {
  const Role r = mk_role(bid, wid); const unsigned head = r.head, j = lane;
  const unsigned n1 = 8 + (r.roleb ? 2 * r.nt1 : 0), n2 = n1 + 2 * r.nt2, np = r.consume ? 2 : 0;
  const unsigned jb = sel(j < n1, j - 8 + 2 * TA, j - n1), kv = (unsigned)WS_KB + head * (unsigned)MB + (jb & 1) * (unsigned)(WS_VB - WS_KB) + (jb >> 1) * 32768;
  unsigned a = sel(j >= 8, kv, (unsigned)WS_KVA + head * 262144 + j * 32768);
  a = sel(j >= n2, (unsigned)WS_WOF + head * 262144 + sel(j > n2, j - n2 - np, 0) * 32768, a) + wid * 8192;
  a = sel(j > n2 && j <= n2 + np, r.po + (j - n2 - 1) * 8192, a);
  Y.tab = sel(j < n2 + np + 8, a, 0x7ffffff0u); PIN(Y.tab);
  const unsigned cq = (256 * head + (lane & 1) * 255) / 192, rq_ = sel(lane & 2, r.qlong, r.qb) >> 1;
  Y.need = (unsigned)WS_F2 + F2S * sel(lane < 16, (head >> 1) + 8 * ((lane & 7) + 8 * ((head & 1) + 2 * (lane >> 3))), sel(lane < 20, (cq & 7) + 8 * (rq_ + 8 * (cq >> 3)), 16 * head + ((lane - 20) & 15))); PIN(Y.need);
  Y.gtab = wf_src(((bid * 4 + wid) & 63) * 4 + (lane & 3), bid >> 4); PIN(Y.gtab);
}
constexpr int g_go(int N2, int kh, int i) {
  if (N2 == 8) { const int m = i >> 3, n = i & 7; return n == 7 ? (kh ? ((m & 1) ? -1 : m >> 1) : (m < 4 ? m : 2 * m - 3)) : (n == 3 && !kh && m >= 4) ? 2 * m - 4 : -1; }
  return (i & 3) != 3 ? -1 : kh ? (((i & 7) == 7 && i < 32) ? i >> 3 : -1) : i >> 2; }
constexpr int g_tr(int N2, int i) {
  if (N2 == 8) return ((i >> 3) < 4 && (i & 7) >= 1 && (i & 7) <= 4) ? 4 * (i >> 3) + (i & 7) - 1 : -1;
  if ((i & 3) > 1 || i % 6 == 0) return -1;
  int c = 0; for (int j = 0; j < i; ++j) c += (j & 3) <= 1 && j % 6 != 0; return c < 12 ? c : -1; }
DI unsigned xnext(unsigned s) { return s == 65536u ? 0u : s + 32768u; }
template <bool SWAP, int NTL, bool FIRST, int QT>
DI void k1_ktile(f32x4 (&acc)[8 * NTL][2], bf16x8 (&xa)[4], s16x4 (&wf)[2][4 * NTL], K1Regs& R, unsigned (&wr)[2 * NTL], K1State& st, Prov& xp, Prov& wp,
                 const Prov& xn, const Prov& wn, const int t, const int NKT, const unsigned wid, Sync& Y) {
  constexpr int N2 = 2 * NTL; constexpr bool BOOT = FIRST && NTL == 3;
  sfor<2>([&](auto KH) __attribute__((always_inline)) { constexpr int kh = decltype(KH)::v;
    constexpr int VW = BOOT ? 14 : QT == 14 ? (kh ? 22 : 16) : QT == 15 ? (kh ? 63 : 30) : 16;
    if constexpr (kh == 0 && QT == 14) {
      if (wid == 0) { asm volatile("s_waitcnt vmcnt(16)" : "+v"(Y.fv));
        if (!__all(Y.fv[0] == Y.token && Y.fv[1] == Y.token && Y.fv[2] == Y.token && Y.fv[3] == Y.token))
#pragma unroll 1
          for (int i = 0; i < 4; ++i) flag_wait(Y.ws + WS_F1 + Y.lane * 16 + i * 4, Y.token); } }
    if constexpr (QT == 0) wait_bar<40, false>();
    else if constexpr (!(BOOT && kh == 0)) wait_bar<VW, false>();
    const unsigned mw = R.mw + kh * 16384, mx = R.mx;
    const unsigned bw2 = R.mw ^ 32768u;
    unsigned so = 0, qso = 0;
    sfor<8 * N2>([&](auto I) __attribute__((always_inline)) { constexpr int i = decltype(I)::v, m = i / N2, n = i % N2, pr = SWAP ? NTL * m + (n >> 1) : N2 * (m >> 1) + n, hf = SWAP ? n & 1 : m & 1;
      const bf16x8 wv = __builtin_shufflevector(wf[kh][2 * n], wf[kh][2 * n + 1], 0, 1, 2, 3, 4, 5, 6, 7);
      constexpr int go = BOOT ? -1 : g_go(N2, kh, i);
#define K1_OPS "v"(SWAP ? wv : xa[m & 3]), "v"(SWAP ? xa[m & 3] : wv)
      if constexpr (go >= 0) {
        const Prov& pp = go < 4 ? wp : xp;
        if constexpr (FIRST && kh == 0) asm volatile("v_mfma_f32_16x16x32_bf16 %0, %1, %2, 0\n\tbuffer_load_dwordx4 %3, %4, %5 offen lds" : "=a"(acc[pr][hf]) : K1_OPS, "v"(pp.voff[go & 1]), "s"(pp.srd), "s"(so) : "memory");
        else if constexpr (QT >= 14) asm volatile("v_mfma_f32_16x16x32_bf16 %0, %1, %2, %0\n\tbuffer_load_dwordx4 %3, %4, %5 offen sc1 lds" : "+a"(acc[pr][hf]) : K1_OPS, "v"(pp.voff[go & 1]), "s"(pp.srd), "s"(so) : "memory");
        else asm volatile("v_mfma_f32_16x16x32_bf16 %0, %1, %2, %0\n\tbuffer_load_dwordx4 %3, %4, %5 offen lds" : "+a"(acc[pr][hf]) : K1_OPS, "v"(pp.voff[go & 1]), "s"(pp.srd), "s"(so) : "memory");
      } else if constexpr (FIRST && kh == 0) asm volatile("v_mfma_f32_16x16x32_bf16 %0, %1, %2, 0" : "=a"(acc[pr][hf]) : K1_OPS);
      else asm volatile("v_mfma_f32_16x16x32_bf16 %0, %1, %2, %0" : "+a"(acc[pr][hf]) : K1_OPS);
#undef K1_OPS
      SBAR();
      if constexpr (n == 0) xa[(m + 2) & 3] = ldsr(m < 6 ? (kh ? R.xr1 : R.xr0) + (m + 2) * 2048 : (kh ? R.xr0 : R.xr1) + (m - 6) * 2048);
      constexpr int tr = g_tr(N2, i);
      if constexpr (tr >= 0) wf[kh ^ 1][tr] = __builtin_amdgcn_ds_read_tr16_b64_v4i16((LAS s16x4*)(uintptr_t)(wr[tr >> 1] + (kh ? 0 : 16384) + (tr & 1) * 2048));
      if constexpr (BOOT) {
        constexpr int p = (i & 3) == 3 ? i >> 2 : -1;
        if constexpr (kh == 0 && p >= 0) { if constexpr (p < 4) k1_dma<p, 0>(wp, bw2 + 16384); else k1_dma<p - 4, 0>(xp, mx); if constexpr (p == 3) prov_next(wp); if constexpr (p == 11) prov_next(xp); }
        if constexpr (kh == 1 && p >= 0 && p < 8) { if constexpr (p < 4) k1_dma<p, 0>(wp, mw - 16384); else k1_dma<p - 4, 0>(wp, mw); if constexpr (p == 3) prov_next(wp); }
      } else {
        constexpr int pm = g_go(N2, kh, i + (N2 == 8 ? 2 : 1)), ps = g_go(N2, kh, i + 1);
        if constexpr (pm >= 0 && i + 1 < 8 * N2) set_m0(pm < 4 ? mw + pm * 4096 : mx + (pm - 4) * 4096);
        if constexpr (ps >= 0 && i + 1 < 8 * N2) { so = ps == 0 ? wp.soff : ps == 4 ? xp.soff : so + (ps < 4 ? wp.pstr : xp.pstr); PINS(so); }
      }
      constexpr int wx = kh ? -1 : N2 == 8 ? ((m >= 4 && (n & 3) == 0) ? 2 * (m - 4) + (n >> 2) : -1) : i == 29 ? 0 : i == 32 ? 1 : i == 33 ? 2 : i == 37 ? 3 : i == 40 ? 4 : i == 41 ? 5 : -1;
      if constexpr (wx >= 0) { wr[wx] ^= 32768u; PIN(wr[wx]); }
      if constexpr (!BOOT && n == 0 && m == 2) { prov_next(wp); PINS(wp.soff); }
      if constexpr (!BOOT && kh == 0 && n == 0 && m == (N2 == 8 ? 5 : 4)) { prov_next(xp); PINS(xp.soff); }
      if constexpr (kh == 1 && m == 4 && n == 1) { st.xs = xnext(st.xs); PINS(st.xs); }
      if constexpr (kh == 1 && m == 4 && n == 3) { R.xr0 = R.xl + st.xs; PIN(R.xr0); }
      if constexpr (kh == 1 && m == 6 && n == 1) { R.xr1 = R.xr0 ^ 64u; PIN(R.xr1); }
      if constexpr (kh == 1 && m == 5 && n == 3) { R.mx = R.mx >= XRING + 65536u ? R.mx - 65536u : R.mx + 32768u; PINS(R.mx); }
      if constexpr (kh == 1 && m == 7 && n == 1) { R.mw ^= 32768u; PINS(R.mw); }
      if constexpr (kh == 1 && m == 7 && n == 2) { if (t == NKT - 3) { wp = wn; xp = xn; } }
      if constexpr (NTL == 4 && QT == -1 && kh == 1 && m == 7 && n == 4) { if (t == 6 && Y.stored) DRAIN(); }
      if constexpr (NTL == 4 && QT == -1 && kh == 1 && m == 5 && n == 5) { if (t == 7 && wid == 0) flag_set(Y.ws + WS_F1 + blockIdx.x * 4, Y.token); }
      if constexpr (n == N2 - (N2 == 8 ? 4 : 2) && (N2 == 6 || m >= 4)) wait_lgkm<N2 == 8 ? 1 : 3>();
      if constexpr (QT >= 14 && m >= 1 && m <= 6) { constexpr int le = 12 * (QT - 14) + 6 * kh + m - 1;
        if constexpr (n == 0) { qso = __builtin_amdgcn_readlane(Y.sql, le); PINS(qso); }
        if constexpr (n == (m < 4 ? 5 : 4)) st16(Y.rs, Y.lane * 16, qso, Y.sq[le]); }
      if constexpr (QT == 14 && kh == 1 && n == 3 && (m == 5 || m == 7))
        asm volatile("s_nop 4\n\tbuffer_load_dwordx4 %0, %1, %2, %3 offen offset:%4 sc1" : "=v"(Y.qa[m == 7]) : "v"(Y.lane * 16), "s"(xn.srd), "s"(Y.qao), "i"(m == 7 ? 1024 : 0) : "memory");
      SBAR();
    });
    if constexpr (BOOT && kh == 1) prov_next(wp);
  });
}

template <bool SWAP, int NTL>
DI void k1_gemm(f32x4 (&acc)[8 * NTL][2], K1State& st, Prov& xp, Prov& wp, const Prov& xn, const Prov& wn, const int NKT, const unsigned wid, const unsigned lane, Sync& Y) {
  const unsigned c = lane & 15, g = lane >> 4, q = (lane >> 2) & 3, wrow = wid >> 1, wc = wid & 1;
  if constexpr (NTL == 4) { if constexpr (SWAP) LABEL(f0); else LABEL(f1); }
  K1Regs R; unsigned wr[2 * NTL];
  R.xl = XRING + (128 * wrow + c) * 128 + 16 * (g ^ (c >> 1)); R.xr0 = R.xl + st.xs; R.xr1 = R.xr0 ^ 64u; R.mw = st.ws + wid * 1024; R.mx = XRING + st.xd + wid * 1024; PIN(R.xl); PIN(R.xr0); PIN(R.xr1);
#pragma unroll
  for (int n = 0; n < 2 * NTL; ++n) { wr[n] = st.ws + (8 * g + q) * 512 + 32 * ((2 * NTL * wc + n) ^ (q | ((g & 1) << 2))) + 8 * (lane & 3); PIN(wr[n]); }
  bf16x8 xa[4]; s16x4 wf[2][4 * NTL];
  if constexpr (NTL == 3) wait_bar<14, false>();
  xa[0] = ldsr(R.xr0); xa[1] = ldsr(R.xr0 + 2048);
#pragma unroll
  for (int n = 0; n < 4 * NTL; ++n) wf[0][n] = __builtin_amdgcn_ds_read_tr16_b64_v4i16((LAS s16x4*)(uintptr_t)(wr[n >> 1] + (n & 1) * 2048));
  SBAR();
  k1_ktile<SWAP, NTL, true, NTL == 4 ? 0 : -1>(acc, xa, wf, R, wr, st, xp, wp, xn, wn, 0, NKT, wid, Y);
  if constexpr (NTL == 4) {
    for (int t = 1; t < 13; ++t) k1_ktile<SWAP, NTL, false, -1>(acc, xa, wf, R, wr, st, xp, wp, xn, wn, t, NKT, wid, Y);
    if constexpr (SWAP) LABEL(k0); else LABEL(k1);
    asm volatile("" ::"v"(Y.pfd[0]));
    asm volatile("global_load_dword %0, %3, off\n\tglobal_load_dword %1, %4, off\n\tglobal_load_dword %2, %5, off" : "=&v"(Y.pfd[0]), "=&v"(Y.pfd[1]), "=&v"(Y.pfd[2])
                 : "v"(Y.pc + (SWAP ? LOFF(k0) : LOFF(k1)) + (wid * 64 + lane) * 64 % 9216), "v"(code_line(Y.pc, LOFF(a), LOFF(b), wid * 64 + lane)), "v"(code_line(Y.pc, LOFF(a), LOFF(b), 256 + wid * 64 + lane)) : "memory");
    if (wid == 0) asm volatile("global_load_dwordx4 %0, %1, off sc1" : "=v"(Y.fv) : "v"(Y.ws + WS_F1 + Y.lane * 16) : "memory");
    sfor<3>([&](auto T) __attribute__((always_inline)) { constexpr int t = 13 + decltype(T)::v; k1_ktile<SWAP, NTL, false, t>(acc, xa, wf, R, wr, st, xp, wp, xn, wn, t, 16, wid, Y); });
    asm volatile("" ::"v"(Y.pfd[0]), "v"(Y.pfd[1]), "v"(Y.pfd[2]));
  } else
    for (int t = 1; t < NKT; ++t) k1_ktile<SWAP, NTL, false, -1>(acc, xa, wf, R, wr, st, xp, wp, xn, wn, t, NKT, wid, Y);
  if constexpr (NTL == 3) asm volatile("global_load_dword %0, %1, off" : "=v"(Y.pfd[0]) : "v"(Y.pc + ((blockIdx.x >> 7) ? LOFF(f1) : LOFF(f0)) + (wid * 64 + lane) * 64 % 8192) : "memory");
  st.ws = R.mw - wid * 1024; st.xd = R.mx - XRING - wid * 1024;
  asm volatile("s_nop 7" ::: "memory"); SBAR();
}

DI Prov mk_xprov(const char* X, unsigned ld, unsigned bytes, unsigned rb, unsigned wid, unsigned lane, bool vperm) {
  Prov p; p.srd = mksrd(X, bytes);
  p.voff[0] = p.voff[1] = (lane >> 3) * ld + 16 * ((lane & 7) ^ (4 * (wid & 1) + (lane >> 4)));
  p.soff = (256 * rb + 8 * (vperm ? ((wid & 1) << 1) | (wid >> 1) : wid)) * ld; p.pstr = 32 * ld; p.kstr = 128; return p;
}
template <class F> DI Prov mk_wprov(const char* W, unsigned ld, unsigned bytes, unsigned wid, unsigned lane, F colf) {
  Prov p; p.srd = mksrd(W, bytes);
#pragma unroll
  for (int o = 0; o < 2; ++o) { const unsigned ch = (lane & 31) ^ ((2 * (wid & 1) + (lane >> 5) + 4 * o) << 1), col = colf(ch);
    p.voff[o] = col < 0x10000u ? (lane >> 5) * ld + 2 * col : 0x7ffffff0u; }
  p.soff = 2 * wid * ld; p.pstr = 8 * ld; p.kstr = 32 * ld; return p;
}
DI void proj_phase(f32x4 (&acc4)[32][2], const char* x, const char* wqkv, const char* ckv, const char* wq, const char* wuk, const char* wuv, const char* gate_w,
                   const char* wo_a, const char* wo_b, char* ws, const unsigned kva_off, Sync& Y, const unsigned lane, const unsigned wid) {
  const unsigned wrow = wid >> 1, wc = wid & 1, lane16 = lane * 16;
  const unsigned bid = blockIdx.x, xcd = bid & 7, loc = bid >> 3, rb = loc & 7, sel = loc >> 3;
  const unsigned cb = xcd + 8 * sel;
  const unsigned head2 = 2 * xcd + (sel & 1), isV = sel >> 1;
  const Prov xkv = mk_xprov(ckv, 2048, 2048 * 2048, rb, wid, lane, false);
  const Prov wkv = mk_wprov(isV ? wuv : wuk, 8192, 1024 * 8192, wid, lane, [&](unsigned ch) { return head2 * 256 + 8 * ch; });
  Prov xp = cb < 31 ? mk_xprov(x, 896, 2048 * 896, rb, wid, lane, cb >= 28) : xkv;
  Prov wp = cb < 22 ? mk_wprov(wq, 8192, 448 * 8192, wid, lane, [&](unsigned ch) { return ch < 24 ? cb * 192 + 8 * ch : 0x10000u; })
          : cb < 31 ? mk_wprov(wqkv, 2688, 448 * 2688, wid, lane, [&](unsigned ch) { const unsigned vc = (cb < 28 ? cb - 22 : cb - 28) * 24 + ch, slot = vc >> 2;
                         return (ch < 24 && slot < (cb < 28 ? 32u : 16u)) ? (cb < 28 ? 0u : 896u) + 28 * slot + 8 * (vc & 3) : 0x10000u; })
          : wkv;
  K1State st = cb < 31 ? K1State{32768u, 32768u, 0u} : K1State{65536u, 0u, 32768u};
  const unsigned x0 = XRING + st.xs + wid * 1024, w0 = st.ws + wid * 1024;
  sfor<8>([&](auto I) __attribute__((always_inline)) { k1_dma<decltype(I)::v, 0>(xp, x0); }); prov_next(xp);
  sfor<4>([&](auto I) __attribute__((always_inline)) { k1_dma<decltype(I)::v, 0>(wp, w0); }); prov_next(wp);
  sfor<4>([&](auto I) __attribute__((always_inline)) { k1_dma<decltype(I)::v, 0>(wp, w0 + 16384); }); prov_next(wp);
  sfor<4>([&](auto I) __attribute__((always_inline)) { k1_dma<decltype(I)::v, 0>(wp, w0 ^ 32768u); }); prov_next(wp);
  sfor<8>([&](auto I) __attribute__((always_inline)) { k1_dma<decltype(I)::v, 0>(xp, XRING + xnext(st.xs) + wid * 1024); }); prov_next(xp);
  touch(code_line(Y.pc, 0, LOFF(end), (rb * 4 + wid) * 64 + lane));
  { const unsigned e = wf_src(((bid * 4 + wid) & 63) * 4 + 2 * (lane >> 5), bid >> 4), row = ((e & 0x40000000u) && (lane & 31) >= 28) ? 0 : lane & 31;
    touch(((e & 0x40000000u) ? wo_a : wo_b) + (e & 0x3fffffffu) + row * 896); }
  mk_tables(Y, bid, wid, lane);
  Y.stored = cb >= 22; Y.sv = (lane >> 5) * 1024 + ((lane >> 4) & 1) * 512 + (lane & 15) * 16;
  const unsigned tb0 = rb * 8 + wrow * 4;
  {
    const bool vt = cb >= 28; const unsigned hi = vt ? lane / 6 : lane / 3, lo = lane - hi * (vt ? 6 : 3), tb = tb0 + (vt ? hi : hi >> 1), odd = (vt ? lo : hi) & 1;
    const unsigned c32 = (cb < 22 ? cb : cb < 28 ? cb - 22 : cb - 28) * 6 + wc * 3 + (vt ? lo >> 1 : lo);
    unsigned d = cb < 22 ? (unsigned)WS_QB + (((c32 >> 3) * 64 + tb) * 16 + odd * 8 + (c32 & 7)) * 1024
               : vt ? (unsigned)WS_KVA + (c32 * 64 + tb) * 4096 + 2048
               : c32 < 16 ? (unsigned)WS_QA + (c32 * 64 + tb) * 2048 : (unsigned)WS_KVA + ((c32 - 16) * 64 + tb) * 4096;
    d += cb < 22 ? 0 : odd * 256;
    if (cb == 31 || c32 >= (cb < 22 ? 128u : cb < 28 ? 32u : 16u)) d = 0x7ffffff0u;
    Y.sqe = cb >= 22 ? d : 0x7ffffff0u; Y.sql = cb < 22 ? d : 0x7ffffff0u;
  }
  if (cb < 31) {
    f32x4 acc[24][2];
    if (cb < 28) {
      k1_gemm<true, 3>(acc, st, xp, wp, xkv, wkv, 7, wid, lane, Y);
      const bool pad = cb >= 22 && lane >= 48;
#pragma unroll
      for (int e = 0; e < 24; ++e) { const float sc = cb < 22 ? QSCALE_B : (cb - 22) * 6 + wc * 3 + e % 3 < 16 ? QSCALE_A : 1.f;
        unsigned so = __builtin_amdgcn_readlane(Y.sqe, e); PINS(so);
        PINA(acc[e][0]); PINA(acc[e][1]); u32x4 w = pack44(acc[e][0], acc[e][1], sc); if (pad) { w[2] = 0u; w[3] = 0u; }
        Y.sq[e] = __builtin_bit_cast(bf16x8, w); PIN(Y.sq[e]); st16(Y.rs, Y.sv, so, Y.sq[e]); SBAR(); }
    } else {
      k1_gemm<false, 3>(acc, st, xp, wp, xkv, wkv, 7, wid, lane, Y);
      const unsigned fill = (lane & 15) == 12 ? 0x3f803f80u : 0u;
#pragma unroll
      for (int e = 0; e < 24; ++e) { unsigned so = __builtin_amdgcn_readlane(Y.sqe, e); PINS(so); PINA(acc[e][0]); PINA(acc[e][1]); u32x4 w = pack44(acc[e][0], acc[e][1], 1.f); if ((e & 1) && (lane & 15) >= 12) w = (u32x4){fill, fill, fill, fill};
        Y.sq[e] = __builtin_bit_cast(bf16x8, w); PIN(Y.sq[e]); st16(Y.rs, Y.sv, so, Y.sq[e]); SBAR(); }
    }
  } else {
    sfor<4>([&](auto I) __attribute__((always_inline)) { k1_dma<decltype(I)::v, 0>(wp, (w0 ^ 32768u) + 16384); }); prov_next(wp);
    u32x4 gw_[7];
#pragma unroll
    for (int i = 0; i < 7; ++i) gw_[i] = *(const u32x4*)(gate_w + (8 * i + (lane & 7)) * 16);
#pragma unroll 2
    for (int p = 0; p < 8; ++p) { const unsigned rw = rb * 256 + wid * 64 + p * 8 + (lane >> 3); float s = 0.f;
#pragma unroll
      for (int i = 0; i < 7; ++i) { const u32x4 a = *(const u32x4*)(x + rw * 896 + (8 * i + (lane & 7)) * 16);
#pragma unroll
        for (int e = 0; e < 4; ++e) s = dot2(a[e], gw_[i][e], s); }
      s += __shfl_xor(s, 1); s += __shfl_xor(s, 2); s += __shfl_xor(s, 4);
      flag_set(ws + WS_G + rw * 4, __float_as_uint(1.f / (1.f + __expf(-s)))); }
#pragma unroll
    for (int e = 0; e < 24; ++e) { Y.sq[e] = bf16x8{}; PIN(Y.sq[e]); }
    Y.pfd[0] = 0u;
    wait_bar<0, false>();
  }
  asm volatile("" : "+v"(Y.token)); Y.token = rfl(Y.token) + 1u;
  Prov xa_, wa_; xa_.srd = mksrd(ws, (unsigned)WS_END); xa_.voff[0] = xa_.voff[1] = lane16; xa_.soff = kva_off + wid * 1024; xa_.pstr = 4096; xa_.kstr = 32768;
  wa_ = xa_; wa_.soff += 3 * 32768; wa_.kstr = 16384;
  if (!isV) k1_gemm<true, 4>(acc4, st, xp, wp, xa_, wa_, 16, wid, lane, Y);
  else k1_gemm<false, 4>(acc4, st, xp, wp, xa_, wa_, 16, wid, lane, Y);
  Y.tso = xp.soff; Y.tm0 = XRING + st.xd + wid * 1024;
}

struct Desc { unsigned so, m0; };
struct Stream {
  u32x4 srd; unsigned tab, lane16;
  unsigned dseq, dslot, rslot, so;
  Desc y;
};
DI unsigned nslot(unsigned s) { return s >= 131072u ? s - 131072u : s + 32768u; }
DI void st_prep(Stream& S, Desc& D) { D.so = __builtin_amdgcn_readlane(S.tab, S.dseq); PINS(D.so); D.m0 = S.dslot; S.dslot = nslot(S.dslot); S.dseq += 1; PINS(S.dslot); }
template <int Q> DI void st_pre(Stream& S, const Desc& D) {
  if constexpr (Q == 0) { set_m0(D.m0); S.so = D.so; } else if constexpr (Q == 4) { set_m0(D.m0 + 4096u); S.so = D.so + 4096u; PINS(S.so); }
}
template <int Q> DI void st_go(Stream& S) { dma16<(Q & 3) * 1024, SPOL>(S.lane16, S.srd, S.so); }
DI void st_item(Stream& S) {
  sfor<8>([&](auto Q) __attribute__((always_inline)) { st_pre<decltype(Q)::v>(S, S.y); if constexpr (!(decltype(Q)::v & 3)) NOP0(); st_go<decltype(Q)::v>(S); });
  st_prep(S, S.y); asm volatile("s_nop 4" ::: "memory");
}
DI float max16(const f32x16& s) {
  float a = fmaxf(fmaxf(s[0], s[1]), s[2]), b = fmaxf(fmaxf(s[3], s[4]), s[5]);
  a = fmaxf(fmaxf(a, s[6]), s[7]); b = fmaxf(fmaxf(b, s[8]), s[9]); a = fmaxf(fmaxf(a, s[10]), s[11]); b = fmaxf(fmaxf(b, s[12]), s[13]);
  a = fmaxf(fmaxf(a, s[14]), s[15]); a = fmaxf(a, b);
  return hmax(a);
}
DI float max3f(float a, float b, float c) { float r; asm("v_max3_f32 %0, %1, %2, %3" : "=v"(r) : "v"(a), "v"(b), "v"(c)); return r; }
DI f32x16 splat16(float v) { f32x16 r; for (int i = 0; i < 16; ++i) r[i] = v; return r; }
DI void open_item(Stream& S, unsigned& rb_, unsigned off) { rb_ = S.rslot + off; PIN(rb_); S.rslot = nslot(S.rslot); PINS(S.rslot); }
DI void unopen_item(Stream& S) { S.rslot = S.rslot == 0u ? 131072u : S.rslot - 32768u; }
DI void set_c(f32x4 (&c)[4][2], float v0, float v1, int dq) {
#pragma unroll
  for (int kt = 0; kt < 4; ++kt)
#pragma unroll
    for (int qt = 0; qt < 2; ++qt) {
#pragma unroll
      for (int r = 0; r < 4; ++r) c[kt][qt][r] = dq + 16 * (qt - kt) >= r ? (qt ? v1 : v0) : -__builtin_inff();
      PIN(c[kt][qt]); }
  asm volatile("s_nop 1" ::: "memory");
}

constexpr int b_adds(bool first, int j) {
  int n = 0; for (int i = first ? 91 : 83; i < j; ++i) n += (i & 1) ? ((i & 7) != 7 && i != 97 && i != 123 && !(first && i >= 112)) : (i >= 92 && ((i & 3) == 0 || i >= 114)) ? 2 : 0;
  return n < 32 ? n : 32; }
static_assert(b_adds(false, 128) == 32 && b_adds(true, 128) == 32, "every probability is summed");
template <bool FIRST>
DI void b_tile(f32x4 (&o)[16][2], const bf16x8 (&q)[16], bf16x8 (&fr)[8], f32x4 (&c)[4][2], float (&mref)[2], float (&rs)[2], Stream& S, unsigned& rb_) {
  f32x4 s[4][2]; u32x4 p[2][2]; float pe[32]; Desc X; unsigned rn_, dl;
  constexpr int E0 = FIRST ? 26 : 18;
  sfor<128>([&](auto J) __attribute__((always_inline)) { constexpr int j = decltype(J)::v, f = j >> 1, qt = j & 1, kt = j >> 4;
    if constexpr (j == 48 || j == 112) { wait_bar<16, false>(); rb_ = rn_; }
    if constexpr (j < 64) {
      if constexpr ((f & 7) == 0) asm volatile("v_mfma_f32_16x16x32_bf16 %0, %1, %2, %3" : "=&v"(s[kt & 3][qt]) : "v"(fr[0]), "v"(q[8 * qt]), "v"(c[kt & 3][qt]));
      else asm volatile("v_mfma_f32_16x16x32_bf16 %0, %1, %2, %0" : "+v"(s[kt & 3][qt]) : "v"(fr[f & 7]), "v"(q[8 * qt + (f & 7)]));
    } else if constexpr (FIRST && j < 96) asm volatile("v_mfma_f32_16x16x32_bf16 %0, %1, %2, 0" : "=&a"(o[f & 15][qt]) : "v"(fr[f & 7]), "v"(p[0][qt]));
    else asm volatile("v_mfma_f32_16x16x32_bf16 %0, %1, %2, %0" : "+a"(o[f & 15][qt]) : "v"(fr[f & 7]), "v"(p[j >= 96][qt]));
    SBAR();
    if constexpr (qt) fr[f & 7] = ldsr(rb_ + ((f + 8) & 31) * 1024);
    if constexpr (j < 16 && (j & 3) == 2) st_go<(j >> 2)>(S);
    if constexpr (j >= 19 && j <= 43 && (j & 7) == 3) st_go<4 + ((j - 19) >> 3)>(S);
    if constexpr (j >= 82 && j <= 110 && (j & 3) == 2) st_go<((j - 82) >> 2)>(S);
    SBAR();
    if constexpr (FIRST) {
      if constexpr (j == 18 || j == 19) { float m = fmaxf(max3f(s[0][qt][0], s[0][qt][1], s[0][qt][2]), s[0][qt][3]); PIN(m); mref[qt] = m; }
      if constexpr (j == 20 || j == 22) { constexpr int u = (j - 20) >> 1; float m = gmax(mref[u]); m = m == -__builtin_inff() ? 0.f : m + MBIAS; PIN(m); mref[u] = m; }
      if constexpr (j >= 112) { constexpr int e = j - 112; const float v = -mref[(e >> 1) & 1]; c[e >> 2][(e >> 1) & 1][2 * (e & 1)] = v; c[e >> 2][(e >> 1) & 1][2 * (e & 1) + 1] = v; if constexpr (e & 1) PIN(c[e >> 2][(e >> 1) & 1]); }
    }
    if constexpr (!qt && j >= E0 && j < E0 + 64) { constexpr int e = (j - E0) >> 1;
      float v = s[e >> 3][(e >> 2) & 1][e & 3]; if constexpr (FIRST) v -= mref[(e >> 2) & 1]; v = __builtin_amdgcn_exp2f(v); PIN(v); pe[e] = v; }
    if constexpr (qt && j > E0 && j < E0 + 64 && (((j - E0) >> 1) & 1)) { constexpr int e = (j - E0) >> 1;
      unsigned w = cvtpk(pe[e - 1], pe[e]); PIN(w); p[e >> 4][(e >> 2) & 1][2 * ((e >> 3) & 1) + ((e >> 1) & 1)] = w; }
    constexpr int a0 = b_adds(FIRST, j), an = b_adds(FIRST, j + 1) - a0;
    sfor<2>([&](auto K) __attribute__((always_inline)) { constexpr int a = a0 + decltype(K)::v;
      if constexpr (decltype(K)::v < an) { constexpr int e = (a & ~7) | ((a & 1) << 2) | ((a >> 1) & 3); rs[a & 1] += pe[e]; PIN(rs[a & 1]); } });
    if constexpr ((j & 7) == 7) wait_lgkm<4>();
    if constexpr (j == 1) st_pre<0>(S, S.y);
    if constexpr (j == 17) st_pre<4>(S, S.y);
    if constexpr (j == 4 || j == 84) { Desc& D = j == 4 ? X : S.y; D.so = __builtin_amdgcn_readlane(S.tab, S.dseq); PINS(D.so); S.dseq += 1; PINS(S.dseq); }
    if constexpr (j == 8 || j == 88) { dl = S.dslot >= 131072u ? (unsigned)-131072 : 32768u; PINS(dl); }
    if constexpr (j == 9 || j == 59 + 64) { Desc& D = j == 9 ? X : S.y; D.m0 = S.dslot; S.dslot += dl; PINS(S.dslot); }
    if constexpr (j == 5 || j == 51) { rn_ = S.rslot + S.lane16; PIN(rn_); }
    if constexpr (j == 12) { dl = S.rslot >= 131072u ? (unsigned)-131072 : 32768u; PINS(dl); }
    if constexpr (j == 13) { S.rslot += dl; PINS(S.rslot); }
    if constexpr (j == 59) { S.rslot = nslot(S.rslot); PINS(S.rslot); }
    if constexpr (j == 81) st_pre<0>(S, X);
    if constexpr (j == 97) st_pre<4>(S, X);
    SBAR();
  });
}

DI void hand_step(const char* fh, const char* ml, const unsigned token, const int left, bool& late) {
  if (left == 1) {
    unsigned fw; asm volatile("v_accvgpr_read_b32 %0, a254" : "=v"(fw));
    late = rfl(fw) != token;
    asm volatile("global_load_dwordx4 a[248:251], %0, off sc1" ::"v"(ml) : "memory", "a248", "a249", "a250", "a251");
  }
  if (left == 2) asm volatile("global_load_dword a254, %0, off sc1" ::"v"(fh) : "memory", "a254");
}

DI void b_segment(f32x4 (&o)[16][2], const bf16x8 (&q)[16], float (&mref)[2], float (&lsum)[2], Stream& S, const int qb, const int t0, const int t1,
                  const unsigned lane, const unsigned wid, const char* fh, const char* ml, const unsigned token, const bool consume, f32x4& mlb, bool& late, const unsigned pj, const char* opc) {
  const int qrow = qb * 128 + (int)wid * 32 + (int)(lane & 15) - 4 * (int)(lane >> 4);
  unsigned rb_; open_item(S, rb_, S.lane16);
  bf16x8 fr[8];
#pragma unroll
  for (int j = 0; j < 8; ++j) fr[j] = ldsr(rb_ + j * 1024);
  f32x4 c[4][2]; float rs[2] = {0.f, 0.f};
  set_c(c, 0.f, 0.f, qrow - t0 * 64);
  SBAR();
  b_tile<true>(o, q, fr, c, mref, rs, S, rb_);
  const int tc = consume ? t1 : -8;
  for (int t = t0 + 1; t < t1; ++t) {
    if (t >= t1 - 2) {
      hand_step(fh, ml, token, tc - t, late);
      if (tc - t == 1 && late) { S.y.so = 0x7ffffff0u; S.tab = lane == pj + 1 ? 0x7ffffff0u : S.tab; }
      if (t == t1 - 2 || t1 - t0 == 2) touch(opc);
      if (t >= 2 * qb) set_c(c, -mref[0], -mref[1], qrow - t * 64);
    }
    b_tile<false>(o, q, fr, c, mref, rs, S, rb_);
  }
  SBAR();
  asm volatile("v_accvgpr_read_b32 %0, a248\n\tv_accvgpr_read_b32 %1, a249\n\tv_accvgpr_read_b32 %2, a250\n\tv_accvgpr_read_b32 %3, a251\n\ts_nop 4" : "=v"(mlb[0]), "=v"(mlb[1]), "=v"(mlb[2]), "=v"(mlb[3]));
  SBAR();
  unopen_item(S);
  lsum[0] = gsum(rs[0]); lsum[1] = gsum(rs[1]);
}

DI void a_finish(bf16x8 (&ca)[2], f32x16 o, const float g, const unsigned lane) {
  const bool up = lane >= 32;
  const float l = halves(o[12])[1];
  const float sc = g * __builtin_amdgcn_rcpf(l) + l * 0.f;
#pragma unroll
  for (int e = 12; e < 16; ++e) o[e] = up ? 0.f : o[e];
  const u32x4 a = __builtin_bit_cast(u32x4, pack8(o, 0, sc)), b = __builtin_bit_cast(u32x4, pack8(o, 1, sc)); u32x4 x, y;
#pragma unroll
  for (int e = 0; e < 4; ++e) { auto r = __builtin_amdgcn_permlane16_swap(a[e], b[e], false, false); x[e] = r[0]; y[e] = r[1]; }
  ca[0] = __builtin_bit_cast(bf16x8, x); ca[1] = __builtin_bit_cast(bf16x8, y);
}
DI bf16x8 ld16(__amdgpu_buffer_rsrc_t rs, unsigned voff, unsigned soff) { return __builtin_bit_cast(bf16x8, __builtin_amdgcn_raw_buffer_load_b128(rs, voff, soff, 16)); }
struct KvOut { __amdgpu_buffer_rsrc_t rs; unsigned dst, s2, wof, gtab, gva, gvb; const char* woa; const char* wob; };
#define AC 52
constexpr int a_ops(int st) {
  return (st < 32) + (st < 4 ? 2 : 0) + (st < 16 ? 2 : 0) + (st >= 26 && st < 30) + (((st >= 16 && st < 20) || (st >= 24 && st < 28) || (st >= 38 && st < 42)) ? 2 : 0) + (st == AC - 4) + (st == AC - 3) + (st == AC - 1) + (st >= AC && st < AC + 8 ? 4 : 0) + (st >= AC + 8 && st < AC + 12 ? 2 : 0); }
constexpr int a_sum(int a, int b) { int n = 0; for (int i = a; i < b; ++i) n += a_ops(i); return n; }
DI void a_phase(bf16x8 (&ca)[2], bf16x8 (&q)[16], const unsigned qoff, const f32x4 (&acc)[32][2], const Sync& Y, const KvOut& KO, Stream& S, float& g, const unsigned gp, const unsigned lane,
                char* f2_mine, const unsigned f2_need, const unsigned token, const char* pc, const unsigned line, const unsigned wid) {
  const bf16x8 q0 = Y.qa[0], q1 = Y.qa[1];
  unsigned rb_, rn_; open_item(S, rb_, S.lane16); rn_ = rb_;
  bf16x8 kf[2][2], va[2], vb[2];
  kf[0][0] = ldsr(rb_); kf[0][1] = ldsr(rb_ + 1024); kf[1][0] = ldsr(rb_ + 4096); kf[1][1] = ldsr(rb_ + 5120);
  va[0] = ldsr(rb_ + 2048); vb[0] = ldsr(rb_ + 3072); va[1] = ldsr(rb_ + 4096 + 2048); vb[1] = vb[0];
  f32x16 s[2], o = {}, negm;
  { f32x16 z; asm volatile("v_mfma_f32_32x32x16_bf16 %0, %1, %2, 0" : "=&v"(z) : "v"(kf[0][0]), "v"(q0)); mfma_v(z, kf[0][1], q1); MFMA_DRAIN_V(z);
    negm = splat16(-(max16(z) + MBIAS)); PIN(negm); }
  u32x4 p0, p1 = {0u, 0u, 0u, 0u}, ow, gsrd; float pt[4] = {0.f, 0.f, 0.f, 0.f}; unsigned fw, gt[4][8], gso, gv0, gv1, tso_;
  PIN(o); asm volatile("s_nop 1" ::: "memory");
  mfma_vc(s[0], kf[0][0], q0, negm); mfma_v(s[0], kf[0][1], q1);
  kf[0][0] = ldsr(rb_ + 8192); kf[0][1] = ldsr(rb_ + 9216);
  MFMA_DRAIN_V(s[0]);
  SBAR();
#define AADDR(blk, off) ((blk) < 8 ? rb_ + (blk) * 4096 + (off) : rn_ + ((blk) - 8) * 4096 + (off))
#define AEXP(e) do { pe[e] = __builtin_amdgcn_exp2f(s[c][e]); PIN(pe[e]); } while (0)
#define ACVT(P, i, a, b) do { unsigned w_ = cvtpk(a, b); PIN(w_); P[i] = w_; } while (0)
#define TPRE(p) do { set_m0(Y.tm0 + (p) * 4096); tso_ = Y.tso + (p) * 4096; PINS(tso_); SBAR(); } while (0)
#define TGO(p) dma16<0, 1>(S.lane16, S.srd, tso_)
#define BGO(p) do { if constexpr (((p) & 3) == 0) { st_pre<(p) & 4>(S, S.y); NOP0(); } st_go<(p) & 7>(S); if constexpr (((p) & 7) == 7) { st_prep(S, S.y); asm volatile("s_nop 3" ::: "memory"); } } while (0)
#define GDESC(f) do { const unsigned e_ = __builtin_amdgcn_readlane(KO.gtab, f); const bool a_ = e_ & 0x40000000u; gsrd = a_ ? mksrd(KO.woa, 448 * 896) : mksrd(KO.wob, 4096 * 896); gso = e_ & 0x3fffffffu; \
    gv0 = a_ ? KO.gva : KO.gvb; gv1 = a_ ? (lane >= 48 ? 0x7ffffff0u : KO.gva + 8 * 896) : KO.gvb + 16 * 896; PINS(gsrd); PINS(gso); PIN(gv0); PIN(gv1); } while (0)
#define GLD(j) asm volatile("buffer_load_ushort %0, %1, %2, %3 offen offset:%4" : "=v"(gt[st >> 2][j]) : "v"((j) < 4 ? gv0 : gv1), "s"(gsrd), "s"(gso), "i"(((j) & 3) * 896) : "memory")
#define QLD(f) asm volatile("buffer_load_dwordx4 %0, %1, %2, %3 offen sc1" : "=v"(q[f]) : "v"(S.lane16), "s"(S.srd), "s"(qoff + (f) * 1024) : "memory")
  GDESC(0); asm volatile("s_nop 4" ::: "memory");
  sfor<64>([&](auto ST) __attribute__((always_inline)) { constexpr int st = decltype(ST)::v, it = st >> 3, kb = st & 7, c = kb & 1, n = c ^ 1;
    constexpr bool has_st = st < 32; constexpr int se = st & 31;
    constexpr int rq = (st >= 16 && st < 20) ? st - 16 : (st >= 24 && st < 28) ? st - 24 : (st >= 38 && st < 42) ? st - 38 : -1;
    if constexpr (kb == 0) { rb_ = rn_; }
    if constexpr (st == 36) DRAIN();
    if constexpr (kb == 5 && it < 7) { wait_bar<it == 0 ? a_sum(0, 5) : it == 1 ? a_sum(4, 13) : it == 6 ? a_sum(42, 53) : 63, false>(); open_item(S, rn_, S.lane16); }
    if constexpr (st == 37) { if (wid == 0) flag_set(f2_mine, token); }
    if constexpr (st == 26) asm volatile("s_waitcnt vmcnt(%0)" ::"i"(a_sum(16, 26)) : "memory");
        if constexpr (st == AC) { asm volatile("s_waitcnt vmcnt(2)" : "+v"(fw)); if (!__all(fw == token)) flag_wait(Y.ws + f2_need, token); }
    float pe[16];
    if constexpr (st < 62) wait_lgkm<4>();
    if constexpr (st < 63) mfma_vc(s[n], kf[n][0], q0, negm);
    if constexpr (st < 61) kf[n][0] = ldsr(AADDR(kb + 3, 0));
    if constexpr (st < 16) GLD(2 * (st & 3));
    if constexpr (st >= AC && st < AC + 8) QLD(2 * (st - AC));
    SBAR();
    float y[4];
    if constexpr (st < 4) TPRE(2 * st);
    if constexpr (has_st) { y[0] = ard(acc[se][0][0]); y[1] = ard(acc[se][0][1]); y[2] = ard(acc[se][0][2]); y[3] = ard(acc[se][0][3]); SBAR(); }
    AEXP(0); AEXP(1); ACVT(p1, 2, pt[0], pt[1]); AEXP(2); AEXP(3); ACVT(p1, 3, pt[2], pt[3]);
    if constexpr (has_st) { ACVT(ow, 0, y[0], y[1]); ACVT(ow, 1, y[2], y[3]); }
    if constexpr (rq >= 0 && (rq & 1) == 0) st_pre<(rq & 2) * 2>(S, S.y);
    SBAR();
    if constexpr (st < 63) mfma_v(s[n], kf[n][1], q1);
    if constexpr (st < 61) kf[n][1] = ldsr(AADDR(kb + 3, 1024));
    if constexpr (rq >= 0) st_go<2 * (rq & 3)>(S);
    if constexpr (st < 4) TGO(2 * st);
    if constexpr (st == AC - 3) touch(code_line(pc, LOFF(b), LOFF(c), line));
    if constexpr (st == AC - 4) asm volatile("buffer_load_dword %0, %1, %2, 0 offen sc1" : "=v"(fw) : "v"(f2_need), "s"(S.srd) : "memory");
    if constexpr (st == AC - 1) asm volatile("buffer_load_dword %0, %1, %2, %3 offen sc1" : "=v"(g) : "v"((lane & 31) * 4), "s"(S.srd), "s"(gp) : "memory");
    if constexpr (st >= AC && st < AC + 12) BGO(2 * (st - AC));
    SBAR();
    if constexpr (has_st) { y[0] = ard(acc[se][1][0]); y[1] = ard(acc[se][1][1]); y[2] = ard(acc[se][1][2]); y[3] = ard(acc[se][1][3]); SBAR(); }
    AEXP(4); AEXP(5); ACVT(p0, 0, pe[0], pe[1]); AEXP(6); AEXP(7); ACVT(p0, 1, pe[2], pe[3]);
    if constexpr (has_st) { ACVT(ow, 2, y[0], y[1]); ACVT(ow, 3, y[2], y[3]); }
    SBAR();
    mfma_v(o, vb[n], __builtin_bit_cast(bf16x8, p1));
    if constexpr (st < 63) vb[n] = ldsr(AADDR(kb + 1, 3072));
    if constexpr (has_st) __builtin_amdgcn_raw_buffer_store_b128(ow, KO.rs, S.lane16 + (se & 3) * 1024, KO.dst + (se >> 3) * 16384 + ((se >> 2) & 1) * KO.s2, ST_AUX);
    if constexpr (st >= AC && st < AC + 8) QLD(2 * (st - AC) + 1);
    SBAR();
    if constexpr (st < 4) TPRE(2 * st + 1);
    AEXP(8); ACVT(p0, 2, pe[4], pe[5]); AEXP(9); ACVT(p0, 3, pe[6], pe[7]); SBAR();
    if constexpr (has_st) asm volatile("" ::"v"(ow));
    AEXP(10); AEXP(11);
    if constexpr (st >= 26 && st < 30) {
#pragma unroll
      for (int e = 0; e < 4; ++e) { asm volatile("" : "+v"(gt[st - 26][2 * e]), "+v"(gt[st - 26][2 * e + 1])); ow[e] = gt[st - 26][2 * e] | (gt[st - 26][2 * e + 1] << 16); }
      PIN(ow); SBAR(); }
    SBAR();
    mfma_v(o, va[c], __builtin_bit_cast(bf16x8, p0));
    if constexpr (st < 62) va[c] = ldsr(AADDR(kb + 2, 2048));
    if constexpr (rq >= 0) st_go<2 * (rq & 3) + 1>(S);
    if constexpr (st < 4) TGO(2 * st + 1);
    if constexpr (st < 16) GLD(2 * (st & 3) + 1);
    if constexpr (st >= 26 && st < 30) st16(KO.rs, S.lane16, KO.wof + (st - 26) * 1024, __builtin_bit_cast(bf16x8, ow));
    if constexpr (st >= AC && st < AC + 12) BGO(2 * (st - AC) + 1);
    SBAR();
    if constexpr (st < 12 && (st & 3) == 3) GDESC((st >> 2) + 1);
    AEXP(12); AEXP(13); ACVT(p1, 0, pe[8], pe[9]); AEXP(14); AEXP(15); ACVT(p1, 1, pe[10], pe[11]);
    pt[0] = pe[12]; pt[1] = pe[13]; pt[2] = pe[14]; pt[3] = pe[15];
    if constexpr (rq == 3) st_prep(S, S.y);
    SBAR();
  });
#undef AADDR
#undef AEXP
#undef ACVT
#undef TGO
#undef TPRE
#undef BGO
#undef GDESC
#undef GLD
#undef QLD
  asm volatile("" ::"v"(negm));
  p1[2] = cvtpk(pt[0], pt[1]); p1[3] = cvtpk(pt[2], pt[3]);
  mfma_vn(o, vb[1], __builtin_bit_cast(bf16x8, p1));
  MFMA_DRAIN_V(o);
  wait_bar<8, false>();
  asm volatile("" : "+v"(g), "+v"(q[0]), "+v"(q[1]), "+v"(q[2]), "+v"(q[3]), "+v"(q[4]), "+v"(q[5]), "+v"(q[6]), "+v"(q[7]));
  asm volatile("" : "+v"(q[8]), "+v"(q[9]), "+v"(q[10]), "+v"(q[11]), "+v"(q[12]), "+v"(q[13]), "+v"(q[14]), "+v"(q[15]));
  a_finish(ca, o, g, lane);
}

DI void skip_slot(unsigned& s, unsigned res) { s = s == res ? nslot(s) : s; PINS(s); }
#define OP_MFMA(A, Z, fr_, b_) do { if constexpr (Z) asm volatile("v_mfma_f32_16x16x32_bf16 %0, %1, %2, 0" : "=&a"(A) : "v"(fr_), "v"(b_)); else asm volatile("v_mfma_f32_16x16x32_bf16 %0, %1, %2, %0" : "+a"(A) : "v"(fr_), "v"(b_)); } while (0)
template <bool MERGE>
DI void op_head(f32x4 (&o)[16][2], const bf16x8 (&ca)[2], u32x4 (&cb)[16], f32x4 (&c0)[2][2], f32x4 (&z)[2][2], bf16x8 (&fr)[9], const float (&fa)[2], const float (&fb)[2], Stream& S, unsigned& ra, unsigned& rb_, unsigned& res, const unsigned wid) {
  u32x4 pb[16];
  float x[8];
#define CONV(c, st) do { constexpr int c_ = (c), t_ = c_ & 1; \
    if constexpr ((st) == 0) { _Pragma("unroll") for (int e = 0; e < 8; ++e) x[e] = ard(o[(c_ & ~1) + (e >> 2)][t_][e & 3]); } \
    if constexpr ((st) == 1) { _Pragma("unroll") for (int e = 0; e < 8; ++e) { x[e] = MERGE ? fmaf(x[e], fa[t_], ((e & 1) ? bfhi(pb[c_][e >> 1]) : bflo(pb[c_][e >> 1])) * fb[t_]) : x[e] * fa[t_]; } \
      asm volatile("" : "+v"(x[0]), "+v"(x[1]), "+v"(x[2]), "+v"(x[3]), "+v"(x[4]), "+v"(x[5]), "+v"(x[6]), "+v"(x[7])); } \
    if constexpr ((st) == 2) { cb[c_] = (u32x4){cvtpk(x[0], x[1]), cvtpk(x[2], x[3]), cvtpk(x[4], x[5]), cvtpk(x[6], x[7])}; PIN(cb[c_]); } } while (0)
  res = S.rslot; open_item(S, ra, S.lane16);
  fr[0] = ldsr(ra); fr[1] = ldsr(ra + 1024); fr[2] = ldsr(ra + 26 * 1024); fr[3] = ldsr(ra + 27 * 1024);
  if constexpr (MERGE) {
    wait_bar<8, false>();
    sfor<8>([&](auto C) __attribute__((always_inline)) { constexpr int c = decltype(C)::v;
      if constexpr ((c & 3) == 0) { if constexpr (c) asm volatile("s_waitcnt vmcnt(8)" ::: "memory");
        open_item(S, rb_, wid * 8192 + S.lane16);
#pragma unroll
        for (int i = 0; i < 8; ++i) pb[2 * c + i] = __builtin_bit_cast(u32x4, ldsr(rb_ + i * 1024)); }
      st_pre<(2 * c) & 7>(S, S.y); CONV(c, 0); SBAR(); st_go<(2 * c) & 7>(S); SBAR(); CONV(c, 1); SBAR(); st_go<((2 * c) & 7) + 1>(S); SBAR(); CONV(c, 2);
      if constexpr (c == 7) skip_slot(S.dslot, res + wid * 8192);
      if constexpr ((c & 3) == 3) st_prep(S, S.y);
      SBAR(); });
  } else { CONV(0, 0); SBAR(); CONV(0, 1); SBAR(); CONV(0, 2); SBAR(); CONV(1, 0); SBAR(); CONV(1, 1); SBAR(); CONV(1, 2); SBAR(); }
  wait_bar<8, false>(); open_item(S, rb_, S.lane16);
#pragma unroll
  for (int i = 0; i < 5; ++i) fr[4 + i] = ldsr(rb_ + i * 1024);
  asm volatile("s_nop 1" ::: "memory"); SBAR();
  sfor<72>([&](auto J) __attribute__((always_inline)) { constexpr int j = decltype(J)::v, u = j >> 1, qt = j & 1, v = u + 9;
    if constexpr (j == 58) { wait_bar<8, false>(); skip_slot(S.rslot, res); open_item(S, rb_, S.lane16); }
    f32x4& A = (u & 2) ? z[u & 1][qt] : c0[u & 1][qt];
    if constexpr (u < 4) OP_MFMA(A, true, fr[u], ca[qt]); else OP_MFMA(A, false, fr[u % 9], cb[2 * ((u - 4) >> 2) + qt]);
    SBAR();
    if constexpr (qt) fr[u % 9] = ldsr(v < 36 ? rb_ + (v - 4) * 1024 : v < 38 ? ra + (v - 34) * 1024 : rb_ + (v - 38) * 1024);
    if constexpr (j < 32 && (j & 3) == 2) st_go<(j >> 2)>(S);
    SBAR();
    if constexpr (j == 0 || j == 16) st_pre<j / 4>(S, S.y);
    if constexpr (j == 36) { skip_slot(S.dslot, res + wid * 8192); st_prep(S, S.y); }
    if constexpr (!MERGE && j < 56) CONV(2 + (j >> 2), j & 3);
    if constexpr (MERGE && j < 64 && !(j & 1)) CONV(8 + (j >> 3), (j >> 1) & 3);
    SBAR(); });
#undef CONV
}
DI float op_body(const bf16x8 (&ca)[2], const u32x4 (&cb)[16], f32x4 (&c0)[2][2], f32x4 (&z)[2][2], bf16x8 (&fr)[9], Stream& S, unsigned ra, unsigned rb_, const unsigned res_,
                 const __amdgpu_buffer_rsrc_t rs, unsigned og, const unsigned wid, unsigned& f3v, const char* f3p) {
  f32x4 c1[2][2]; u32x4 ow; float x0, x1; unsigned rn_ = rb_; const unsigned res = rfl(res_);
  S.rslot = rfl(S.rslot); S.dslot = rfl(S.dslot); S.dseq = rfl(S.dseq); S.y.so = rfl(S.y.so); S.y.m0 = rfl(S.y.m0);
#pragma unroll 1
  for (int k = 0; k < 6; ++k) {
    sfor<72>([&](auto J) __attribute__((always_inline)) { constexpr int j = decltype(J)::v, u = j >> 1, qt = j & 1, v = u + 9, w = u % 18, jj = j % 36;
      f32x4 (&cur)[2][2] = u < 18 ? c1 : c0; f32x4 (&prv)[2][2] = u < 18 ? c0 : c1;
      if constexpr (j == 58) { wait_bar<11, false>(); rb_ = rn_; }
      if constexpr (w < 2) OP_MFMA(cur[w][qt], true, fr[u % 9], ca[qt]); else OP_MFMA(cur[w & 1][qt], false, fr[u % 9], cb[2 * ((w - 2) >> 1) + qt]);
      SBAR();
      if constexpr (qt) fr[u % 9] = ldsr(v < 18 ? rb_ + (v - 2) * 1024 : v < 20 ? ra + (v - 14) * 1024 : v < 36 ? rb_ + (v - 4) * 1024 : v < 38 ? ra + (v - 30) * 1024 : rb_ + (v - 38) * 1024);
      if constexpr (j % 6 == 2 && j < 50) st_go<j / 6>(S);
      if constexpr (jj == 16 || jj == 28) st16(rs, S.lane16 + (u >= 18 ? 2048 : 0) + (jj == 28 ? 1024 : 0), og, __builtin_bit_cast(bf16x8, ow));
      SBAR();
      constexpr int tab[36] = {-1, -1, -1, -1, 0, 0, 1, 1, -1, -1, 2, 2, 3, 3, -1, -1, -1, -1, 4, 4, -1, -1, 5, 5, 6, 6, 7, 7, -1, -1, -1, -1, -1, -1, -1, -1};
      constexpr int tb2[36] = {-1, -1, -1, -1, 0, 0, 1, 1, -1, -1, 2, 2, 3, 3, -1, -1, -1, -1, 4, 4, 5, 5, 6, 6, 7, 7, -1, -1, -1, -1, -1, -1, -1, -1, -1, -1};
      constexpr int e = u < 18 ? tab[jj] : tb2[jj];
      if constexpr (e >= 0 && !qt) { x0 = ard(prv[(e >> 1) & 1][e >> 2][2 * (e & 1)]); x1 = ard(prv[(e >> 1) & 1][e >> 2][2 * (e & 1) + 1]); }
      if constexpr (e >= 0 && qt) { unsigned x = cvtpk(x0, x1); PIN(x); ow[e & 3] = x; }
      if constexpr (j % 6 == 5) wait_lgkm<6>();
      if constexpr (j == 1 || j == 25) st_pre<j == 1 ? 0 : 4>(S, S.y);
      if constexpr (j == 52) { skip_slot(S.dslot, res + wid * 8192); }
      if constexpr (j == 66) { S.y.so = __builtin_amdgcn_readlane(S.tab, S.dseq); PINS(S.y.so); S.dseq += 1; }
      if constexpr (j == 68) { S.y.m0 = S.dslot; S.dslot = nslot(S.dslot); PINS(S.dslot); }
      if constexpr (j == 32) skip_slot(S.rslot, res);
      if constexpr (j == 34) { rn_ = S.rslot + S.lane16; PIN(rn_); }
      if constexpr (j == 36) { S.rslot = nslot(S.rslot); PINS(S.rslot); }
      if constexpr (j == 70) { ra += 4096; PIN(ra); og += 4096; }
      SBAR(); });
  }
  asm volatile("global_load_dword %0, %9, off sc1\n\ts_nop 7" : "=v"(f3v), "+a"(c0[0][0]), "+a"(c0[0][1]), "+a"(c0[1][0]), "+a"(c0[1][1]), "+a"(z[0][0]), "+a"(z[0][1]), "+a"(z[1][0]), "+a"(z[1][1]) : "v"(f3p) : "memory");
  SBAR();
  st16(rs, S.lane16 + 2048, og, __builtin_bit_cast(bf16x8, pack44(z[0][0], z[1][0], 1.f))); st16(rs, S.lane16 + 3072, og, __builtin_bit_cast(bf16x8, pack44(z[0][1], z[1][1], 1.f)));
  st16(rs, S.lane16, og, __builtin_bit_cast(bf16x8, pack44(c0[0][0], c0[1][0], 1.f))); st16(rs, S.lane16 + 1024, og, __builtin_bit_cast(bf16x8, pack44(c0[0][1], c0[1][1], 1.f)));
  return z[0][0][0] + z[0][1][0];
}
#undef OP_MFMA

DI void safe_redo(const __amdgpu_buffer_rsrc_t rs, const unsigned og, const unsigned head, const unsigned tb, const float g, const unsigned lane) {
  const unsigned lane16 = lane * 16;
  bf16x8 ca[2], cb[16];
  {
    const unsigned qa = (unsigned)WS_QA + (head * 64 + tb) * 2048; const bf16x8 q0 = ld16(rs, lane16, qa), q1 = ld16(rs, lane16, qa + 1024);
    float m = -1e30f; f32x16 o = splat16(opaque0());
    for (int kb = 0; kb < 64; ++kb) {
      const unsigned p = (unsigned)WS_KVA + (head * 64 + kb) * 4096;
      f32x16 t = {}; t = MFMA(ld16(rs, lane16, p), q0, t); t = MFMA(ld16(rs, lane16, p + 1024), q1, t);
      const float mn = fmaxf(m, max16(t)), al = __builtin_amdgcn_exp2f(m - mn); m = mn;
#pragma unroll
      for (int e = 0; e < 16; ++e) t[e] = __builtin_amdgcn_exp2f(t[e] - mn);
      o *= al; o = MFMA(ld16(rs, lane16, p + 2048), pack8(t, 0), o); o = MFMA(ld16(rs, lane16, p + 3072), pack8(t, 1), o);
    }
    a_finish(ca, o, g, lane);
  }
  const f32x2 g2 = rows16(g);
#pragma unroll
  for (int qt = 0; qt < 2; ++qt) {
    const unsigned qg = (unsigned)WS_QB + (head * 64 + tb) * 16384 + qt * 8192;
    const int qrow = (int)tb * 32 + 16 * qt + (int)(lane & 15) - 4 * (int)(lane >> 4);
    float m = -1e30f, ls = 0.f; f32x4 o[16]; bf16x8 q[8];
#pragma unroll
    for (int d = 0; d < 16; ++d) { const float z = opaque0(); o[d] = (f32x4){z, z, z, z}; }
#pragma unroll
    for (int F = 0; F < 8; ++F) q[F] = ld16(rs, lane16, qg + F * 1024);
#pragma unroll 1
    for (int kb = 0; kb <= (int)tb; ++kb) {
      const unsigned kp = (unsigned)WS_KB + (head * 64 + kb) * 16384, vp = (unsigned)WS_VB + (head * 64 + kb) * 16384;
      f32x4 s[2] = {};
#pragma unroll
      for (int j = 0; j < 16; ++j) s[j >> 3] = MFMA16(ld16(rs, lane16, kp + j * 1024), q[j & 7], s[j >> 3]);
      float mx = m;
#pragma unroll
      for (int e = 0; e < 8; ++e) { s[e >> 2][e & 3] = qrow - kb * 32 - 16 * (e >> 2) >= (e & 3) ? s[e >> 2][e & 3] : -__builtin_inff(); mx = fmaxf(mx, s[e >> 2][e & 3]); }
      const float mn = gmax(mx), al = __builtin_amdgcn_exp2f(m - mn); m = mn; ls *= al;
#pragma unroll
      for (int e = 0; e < 8; ++e) { s[e >> 2][e & 3] = __builtin_amdgcn_exp2f(s[e >> 2][e & 3] - mn); ls += s[e >> 2][e & 3]; }
      const bf16x8 p = __builtin_bit_cast(bf16x8, pack44(s[0], s[1], 1.f));
#pragma unroll
      for (int d = 0; d < 16; ++d) { o[d] *= al; o[d] = MFMA16(ld16(rs, lane16, vp + d * 1024), p, o[d]); }
    }
    const float inv = (1.f - g2[qt]) / gsum(ls);
#pragma unroll
    for (int F = 0; F < 8; ++F) cb[2 * F + qt] = __builtin_bit_cast(bf16x8, pack44(o[2 * F], o[2 * F + 1], inv));
  }
  const unsigned wf = (unsigned)WS_WOF + head * 262144;
  DRAIN();
#pragma unroll 1
  for (int p = 0; p < 14; ++p) {
    const unsigned f0 = p == 0 ? 32 : p == 13 ? 34 : 48 + 16 * p, fs = (p == 0 || p == 13) ? 4 : 2; f32x4 a[2][2] = {};
#pragma unroll
    for (int h = 0; h < 2; ++h) { const bf16x8 w = ld16(rs, lane16, wf + (2 * p + h) * 1024); a[h][0] = MFMA16(w, ca[0], a[h][0]); a[h][1] = MFMA16(w, ca[1], a[h][1]);
#pragma unroll
      for (int F = 0; F < 8; ++F) { const bf16x8 wb = ld16(rs, lane16, wf + (f0 + fs * F + h) * 1024); a[h][0] = MFMA16(wb, cb[2 * F], a[h][0]); a[h][1] = MFMA16(wb, cb[2 * F + 1], a[h][1]); } }
    st16(rs, lane16, og + (2 * p) * 1024, __builtin_bit_cast(bf16x8, pack44(a[0][0], a[1][0], 1.f))); st16(rs, lane16, og + (2 * p + 1) * 1024, __builtin_bit_cast(bf16x8, pack44(a[0][1], a[1][1], 1.f)));
  }
}

template <int H> DI void head_fetch(u32x4& a, u32x4& b, unsigned oa, unsigned ob, unsigned mask, u32x4 srd, unsigned so) {
  asm volatile("s_bitcmp1_b32 %4, %7\n\ts_cbranch_scc0 1f\n\tbuffer_load_dwordx4 %0, %2, %5, %6 offen sc1\n\tbuffer_load_dwordx4 %1, %3, %5, %6 offen sc1\n1:"
               : "+v"(a), "+v"(b) : "v"(oa), "v"(ob), "s"(mask), "s"(srd), "s"(so), "i"(H) : "memory", "scc");
}
__global__ __launch_bounds__(256, 1) void fused_fwd(const char* __restrict__ x, const char* __restrict__ wqkv, const char* __restrict__ ckv, const char* __restrict__ wq,
                                                    const char* __restrict__ wuk, const char* __restrict__ wuv, const char* __restrict__ gate_w,
                                                    const char* __restrict__ wo_a, const char* __restrict__ wo_b, char* ws, char* __restrict__ out) {
  extern __shared__ __attribute__((aligned(16))) char lds[];
  const unsigned wid = rfl(threadIdx.x >> 6);
  Sync Y; asm volatile("s_getpc_b64 %0\n.Lgm_base:" : "=s"(Y.pc)); Y.ws = ws; Y.lane = threadIdx.x & 63;
  touch(Y.pc + threadIdx.x * 64);
  asm volatile("global_load_dword %0, %1, off sc1" : "=v"(Y.token) : "v"(ws + WS_W0) : "memory");
  const __amdgpu_buffer_rsrc_t rs = __builtin_amdgcn_make_buffer_rsrc(ws, 0, (int)WS_END, 0x20000);
  Y.rs = rs;
  { const unsigned b_ = blockIdx.x, p_ = b_ >> 5; Y.qao = (unsigned)WS_QA + ((2 * (b_ & 7) + ((b_ >> 3) & 1)) * 64 + (((b_ >> 4) & 1) ? p_ : 15 - p_) * 4 + wid) * 2048; }
  f32x4 acc4[32][2];
  proj_phase(acc4, x, wqkv, ckv, wq, wuk, wuv, gate_w, wo_a, wo_b, ws, (unsigned)WS_KVA + (2 * (blockIdx.x & 7) + ((blockIdx.x >> 3) & 1)) * 262144, Y, Y.lane, wid);
  LABEL(a);
  unsigned lane, bid = blockIdx.x;
  asm volatile("v_mbcnt_lo_u32_b32 %0, -1, 0\n\tv_mbcnt_hi_u32_b32 %0, -1, %0" : "=v"(lane)); PINS(bid);
  const unsigned lane16 = lane * 16, tid = wid * 64 + lane;
  const unsigned token = Y.token;
  if (bid == 0 && wid == 0) flag_set(ws + WS_W0, token);
  const Role R_ = mk_role(bid, wid);
  const unsigned head = R_.head, pw = R_.pw, po = R_.po, tb = R_.tb, pj = 9 + 2 * TA; const int qlong = R_.qlong, nt1 = R_.nt1, qb = R_.qb, nt2 = R_.nt2; const bool produce = R_.produce, consume = R_.consume;
  Stream S; S.tab = Y.tab; S.srd = mksrd(ws, (unsigned)WS_END); S.lane16 = lane16; S.dseq = 5; S.dslot = 65536 + wid * 8192; S.rslot = 65536;
  st_prep(S, S.y);
  wait_bar<28, false>();
  asm volatile("" : "+v"(Y.qa[0]), "+v"(Y.qa[1]));
  float g;
  bf16x8 ca[2], q[16];
  { const unsigned xcd = bid & 7, loc = bid >> 3, rb = loc & 7, sel = loc >> 3, head2 = 2 * xcd + (sel & 1), isV = sel >> 1, tb0 = rb * 8 + (wid >> 1) * 4;
    KvOut KO; KO.rs = rs; KO.dst = (isV ? (unsigned)WS_VB : (unsigned)WS_KB) + ((head2 * 64 + tb0) * 16 + (wid & 1) * (isV ? 8 : 4)) * 1024; KO.s2 = isV ? 4096 : 8192;
    KO.wof = (unsigned)WS_WOF + (bid * 4 + wid) * 4096; KO.gtab = Y.gtab; KO.gva = (16 * ((lane >> 4) & 1) + 4 * (lane >> 5)) * 896 + 2 * (lane & 15); KO.gvb = 4 * (lane >> 4) * 896 + 2 * (lane & 15); KO.woa = wo_a; KO.wob = wo_b;
    a_phase(ca, q, (unsigned)WS_QB + (head * 64 + (produce ? qlong * 4 + wid : tb)) * 16384, acc4, Y, KO, S, g, (unsigned)WS_G + tb * 128, lane,
            ws + WS_F2 + bid * F2S, Y.need, token, Y.pc, tid, wid); }
  LABEL(b);
  char* fh = ws + WS_FH + pw * 64; char* ml = ws + WS_ML + pw * 1024 + lane16;
  f32x4 o[16][2], mlb; float mref[2], lsum[2]; bool late = false;
  for (int pass = produce ? 0 : 1; pass < 2; ++pass) {
    b_segment(o, q, mref, lsum, S, pass ? qb : qlong, pass ? 0 : TA, pass ? nt2 : TA + nt1, lane, wid, fh, ml, token, consume, mlb, late, pj, code_line(Y.pc, LOFF(c), LOFF(end), tid));
    if (pass == 0) {
      const float inv[2] = {lsum[0] > 0.f ? __builtin_amdgcn_rcpf(lsum[0]) : 0.f, lsum[1] > 0.f ? __builtin_amdgcn_rcpf(lsum[1]) : 0.f};
#pragma unroll
      for (int c = 0; c < 16; ++c) { PINA(o[c & ~1][c & 1]); PINA(o[c | 1][c & 1]); st16(rs, lane16, po + c * 1024, __builtin_bit_cast(bf16x8, pack44(o[c & ~1][c & 1], o[c | 1][c & 1], inv[c & 1]))); SBAR(); }
      st16(rs, lane16, (unsigned)WS_ML + pw * 1024, __builtin_bit_cast(bf16x8, (f32x4){mref[0], lsum[0], mref[1], lsum[1]}));
      DRAIN();
      flag_set(fh, token);
#pragma unroll
      for (int j = 0; j < 16; ++j) q[j] = ld16(rs, lane16, (unsigned)WS_QB + ((head * 64 + tb) * 16 + j) * 1024);
      DRAIN();
    }
  }
  LABEL(c);
  const unsigned og = (unsigned)WS_OP + (head * 64 + tb) * 28 * 1024;
  u32x4 cb[16]; f32x4 c0[2][2], z[2][2]; bf16x8 fr[9]; unsigned ra, rb_, res;
  float chk, fa[2], fb[2]; unsigned f3v; const char* f3p = ws + WS_F3 + (tb * 16 + (lane & 15)) * F3S; const f32x2 g2 = rows16(g);
  if (consume) {
    if (late) {
      flag_wait(fh, token);
      mlb = __builtin_bit_cast(f32x4, ld16(rs, lane16, (unsigned)WS_ML + pw * 1024));
      Desc D; D.so = po; D.m0 = nslot(S.rslot) + wid * 8192;
      sfor<16>([&](auto Q) __attribute__((always_inline)) { constexpr int qq = decltype(Q)::v;
        if constexpr (qq == 8) { D.so = po + 8192; D.m0 = nslot(nslot(S.rslot)) + wid * 8192; }
        st_pre<(qq & 7)>(S, D); if constexpr (!(qq & 3)) NOP0(); st_go<(qq & 7)>(S); });
      DRAIN();
    }
    chk = 0.f;
#pragma unroll
    for (int t = 0; t < 2; ++t) { const float mb = mlb[2 * t], lb = mlb[2 * t + 1];
      const float m = lb > 0.f ? fmaxf(mref[t], mb) : mref[t], ea = __builtin_amdgcn_exp2f(mref[t] - m), wb = lb > 0.f ? lb * __builtin_amdgcn_exp2f(mb - m) : lb * 0.f;
      const float den = lsum[t] * ea + wb, inv = (1.f - g2[t]) * __builtin_amdgcn_rcpf(den); fa[t] = ea * inv; fb[t] = wb * inv; chk += den; }
    op_head<true>(o, ca, cb, c0, z, fr, fa, fb, S, ra, rb_, res, wid);
  } else { chk = lsum[0] + lsum[1]; fa[0] = (1.f - g2[0]) * __builtin_amdgcn_rcpf(lsum[0]); fa[1] = (1.f - g2[1]) * __builtin_amdgcn_rcpf(lsum[1]); op_head<false>(o, ca, cb, c0, z, fr, fa, fa, S, ra, rb_, res, wid); }
  chk += op_body(ca, cb, c0, z, fr, S, ra, rb_, res, rs, og, wid, f3v, f3p);
  DRAIN(); asm volatile("" : "+v"(f3v));
  if (__builtin_expect(__any(!(chk * 0.f == 0.f)), 0)) { safe_redo(rs, og, head, tb, g, lane); DRAIN(); }
  flag_set(ws + WS_F3 + (tb * 16 + head) * F3S, token);
  {
    u32x4 v[2][16]; unsigned elo = 0x00003f80u, ehi = 0x3f800000u; PIN(elo); PIN(ehi);
    const unsigned c0 = head * 112 + lane, c1 = lane < 48 ? c0 + 64 : c0;
    unsigned done = 0u, seen = (unsigned)__ballot(f3v == token) | (1u << head);
#pragma unroll
    for (int h = 0; h < 16; ++h) { asm volatile("" : "=v"(v[0][h])); asm volatile("" : "=v"(v[1][h])); }
#pragma unroll 1
    for (unsigned spins = 0; spins < SPIN_CAP; ++spins) {
      const unsigned nw = seen & 0xffffu & ~done; done |= nw;
      if (nw) sfor<16>([&](auto H) __attribute__((always_inline)) { constexpr int h = decltype(H)::v;
        head_fetch<h>(v[0][h], v[1][h], c0 * 16, c1 * 16, nw, S.srd, (unsigned)WS_OP + (h * 64 + tb) * 28 * 1024); });
      if (done == 0xffffu) break;
      __builtin_amdgcn_s_sleep(SLP3);
      seen = (unsigned)__ballot(flag_get(f3p) == token);
    }
    asm volatile("s_waitcnt vmcnt(0)" : "+v"(v[0][0]), "+v"(v[0][1]), "+v"(v[0][2]), "+v"(v[0][3]), "+v"(v[0][4]), "+v"(v[0][5]), "+v"(v[0][6]), "+v"(v[0][7]),
                 "+v"(v[0][8]), "+v"(v[0][9]), "+v"(v[0][10]), "+v"(v[0][11]), "+v"(v[0][12]), "+v"(v[0][13]), "+v"(v[0][14]), "+v"(v[0][15]) :: "memory");
    asm volatile("" : "+v"(v[1][0]), "+v"(v[1][1]), "+v"(v[1][2]), "+v"(v[1][3]), "+v"(v[1][4]), "+v"(v[1][5]), "+v"(v[1][6]), "+v"(v[1][7]),
                 "+v"(v[1][8]), "+v"(v[1][9]), "+v"(v[1][10]), "+v"(v[1][11]), "+v"(v[1][12]), "+v"(v[1][13]), "+v"(v[1][14]), "+v"(v[1][15]) :: "memory");
#pragma unroll
    for (int k = 0; k < 2; ++k) { float a[8] = {};
#pragma unroll
      for (int h = 0; h < 16; ++h)
#pragma unroll
        for (int e = 0; e < 4; ++e) { a[2 * e] = dot2(v[k][h][e], elo, a[2 * e]); a[2 * e + 1] = dot2(v[k][h][e], ehi, a[2 * e + 1]); }
      const unsigned c = k ? c1 : c0;
      char* op = out + (size_t)(tb * 32 + ((c >> 6) & 1) * 16 + (c & 15)) * 896 + ((c >> 7) * 32 + 4 * ((c >> 4) & 3)) * 2;
      if (k == 0 || lane < 48) { *(u32x2*)op = (u32x2){cvtpk(a[0], a[1]), cvtpk(a[2], a[3])}; *(u32x2*)(op + 32) = (u32x2){cvtpk(a[4], a[5]), cvtpk(a[6], a[7])}; } }
  }
  LABEL(end);
}

extern "C" void kernel_launch(void* const* d_in, const int* in_sizes, int n_in, void* d_out, int out_size, void* d_ws, size_t ws_size, hipStream_t stream) {
  (void)in_sizes; (void)n_in; (void)out_size; (void)ws_size;
  (void)hipFuncSetAttribute((const void*)fused_fwd, hipFuncAttributeMaxDynamicSharedMemorySize, 163840);
  fused_fwd<<<256, 256, 163840, stream>>>((const char*)d_in[0], (const char*)d_in[1], (const char*)d_in[4], (const char*)d_in[5], (const char*)d_in[6], (const char*)d_in[7], (const char*)d_in[9],
                                          (const char*)d_in[2], (const char*)d_in[8], (char*)d_ws, (char*)d_out);
}
```
